```python
import torch

def _softmax(x, axis=-1):
    return torch.softmax(x, dim=axis)

def _gelu(x):
    return torch.nn.functional.gelu(x, approximate='tanh')

def _silu(x):
    return torch.nn.functional.silu(x)

def _sigmoid(x):
    return torch.sigmoid(x)

def _layernorm(x, axis=-1, eps=1e-05):
    m = x.mean(dim=axis, keepdim=True)
    v = x.var(dim=axis, keepdim=True, unbiased=False)
    return (x - m) / torch.sqrt(v + eps)

def _scatter_add(tgt, idx, src, axis=0):
    return tgt.clone().index_add_(axis, idx, src)

def _segment_sum(src, seg, n):
    out = torch.zeros((n,) + src.shape[1:], dtype=src.dtype)
    return out.index_add_(0, seg, src)

def _pad(x, pw):
    flat = tuple((v for pair in reversed(pw) for v in pair))
    return torch.nn.functional.pad(x, flat)
kernel_name = 'parallel_sum__sliding_local_dilated__mixer_gmlp__prompt'

def setup_inputs():

    _w = lambda a: (lambda t: t.to(torch.bfloat16) if t.is_floating_point() else t)(torch.as_tensor(a))

    def _A_setup():
        B, S, D = (1, 2048, 512)
        return {'x': _w((lambda *s: torch.randn(*s, dtype=torch.bfloat16) * 0.05)(B, S, D)), 'wqkv': _w((lambda *s: torch.randn(*s, dtype=torch.bfloat16) * 0.1)(D, 3 * D)), 'wo': _w((lambda *s: torch.randn(*s, dtype=torch.bfloat16) * 0.05)(D, D))}
    _A_in = _A_setup()

    def _B_setup():
        B, S, D, F = (1, 2048, 512, 1792)
        Fh = F // 2
        return {'x': _w((lambda *s: torch.randn(*s, dtype=torch.bfloat16) * 0.05)(B, S, D)), 'w_in': _w((lambda *s: torch.randn(*s, dtype=torch.bfloat16) * 0.4)(D, 2 * Fh)), 'w_sp': _w(0.01 * (lambda *s: torch.randn(*s, dtype=torch.bfloat16) * 0.4)(S, S)), 'b_sp': _w(1.0 + 0.01 * (lambda *s: torch.randn(*s, dtype=torch.bfloat16) * 0.05)(S)), 'w_out': _w((lambda *s: torch.randn(*s, dtype=torch.bfloat16) * 0.05)(Fh, D))}
    _B_in = _B_setup()
    return {'A_x': _A_in['x'], 'A_wqkv': _A_in['wqkv'], 'A_wo': _A_in['wo'], 'B_x': _B_in['x'], 'B_w_in': _B_in['w_in'], 'B_w_sp': _B_in['w_sp'], 'B_b_sp': _B_in['b_sp'], 'B_w_out': _B_in['w_out']}

def reference(A_x, A_wqkv, A_wo, B_x, B_w_in, B_w_sp, B_b_sp, B_w_out):

    def _A_ref(x, wqkv, wo):
        B, S, D = x.shape
        H = 32
        Dh = D // H
        W, dil = (8, 2)
        qkv = (lambda x, *d: x.permute(*d))((x @ wqkv).reshape(B, S, 3, H, Dh), 2, 0, 3, 1, 4)
        q, k, v = (qkv[0], qkv[1], qkv[2])
        scale = Dh ** (-0.5)
        pos = torch.arange(S)
        off = torch.arange(W) * dil
        idx = pos[:, None] - off[None, :]
        mask = idx >= 0
        idx_c = torch.where(mask, idx, 0).reshape(-1)
        kg = k[:, :, idx_c, :].reshape(B, H, S, W, Dh)
        vg = v[:, :, idx_c, :].reshape(B, H, S, W, Dh)
        logits = torch.einsum('bhsd,bhswd->bhsw', q, kg) * scale
        logits = torch.where(mask[None, None, :, :], logits, -1000000000.0)
        attn = _softmax(logits, axis=-1)
        o = torch.einsum('bhsw,bhswd->bhsd', attn, vg)
        return (lambda x, *d: x.permute(*d))(o, 0, 2, 1, 3).reshape(B, S, D) @ wo

    def _B_ref(x, w_in, w_sp, b_sp, w_out):
        B, S, D = x.shape
        F = w_in.shape[-1]
        Fh = F // 2
        z = _gelu(torch.einsum('bsd,df->bsf', x, w_in))
        z1 = z[..., :Fh].clone()
        z2 = _layernorm(z[..., Fh:], axis=-1)
        g = torch.einsum('ts,bsf->btf', w_sp, z2) + b_sp[None, :, None]
        return torch.einsum('bsf,fd->bsd', z1 * g, w_out)
    x = A_x
    xn = _layernorm(x, axis=-1)
    return x + _A_ref(xn, A_wqkv, A_wo) + _B_ref(xn, B_w_in, B_w_sp, B_b_sp, B_w_out)
```

```cpp
#include <hip/hip_runtime.h>
#include <hip/hip_bf16.h>
typedef unsigned short u16;
typedef short bf16x8 __attribute__((ext_vector_type(8)));
typedef float f32x4 __attribute__((ext_vector_type(4)));
__device__ __forceinline__ float bf2f(u16 b){ return __uint_as_float(((unsigned)b)<<16); }
__device__ __forceinline__ u16 f2bf(float f){ unsigned u=__float_as_uint(f); u += 0x7FFFu + ((u>>16)&1u); return (u16)(u>>16); }
constexpr int S=2048, D=512, F=1792, FH=896;

__global__ void ln_rows(const u16* in, int ld, int c0, int n, u16* out){
  int row = blockIdx.x; int t = threadIdx.x;
  float v[16]; float s=0;
  int cnt = n/64;
  #pragma unroll
  for(int i=0;i<16;i++){ if(i<cnt){ v[i]=bf2f(in[(size_t)row*ld+c0+i*64+t]); s+=v[i]; } else v[i]=0; }
  for(int m=32;m>=1;m>>=1) s+=__shfl_xor(s,m);
  float mu=s/n; float q=0;
  #pragma unroll
  for(int i=0;i<16;i++){ if(i<cnt){ float d=v[i]-mu; q+=d*d; } }
  for(int m=32;m>=1;m>>=1) q+=__shfl_xor(q,m);
  float r=rsqrtf(q/n+1e-5f);
  #pragma unroll
  for(int i=0;i<16;i++){ if(i<cnt) out[(size_t)row*n+i*64+t]=f2bf((v[i]-mu)*r); }
}
__device__ __forceinline__ float gelu_t(float x){ float u=0.7978845608028654f*(x+0.044715f*x*x*x); float e=__expf(2.f*u); float t=1.f-2.f/(e+1.f); return 0.5f*x*(1.f+t); }

template<int MODE>
__global__ __launch_bounds__(256) void gemm(const u16* A,int lda,const u16* B,int ldb,int K,u16* C,int ldc,
                     const u16* e0,int lde0,const u16* e1, const u16* A2,int lda2,const u16* B2,int ldb2,int K2){
  __shared__ u16 As[64][40]; __shared__ u16 Bs[64][40];
  int t=threadIdx.x, lane=t&63, w=t>>6, wr=w>>1, wc=w&1, fr=lane&15, fq=lane>>4;
  int m0=blockIdx.y*64, n0=blockIdx.x*64;
  f32x4 acc[2][2]={};
  for(int pass=0; pass<(MODE==3?2:1); ++pass){
    const u16* a = pass? A2:A; const u16* b = pass? B2:B; int la=pass?lda2:lda, lb=pass?ldb2:ldb, kk=pass?K2:K;
    for(int k0=0;k0<kk;k0+=32){
      { int r=t>>2, c=(t&3)*8; *(bf16x8*)&As[r][c] = *(const bf16x8*)&a[(size_t)(m0+r)*la+k0+c]; }
      { int r=t>>3, c=(t&7)*8; bf16x8 v=*(const bf16x8*)&b[(size_t)(k0+r)*lb+n0+c];
        for(int j=0;j<8;j++) Bs[c+j][r]=(u16)v[j]; }
      __syncthreads();
      bf16x8 af[2], bfv[2];
      for(int i=0;i<2;i++){ af[i]=*(bf16x8*)&As[wr*32+i*16+fr][fq*8]; bfv[i]=*(bf16x8*)&Bs[wc*32+i*16+fr][fq*8]; }
      for(int i=0;i<2;i++) for(int j=0;j<2;j++) acc[i][j]=__builtin_amdgcn_mfma_f32_16x16x32_bf16(af[i],bfv[j],acc[i][j],0,0,0);
      __syncthreads();
    }
  }
  for(int i=0;i<2;i++) for(int j=0;j<2;j++) for(int r=0;r<4;r++){
    int row=m0+wr*32+i*16+fq*4+r, col=n0+wc*32+j*16+fr; float v=acc[i][j][r];
    if(MODE==1) v=gelu_t(v);
    if(MODE==2) v=bf2f(e0[(size_t)row*lde0+col])*(v+bf2f(e1[row]));
    if(MODE==3) v+=bf2f(e0[(size_t)row*lde0+col]);
    C[(size_t)row*ldc+col]=f2bf(v);
  }
}
__global__ void attn(const u16* qkv,u16* o){
  int idx=blockIdx.x*blockDim.x+threadIdx.x; int h=idx&31, s=idx>>5;
  float q[16]; for(int d=0;d<16;d++) q[d]=bf2f(qkv[(size_t)s*1536+h*16+d]);
  float l[8]; float mx=-1e30f;
  #pragma unroll
  for(int w=0;w<8;w++){ int p=s-2*w; float a=-1e9f; if(p>=0){ a=0; for(int d=0;d<16;d++) a+=q[d]*bf2f(qkv[(size_t)p*1536+512+h*16+d]); a*=0.25f; } l[w]=a; mx=fmaxf(mx,a);}
  float sum=0;
  #pragma unroll
  for(int w=0;w<8;w++){ l[w]=__expf(l[w]-mx); sum+=l[w]; }
  float acc[16]={};
  #pragma unroll
  for(int w=0;w<8;w++){ int p=s-2*w; if(p<0) p=0; for(int d=0;d<16;d++) acc[d]+=l[w]*bf2f(qkv[(size_t)p*1536+1024+h*16+d]); }
  for(int d=0;d<16;d++) o[(size_t)s*512+h*16+d]=f2bf(acc[d]/sum);
}
extern "C" void kernel_launch(void* const* d_in, const int* in_sizes, int n_in, void* d_out, int out_size, void* d_ws, size_t ws_size, hipStream_t stream){
  const u16* x=(const u16*)d_in[0]; const u16* wqkv=(const u16*)d_in[1]; const u16* wo=(const u16*)d_in[2];
  const u16* win=(const u16*)d_in[4]; const u16* wsp=(const u16*)d_in[5]; const u16* bsp=(const u16*)d_in[6]; const u16* wout=(const u16*)d_in[7];
  u16* ws=(u16*)d_ws; u16* xn=ws; u16* qkv=xn+S*D; u16* z=qkv+S*1536; u16* z2n=z+S*F; u16* o=z2n+S*FH; u16* h=o+S*D;
  ln_rows<<<S,64,0,stream>>>(x,D,0,D,xn);
  gemm<0><<<dim3(1536/64,S/64),256,0,stream>>>(xn,D,wqkv,1536,D,qkv,1536,0,0,0,0,0,0,0,0);
  gemm<1><<<dim3(F/64,S/64),256,0,stream>>>(xn,D,win,F,D,z,F,0,0,0,0,0,0,0,0);
  ln_rows<<<S,64,0,stream>>>(z,F,FH,FH,z2n);
  attn<<<S*32/256,256,0,stream>>>(qkv,o);
  gemm<2><<<dim3(FH/64,S/64),256,0,stream>>>(wsp,S,z2n,FH,S,h,FH,z,F,bsp,0,0,0,0,0);
  gemm<3><<<dim3(D/64,S/64),256,0,stream>>>(o,D,wo,D,D,(u16*)d_out,D,x,D,0,h,FH,wout,D,FH);
}
```

```cpp
#include <hip/hip_runtime.h>
#include <hip/hip_bf16.h>

__global__ __launch_bounds__(512, 2) void fused_block(const void* x, const void* wqkv, const void* wo, const void* w_in, const void* w_sp,
                                                      const void* b_sp, const void* w_out, void* out, void* ws) {
  __shared__ __attribute__((aligned(16))) char smem[163840];
  asm volatile(
    "L_entry_%=:\n"
    "s_load_dwordx16 s[80:95], s[0:1], 0\n"
    "s_load_dwordx2 s[36:37], s[0:1], 64\n"
    "s_getpc_b64 s[40:41]\n"
    "L_pc_%=:\n"
    "s_sub_u32 s40, s40, L_pc_%=-L_entry_%=\n"
    "s_subb_u32 s41, s41, 0\n"
    "s_mov_b32 s42, L_end_%=-L_entry_%=\n"
    "s_mov_b32 s43, 0x20000\n"
    "s_bfe_u32 s50, s2, 0x30003\n"
    "s_lshl_b32 s50, s50, 13\n"
    "v_bfe_u32 v1, v0, 3, 6\n"
    "v_lshlrev_b32 v1, 7, v1\n"
    "buffer_load_dword v255, v1, s[40:43], s50 offen\n"
    "v_lshrrev_b32 v1, 6, v0\n"
    "s_mov_b32 s6, 0x200000\n"
    "s_mov_b32 s7, 0x20000\n"
    "s_mov_b32 s18, 0x1c0000\n"
    "s_mov_b32 s19, 0x20000\n"
    "s_mov_b32 s38, 0xf00000\n"
    "s_mov_b32 s39, 0x20000\n"
    "s_mov_b32 s10, 0x180000\n"
    "s_mov_b32 s11, 0x20000\n"
    "s_mov_b32 s14, 0x80000\n"
    "s_mov_b32 s15, 0x20000\n"
    "s_mov_b32 s22, 0x800000\n"
    "s_mov_b32 s23, 0x20000\n"
    "s_mov_b32 s26, 0x1000\n"
    "s_mov_b32 s27, 0x20000\n"
    "s_mov_b32 s30, 0xe0000\n"
    "s_mov_b32 s31, 0x20000\n"
    "s_mov_b32 s34, 0x200000\n"
    "s_mov_b32 s35, 0x20000\n"
    "v_readfirstlane_b32 s44, v1\n"
    "s_and_b32 s50, s2, 7\n"
    "s_lshr_b32 s51, s2, 3\n"
    "s_lshr_b32 s46, s50, 1\n"
    "s_lshl_b32 s46, s46, 3\n"
    "s_and_b32 s62, s51, 7\n"
    "s_add_u32 s46, s46, s62\n"
    "s_and_b32 s47, s50, 1\n"
    "s_lshl_b32 s47, s47, 2\n"
    "s_lshr_b32 s51, s51, 3\n"
    "s_add_u32 s47, s47, s51\n"
    "s_and_b32 s45, s44, 3\n"
    "s_lshl_b32 s52, s45, 11\n"
    "s_lshl_b32 s57, s47, 7\n"
    "s_lshl_b32 s43, s47, 1\n"
    "s_add_u32 s43, s43, s46\n"
    "s_and_b32 s43, s43, 7\n"
    "s_cmp_ge_u32 s44, 4\n"
    "s_cbranch_scc1 L_H_%=\n"
    "v_and_b32 v1, 63, v0\n"
    "v_and_b32 v202, 15, v0\n"
    "v_bfe_u32 v203, v0, 4, 2\n"
    "v_bfe_u32 v204, v0, 2, 2\n"
    "v_and_b32 v205, 3, v0\n"
    "s_lshl_b32 s50, s46, 3\n"
    "s_add_u32 s50, s50, s44\n"
    "s_lshl_b32 s50, s50, 13\n"
    "v_lshlrev_b32 v82, 4, v202\n"
    "v_lshl_or_b32 v82, v203, 10, v82\n"
    "v_add_u32 v82, s50, v82\n"
    "s_waitcnt lgkmcnt(0)\n"
    "s_mov_b64 s[4:5], s[80:81]\n"
    "s_add_u32 s62, s47, 0\n"
    "s_and_b32 s62, s62, 3\n"
    "s_lshl_b32 s62, s62, 8\n"
    "buffer_load_dwordx4 v[2:5], v82, s[4:7], s62 offen\n"
    "s_add_u32 s62, s47, 1\n"
    "s_and_b32 s62, s62, 3\n"
    "s_lshl_b32 s62, s62, 8\n"
    "buffer_load_dwordx4 v[6:9], v82, s[4:7], s62 offen\n"
    "s_add_u32 s62, s47, 2\n"
    "s_and_b32 s62, s62, 3\n"
    "s_lshl_b32 s62, s62, 8\n"
    "buffer_load_dwordx4 v[10:13], v82, s[4:7], s62 offen\n"
    "s_add_u32 s62, s47, 3\n"
    "s_and_b32 s62, s62, 3\n"
    "s_lshl_b32 s62, s62, 8\n"
    "buffer_load_dwordx4 v[14:17], v82, s[4:7], s62 offen\n"
    "s_mov_b64 s[8:9], s[82:83]\n"
    "s_mov_b64 s[12:13], s[84:85]\n"
    "s_mov_b64 s[20:21], s[88:89]\n"
    "s_mov_b64 s[24:25], s[90:91]\n"
    "s_mov_b64 s[28:29], s[92:93]\n"
    "s_mov_b64 s[32:33], s[94:95]\n"
    "s_lshl_b32 s50, s45, 3\n"
    "s_bfe_u32 s51, s50, 0x20003\n"
    "s_lshl_b32 s51, s51, 1\n"
    "v_bfe_u32 v206, v0, 5, 1\n"
    "v_or_b32 v206, s51, v206\n"
    "v_and_b32 v207, 7, v0\n"
    "v_xor_b32 v206, v206, v207\n"
    "v_lshlrev_b32 v206, 4, v206\n"
    "v_lshl_or_b32 v206, v203, 7, v206\n"
    "s_lshl_b32 s51, s50, 7\n"
    "v_add_u32 v206, s51, v206\n"
    "v_bfe_u32 v207, v0, 3, 1\n"
    "s_add_u32 s51, s47, 0\n"
    "s_and_b32 s51, s51, 3\n"
    "s_lshl_b32 s51, s51, 1\n"
    "s_sub_u32 s51, s51, s43\n"
    "v_add_u32 v208, s51, v207\n"
    "v_and_b32 v208, 7, v208\n"
    "s_movk_i32 s51, 10240\n"
    "v_mad_u32_u24 v78, v208, s51, v206\n"
    "s_add_u32 s51, s47, 1\n"
    "s_and_b32 s51, s51, 3\n"
    "s_lshl_b32 s51, s51, 1\n"
    "s_sub_u32 s51, s51, s43\n"
    "v_add_u32 v208, s51, v207\n"
    "v_and_b32 v208, 7, v208\n"
    "s_movk_i32 s51, 10240\n"
    "v_mad_u32_u24 v79, v208, s51, v206\n"
    "s_add_u32 s51, s47, 2\n"
    "s_and_b32 s51, s51, 3\n"
    "s_lshl_b32 s51, s51, 1\n"
    "s_sub_u32 s51, s51, s43\n"
    "v_add_u32 v208, s51, v207\n"
    "v_and_b32 v208, 7, v208\n"
    "s_movk_i32 s51, 10240\n"
    "v_mad_u32_u24 v80, v208, s51, v206\n"
    "s_add_u32 s51, s47, 3\n"
    "s_and_b32 s51, s51, 3\n"
    "s_lshl_b32 s51, s51, 1\n"
    "s_sub_u32 s51, s51, s43\n"
    "v_add_u32 v208, s51, v207\n"
    "v_and_b32 v208, 7, v208\n"
    "s_movk_i32 s51, 10240\n"
    "v_mad_u32_u24 v81, v208, s51, v206\n"
    "v_mov_b32 v210, 0x3f803f80\n"
    "v_mov_b32 v211, 0x3f803f80\n"
    "v_mov_b32 v212, 0x3f803f80\n"
    "v_mov_b32 v213, 0x3f803f80\n"
    "s_and_b32 s55, s45, 1\n"
    "s_lshr_b32 s56, s45, 1\n"
    "s_lshl_b32 s58, s55, 2\n"
    "s_mov_b32 s59, s58\n"
    "v_bfe_u32 v206, v0, 1, 1\n"
    "v_bfe_u32 v207, v0, 3, 1\n"
    "v_lshl_or_b32 v206, v207, 1, v206\n"
    "v_lshlrev_b32 v130, 10, v207\n"
    "v_bfe_u32 v207, v0, 2, 1\n"
    "v_lshl_or_b32 v206, v207, 2, v206\n"
    "v_xor_b32 v206, v206, v203\n"
    "v_lshl_or_b32 v130, v206, 4, v130\n"
    "v_and_b32 v207, 7, v0\n"
    "v_lshl_or_b32 v130, v207, 7, v130\n"
    "s_lshl_b32 s62, s56, 12\n"
    "v_add_u32 v209, s62, v130\n"
    "v_add_u32 v216, 0x10000, v209\n"
    "v_add_u32 v217, 0x20000, v209\n"
    "v_xor_b32 v218, 64, v209\n"
    "v_add_u32 v219, 0x10000, v218\n"
    "v_add_u32 v220, 0x20000, v218\n"
    "v_bfe_u32 v206, v0, 1, 3\n"
    "v_xor_b32 v206, v206, v203\n"
    "v_lshlrev_b32 v206, 4, v206\n"
    "v_lshl_or_b32 v206, v205, 7, v206\n"
    "v_lshl_or_b32 v206, v204, 10, v206\n"
    "v_add_u32 v221, s62, v206\n"
    "v_add_u32 v222, 0x10000, v221\n"
    "v_xor_b32 v223, 64, v221\n"
    "v_add_u32 v224, 0x10000, v223\n"
    "s_add_u32 s53, s58, 0\n"
    "s_and_b32 s51, s47, 1\n"
    "s_add_u32 s51, s51, s53\n"
    "s_and_b32 s51, s51, 7\n"
    "v_bfe_u32 v206, v0, 4, 1\n"
    "v_lshl_or_b32 v206, v206, 2, v204\n"
    "v_xor_b32 v206, s51, v206\n"
    "v_lshl_or_b32 v207, v203, 3, v204\n"
    "v_lshlrev_b32 v207, 8, v207\n"
    "v_lshl_or_b32 v207, v206, 5, v207\n"
    "v_lshl_or_b32 v208, v205, 3, v207\n"
    "v_add_u32 v225, 0x10000, v208\n"
    "v_add_u32 v226, 0x20000, v208\n"
    "s_add_u32 s54, s58, 1\n"
    "s_and_b32 s51, s47, 1\n"
    "s_add_u32 s51, s51, s54\n"
    "s_and_b32 s51, s51, 7\n"
    "v_bfe_u32 v206, v0, 4, 1\n"
    "v_lshl_or_b32 v206, v206, 2, v204\n"
    "v_xor_b32 v206, s51, v206\n"
    "v_lshl_or_b32 v207, v203, 3, v204\n"
    "v_lshlrev_b32 v207, 8, v207\n"
    "v_lshl_or_b32 v207, v206, 5, v207\n"
    "v_lshl_or_b32 v208, v205, 3, v207\n"
    "v_add_u32 v227, 0x10000, v208\n"
    "v_add_u32 v228, 0x20000, v208\n"
    "s_add_u32 s60, s59, 2\n"
    "s_and_b32 s51, s47, 1\n"
    "s_add_u32 s51, s51, s60\n"
    "s_and_b32 s51, s51, 7\n"
    "v_bfe_u32 v206, v0, 4, 1\n"
    "v_lshl_or_b32 v206, v206, 2, v204\n"
    "v_xor_b32 v206, s51, v206\n"
    "v_lshl_or_b32 v207, v203, 3, v204\n"
    "v_lshlrev_b32 v207, 8, v207\n"
    "v_lshl_or_b32 v207, v206, 5, v207\n"
    "v_lshl_or_b32 v208, v205, 3, v207\n"
    "v_add_u32 v229, 0x10000, v208\n"
    "v_add_u32 v230, 0x20000, v208\n"
    "s_add_u32 s61, s59, 3\n"
    "s_and_b32 s51, s47, 1\n"
    "s_add_u32 s51, s51, s61\n"
    "s_and_b32 s51, s51, 7\n"
    "v_bfe_u32 v206, v0, 4, 1\n"
    "v_lshl_or_b32 v206, v206, 2, v204\n"
    "v_xor_b32 v206, s51, v206\n"
    "v_lshl_or_b32 v207, v203, 3, v204\n"
    "v_lshlrev_b32 v207, 8, v207\n"
    "v_lshl_or_b32 v207, v206, 5, v207\n"
    "v_lshl_or_b32 v208, v205, 3, v207\n"
    "v_add_u32 v231, 0x10000, v208\n"
    "v_add_u32 v232, 0x20000, v208\n"
    "s_cmp_eq_u32 s61, 7\n"
    "s_cselect_b32 s96, 0, 1.0\n"
    "s_mov_b32 s97, s96\n"
    "s_cselect_b32 s61, 0x8000, s61\n"
    "v_lshlrev_b32 v233, 3, v1\n"
    "v_lshlrev_b32 v234, 4, v1\n"
    "s_lshl_b32 s51, s56, 7\n"
    "v_lshlrev_b32 v206, 5, v203\n"
    "v_add_u32 v206, s51, v206\n"
    "v_add_u32 v235, 0x27000, v206\n"
    "v_lshl_add_u32 v206, v204, 2, v206\n"
    "v_lshlrev_b32 v236, 2, v206\n"
    "v_mov_b32 v238, 0xc0135761\n"
    "v_mov_b32 v240, 0xbdd2d3e8\n"
    "v_mov_b32 v239, 0xc0135761\n"
    "v_mov_b32 v241, 0xbdd2d3e8\n"
    "s_cmp_eq_u32 s46, 0\n"
    "s_cselect_b32 s3, 0xf149f2ca, 0\n"
    "s_add_u32 s62, s47, 0\n"
    "s_and_b32 s62, s62, 3\n"
    "s_lshl_b32 s62, s62, 8\n"
    "s_add_u32 s62, s62, 4096\n"
    "buffer_load_dwordx4 v[18:21], v82, s[4:7], s62 offen\n"
    "s_add_u32 s62, s47, 1\n"
    "s_and_b32 s62, s62, 3\n"
    "s_lshl_b32 s62, s62, 8\n"
    "s_add_u32 s62, s62, 4096\n"
    "buffer_load_dwordx4 v[22:25], v82, s[4:7], s62 offen\n"
    "s_add_u32 s62, s47, 2\n"
    "s_and_b32 s62, s62, 3\n"
    "s_lshl_b32 s62, s62, 8\n"
    "s_add_u32 s62, s62, 4096\n"
    "buffer_load_dwordx4 v[26:29], v82, s[4:7], s62 offen\n"
    "s_add_u32 s62, s47, 3\n"
    "s_and_b32 s62, s62, 3\n"
    "s_lshl_b32 s62, s62, 8\n"
    "s_add_u32 s62, s62, 4096\n"
    "buffer_load_dwordx4 v[30:33], v82, s[4:7], s62 offen\n"
    "s_waitcnt vmcnt(4)\n"
    "v_dot2_f32_bf16 v66, v2, v210, 0\n"
    "v_dot2_f32_bf16 v68, v2, v2, 0\n"
    "v_dot2_f32_bf16 v67, v3, v210, 0\n"
    "v_dot2_f32_bf16 v69, v3, v3, 0\n"
    "v_dot2_f32_bf16 v66, v4, v210, v66\n"
    "v_dot2_f32_bf16 v68, v4, v4, v68\n"
    "v_dot2_f32_bf16 v67, v5, v210, v67\n"
    "v_dot2_f32_bf16 v69, v5, v5, v69\n"
    "v_dot2_f32_bf16 v66, v6, v210, v66\n"
    "v_dot2_f32_bf16 v68, v6, v6, v68\n"
    "v_dot2_f32_bf16 v67, v7, v210, v67\n"
    "v_dot2_f32_bf16 v69, v7, v7, v69\n"
    "v_dot2_f32_bf16 v66, v8, v210, v66\n"
    "v_dot2_f32_bf16 v68, v8, v8, v68\n"
    "v_dot2_f32_bf16 v67, v9, v210, v67\n"
    "v_dot2_f32_bf16 v69, v9, v9, v69\n"
    "v_dot2_f32_bf16 v66, v10, v210, v66\n"
    "v_dot2_f32_bf16 v68, v10, v10, v68\n"
    "v_dot2_f32_bf16 v67, v11, v210, v67\n"
    "v_dot2_f32_bf16 v69, v11, v11, v69\n"
    "v_dot2_f32_bf16 v66, v12, v210, v66\n"
    "v_dot2_f32_bf16 v68, v12, v12, v68\n"
    "v_dot2_f32_bf16 v67, v13, v210, v67\n"
    "v_dot2_f32_bf16 v69, v13, v13, v69\n"
    "v_dot2_f32_bf16 v66, v14, v210, v66\n"
    "v_dot2_f32_bf16 v68, v14, v14, v68\n"
    "v_dot2_f32_bf16 v67, v15, v210, v67\n"
    "v_dot2_f32_bf16 v69, v15, v15, v69\n"
    "v_dot2_f32_bf16 v66, v16, v210, v66\n"
    "v_dot2_f32_bf16 v68, v16, v16, v68\n"
    "v_dot2_f32_bf16 v67, v17, v210, v67\n"
    "v_dot2_f32_bf16 v69, v17, v17, v69\n"
    "s_nop 1\n"
    "v_add_f32 v66, v66, v67\n"
    "v_add_f32 v68, v68, v69\n"
    "s_nop 0\n"
    "v_add_f32_dpp v66, v66, v66 row_ror:8 row_mask:0xf bank_mask:0xf\n"
    "v_add_f32_dpp v68, v68, v68 row_ror:8 row_mask:0xf bank_mask:0xf\n"
    "s_nop 0\n"
    "v_add_f32_dpp v66, v66, v66 row_ror:4 row_mask:0xf bank_mask:0xf\n"
    "v_add_f32_dpp v68, v68, v68 row_ror:4 row_mask:0xf bank_mask:0xf\n"
    "s_nop 0\n"
    "v_add_f32_dpp v66, v66, v66 row_ror:2 row_mask:0xf bank_mask:0xf\n"
    "v_add_f32_dpp v68, v68, v68 row_ror:2 row_mask:0xf bank_mask:0xf\n"
    "s_nop 0\n"
    "v_add_f32_dpp v66, v66, v66 row_ror:1 row_mask:0xf bank_mask:0xf\n"
    "v_add_f32_dpp v68, v68, v68 row_ror:1 row_mask:0xf bank_mask:0xf\n"
    "v_mul_f32 v66, 0xbb000000, v66\n"
    "v_mul_f32 v68, 0x3b000000, v68\n"
    "v_fma_f32 v68, -v66, v66, v68\n"
    "v_max_f32 v68, 0, v68\n"
    "v_add_f32 v68, 0x3727c5ac, v68\n"
    "v_rsq_f32 v68, v68\n"
    "s_nop 0\n"
    "v_mul_f32 v66, v66, v68\n"
    "v_lshlrev_b32 v70, 16, v2\n"
    "v_and_b32 v71, 0xffff0000, v2\n"
    "v_lshlrev_b32 v72, 16, v3\n"
    "v_and_b32 v73, 0xffff0000, v3\n"
    "v_lshlrev_b32 v74, 16, v4\n"
    "v_and_b32 v75, 0xffff0000, v4\n"
    "v_lshlrev_b32 v76, 16, v5\n"
    "v_and_b32 v77, 0xffff0000, v5\n"
    "v_pk_fma_f32 v[70:71], v[70:71], v[68:69], v[66:67] op_sel_hi:[1,0,0]\n"
    "v_pk_fma_f32 v[72:73], v[72:73], v[68:69], v[66:67] op_sel_hi:[1,0,0]\n"
    "v_pk_fma_f32 v[74:75], v[74:75], v[68:69], v[66:67] op_sel_hi:[1,0,0]\n"
    "v_pk_fma_f32 v[76:77], v[76:77], v[68:69], v[66:67] op_sel_hi:[1,0,0]\n"
    "v_cvt_pk_bf16_f32 v2, v70, v71\n"
    "v_cvt_pk_bf16_f32 v3, v72, v73\n"
    "v_cvt_pk_bf16_f32 v4, v74, v75\n"
    "v_cvt_pk_bf16_f32 v5, v76, v77\n"
    "ds_write_b128 v78, v[2:5] offset:0\n"
    "v_lshlrev_b32 v70, 16, v6\n"
    "v_and_b32 v71, 0xffff0000, v6\n"
    "v_lshlrev_b32 v72, 16, v7\n"
    "v_and_b32 v73, 0xffff0000, v7\n"
    "v_lshlrev_b32 v74, 16, v8\n"
    "v_and_b32 v75, 0xffff0000, v8\n"
    "v_lshlrev_b32 v76, 16, v9\n"
    "v_and_b32 v77, 0xffff0000, v9\n"
    "v_pk_fma_f32 v[70:71], v[70:71], v[68:69], v[66:67] op_sel_hi:[1,0,0]\n"
    "v_pk_fma_f32 v[72:73], v[72:73], v[68:69], v[66:67] op_sel_hi:[1,0,0]\n"
    "v_pk_fma_f32 v[74:75], v[74:75], v[68:69], v[66:67] op_sel_hi:[1,0,0]\n"
    "v_pk_fma_f32 v[76:77], v[76:77], v[68:69], v[66:67] op_sel_hi:[1,0,0]\n"
    "v_cvt_pk_bf16_f32 v6, v70, v71\n"
    "v_cvt_pk_bf16_f32 v7, v72, v73\n"
    "v_cvt_pk_bf16_f32 v8, v74, v75\n"
    "v_cvt_pk_bf16_f32 v9, v76, v77\n"
    "ds_write_b128 v79, v[6:9] offset:0\n"
    "v_lshlrev_b32 v70, 16, v10\n"
    "v_and_b32 v71, 0xffff0000, v10\n"
    "v_lshlrev_b32 v72, 16, v11\n"
    "v_and_b32 v73, 0xffff0000, v11\n"
    "v_lshlrev_b32 v74, 16, v12\n"
    "v_and_b32 v75, 0xffff0000, v12\n"
    "v_lshlrev_b32 v76, 16, v13\n"
    "v_and_b32 v77, 0xffff0000, v13\n"
    "v_pk_fma_f32 v[70:71], v[70:71], v[68:69], v[66:67] op_sel_hi:[1,0,0]\n"
    "v_pk_fma_f32 v[72:73], v[72:73], v[68:69], v[66:67] op_sel_hi:[1,0,0]\n"
    "v_pk_fma_f32 v[74:75], v[74:75], v[68:69], v[66:67] op_sel_hi:[1,0,0]\n"
    "v_pk_fma_f32 v[76:77], v[76:77], v[68:69], v[66:67] op_sel_hi:[1,0,0]\n"
    "v_cvt_pk_bf16_f32 v10, v70, v71\n"
    "v_cvt_pk_bf16_f32 v11, v72, v73\n"
    "v_cvt_pk_bf16_f32 v12, v74, v75\n"
    "v_cvt_pk_bf16_f32 v13, v76, v77\n"
    "ds_write_b128 v80, v[10:13] offset:0\n"
    "v_lshlrev_b32 v70, 16, v14\n"
    "v_and_b32 v71, 0xffff0000, v14\n"
    "v_lshlrev_b32 v72, 16, v15\n"
    "v_and_b32 v73, 0xffff0000, v15\n"
    "v_lshlrev_b32 v74, 16, v16\n"
    "v_and_b32 v75, 0xffff0000, v16\n"
    "v_lshlrev_b32 v76, 16, v17\n"
    "v_and_b32 v77, 0xffff0000, v17\n"
    "v_pk_fma_f32 v[70:71], v[70:71], v[68:69], v[66:67] op_sel_hi:[1,0,0]\n"
    "v_pk_fma_f32 v[72:73], v[72:73], v[68:69], v[66:67] op_sel_hi:[1,0,0]\n"
    "v_pk_fma_f32 v[74:75], v[74:75], v[68:69], v[66:67] op_sel_hi:[1,0,0]\n"
    "v_pk_fma_f32 v[76:77], v[76:77], v[68:69], v[66:67] op_sel_hi:[1,0,0]\n"
    "v_cvt_pk_bf16_f32 v14, v70, v71\n"
    "v_cvt_pk_bf16_f32 v15, v72, v73\n"
    "v_cvt_pk_bf16_f32 v16, v74, v75\n"
    "v_cvt_pk_bf16_f32 v17, v76, v77\n"
    "ds_write_b128 v81, v[14:17] offset:0\n"
    "s_waitcnt vmcnt(0)\n"
    "v_dot2_f32_bf16 v66, v18, v210, 0\n"
    "v_dot2_f32_bf16 v68, v18, v18, 0\n"
    "v_dot2_f32_bf16 v67, v19, v210, 0\n"
    "v_dot2_f32_bf16 v69, v19, v19, 0\n"
    "v_dot2_f32_bf16 v66, v20, v210, v66\n"
    "v_dot2_f32_bf16 v68, v20, v20, v68\n"
    "v_dot2_f32_bf16 v67, v21, v210, v67\n"
    "v_dot2_f32_bf16 v69, v21, v21, v69\n"
    "v_dot2_f32_bf16 v66, v22, v210, v66\n"
    "v_dot2_f32_bf16 v68, v22, v22, v68\n"
    "v_dot2_f32_bf16 v67, v23, v210, v67\n"
    "v_dot2_f32_bf16 v69, v23, v23, v69\n"
    "v_dot2_f32_bf16 v66, v24, v210, v66\n"
    "v_dot2_f32_bf16 v68, v24, v24, v68\n"
    "v_dot2_f32_bf16 v67, v25, v210, v67\n"
    "v_dot2_f32_bf16 v69, v25, v25, v69\n"
    "v_dot2_f32_bf16 v66, v26, v210, v66\n"
    "v_dot2_f32_bf16 v68, v26, v26, v68\n"
    "v_dot2_f32_bf16 v67, v27, v210, v67\n"
    "v_dot2_f32_bf16 v69, v27, v27, v69\n"
    "v_dot2_f32_bf16 v66, v28, v210, v66\n"
    "v_dot2_f32_bf16 v68, v28, v28, v68\n"
    "v_dot2_f32_bf16 v67, v29, v210, v67\n"
    "v_dot2_f32_bf16 v69, v29, v29, v69\n"
    "v_dot2_f32_bf16 v66, v30, v210, v66\n"
    "v_dot2_f32_bf16 v68, v30, v30, v68\n"
    "v_dot2_f32_bf16 v67, v31, v210, v67\n"
    "v_dot2_f32_bf16 v69, v31, v31, v69\n"
    "v_dot2_f32_bf16 v66, v32, v210, v66\n"
    "v_dot2_f32_bf16 v68, v32, v32, v68\n"
    "v_dot2_f32_bf16 v67, v33, v210, v67\n"
    "v_dot2_f32_bf16 v69, v33, v33, v69\n"
    "s_nop 1\n"
    "v_add_f32 v66, v66, v67\n"
    "v_add_f32 v68, v68, v69\n"
    "s_nop 0\n"
    "v_add_f32_dpp v66, v66, v66 row_ror:8 row_mask:0xf bank_mask:0xf\n"
    "v_add_f32_dpp v68, v68, v68 row_ror:8 row_mask:0xf bank_mask:0xf\n"
    "s_nop 0\n"
    "v_add_f32_dpp v66, v66, v66 row_ror:4 row_mask:0xf bank_mask:0xf\n"
    "v_add_f32_dpp v68, v68, v68 row_ror:4 row_mask:0xf bank_mask:0xf\n"
    "s_nop 0\n"
    "v_add_f32_dpp v66, v66, v66 row_ror:2 row_mask:0xf bank_mask:0xf\n"
    "v_add_f32_dpp v68, v68, v68 row_ror:2 row_mask:0xf bank_mask:0xf\n"
    "s_nop 0\n"
    "v_add_f32_dpp v66, v66, v66 row_ror:1 row_mask:0xf bank_mask:0xf\n"
    "v_add_f32_dpp v68, v68, v68 row_ror:1 row_mask:0xf bank_mask:0xf\n"
    "v_mul_f32 v66, 0xbb000000, v66\n"
    "v_mul_f32 v68, 0x3b000000, v68\n"
    "v_fma_f32 v68, -v66, v66, v68\n"
    "v_max_f32 v68, 0, v68\n"
    "v_add_f32 v68, 0x3727c5ac, v68\n"
    "v_rsq_f32 v68, v68\n"
    "s_nop 0\n"
    "v_mul_f32 v66, v66, v68\n"
    "v_lshlrev_b32 v70, 16, v18\n"
    "v_and_b32 v71, 0xffff0000, v18\n"
    "v_lshlrev_b32 v72, 16, v19\n"
    "v_and_b32 v73, 0xffff0000, v19\n"
    "v_lshlrev_b32 v74, 16, v20\n"
    "v_and_b32 v75, 0xffff0000, v20\n"
    "v_lshlrev_b32 v76, 16, v21\n"
    "v_and_b32 v77, 0xffff0000, v21\n"
    "v_pk_fma_f32 v[70:71], v[70:71], v[68:69], v[66:67] op_sel_hi:[1,0,0]\n"
    "v_pk_fma_f32 v[72:73], v[72:73], v[68:69], v[66:67] op_sel_hi:[1,0,0]\n"
    "v_pk_fma_f32 v[74:75], v[74:75], v[68:69], v[66:67] op_sel_hi:[1,0,0]\n"
    "v_pk_fma_f32 v[76:77], v[76:77], v[68:69], v[66:67] op_sel_hi:[1,0,0]\n"
    "v_cvt_pk_bf16_f32 v18, v70, v71\n"
    "v_cvt_pk_bf16_f32 v19, v72, v73\n"
    "v_cvt_pk_bf16_f32 v20, v74, v75\n"
    "v_cvt_pk_bf16_f32 v21, v76, v77\n"
    "v_xor_b32 v78, 64, v78\n"
    "ds_write_b128 v78, v[18:21] offset:512\n"
    "v_lshlrev_b32 v70, 16, v22\n"
    "v_and_b32 v71, 0xffff0000, v22\n"
    "v_lshlrev_b32 v72, 16, v23\n"
    "v_and_b32 v73, 0xffff0000, v23\n"
    "v_lshlrev_b32 v74, 16, v24\n"
    "v_and_b32 v75, 0xffff0000, v24\n"
    "v_lshlrev_b32 v76, 16, v25\n"
    "v_and_b32 v77, 0xffff0000, v25\n"
    "v_pk_fma_f32 v[70:71], v[70:71], v[68:69], v[66:67] op_sel_hi:[1,0,0]\n"
    "v_pk_fma_f32 v[72:73], v[72:73], v[68:69], v[66:67] op_sel_hi:[1,0,0]\n"
    "v_pk_fma_f32 v[74:75], v[74:75], v[68:69], v[66:67] op_sel_hi:[1,0,0]\n"
    "v_pk_fma_f32 v[76:77], v[76:77], v[68:69], v[66:67] op_sel_hi:[1,0,0]\n"
    "v_cvt_pk_bf16_f32 v22, v70, v71\n"
    "v_cvt_pk_bf16_f32 v23, v72, v73\n"
    "v_cvt_pk_bf16_f32 v24, v74, v75\n"
    "v_cvt_pk_bf16_f32 v25, v76, v77\n"
    "v_xor_b32 v79, 64, v79\n"
    "ds_write_b128 v79, v[22:25] offset:512\n"
    "v_lshlrev_b32 v70, 16, v26\n"
    "v_and_b32 v71, 0xffff0000, v26\n"
    "v_lshlrev_b32 v72, 16, v27\n"
    "v_and_b32 v73, 0xffff0000, v27\n"
    "v_lshlrev_b32 v74, 16, v28\n"
    "v_and_b32 v75, 0xffff0000, v28\n"
    "v_lshlrev_b32 v76, 16, v29\n"
    "v_and_b32 v77, 0xffff0000, v29\n"
    "v_pk_fma_f32 v[70:71], v[70:71], v[68:69], v[66:67] op_sel_hi:[1,0,0]\n"
    "v_pk_fma_f32 v[72:73], v[72:73], v[68:69], v[66:67] op_sel_hi:[1,0,0]\n"
    "v_pk_fma_f32 v[74:75], v[74:75], v[68:69], v[66:67] op_sel_hi:[1,0,0]\n"
    "v_pk_fma_f32 v[76:77], v[76:77], v[68:69], v[66:67] op_sel_hi:[1,0,0]\n"
    "v_cvt_pk_bf16_f32 v26, v70, v71\n"
    "v_cvt_pk_bf16_f32 v27, v72, v73\n"
    "v_cvt_pk_bf16_f32 v28, v74, v75\n"
    "v_cvt_pk_bf16_f32 v29, v76, v77\n"
    "v_xor_b32 v80, 64, v80\n"
    "ds_write_b128 v80, v[26:29] offset:512\n"
    "v_lshlrev_b32 v70, 16, v30\n"
    "v_and_b32 v71, 0xffff0000, v30\n"
    "v_lshlrev_b32 v72, 16, v31\n"
    "v_and_b32 v73, 0xffff0000, v31\n"
    "v_lshlrev_b32 v74, 16, v32\n"
    "v_and_b32 v75, 0xffff0000, v32\n"
    "v_lshlrev_b32 v76, 16, v33\n"
    "v_and_b32 v77, 0xffff0000, v33\n"
    "v_pk_fma_f32 v[70:71], v[70:71], v[68:69], v[66:67] op_sel_hi:[1,0,0]\n"
    "v_pk_fma_f32 v[72:73], v[72:73], v[68:69], v[66:67] op_sel_hi:[1,0,0]\n"
    "v_pk_fma_f32 v[74:75], v[74:75], v[68:69], v[66:67] op_sel_hi:[1,0,0]\n"
    "v_pk_fma_f32 v[76:77], v[76:77], v[68:69], v[66:67] op_sel_hi:[1,0,0]\n"
    "v_cvt_pk_bf16_f32 v30, v70, v71\n"
    "v_cvt_pk_bf16_f32 v31, v72, v73\n"
    "v_cvt_pk_bf16_f32 v32, v74, v75\n"
    "v_cvt_pk_bf16_f32 v33, v76, v77\n"
    "v_xor_b32 v81, 64, v81\n"
    "ds_write_b128 v81, v[30:33] offset:512\n"
    "s_waitcnt lgkmcnt(0)\n"
    "s_barrier\n"
    "buffer_load_dwordx2 v[214:215], off, s[36:39], 0 sc1\n"
    "ds_read_b128 v[2:5], v221 offset:0\n"
    "ds_read_b64_tr_b16 v[10:11], v225 offset:24576\n"
    "ds_read_b64_tr_b16 v[12:13], v225 offset:25600\n"
    "ds_read_b64_tr_b16 v[14:15], v227 offset:24576\n"
    "ds_read_b64_tr_b16 v[16:17], v227 offset:25600\n"
    "ds_read_b128 v[6:9], v223 offset:512\n"
    "ds_read_b64_tr_b16 v[18:19], v229 offset:24576\n"
    "ds_read_b64_tr_b16 v[20:21], v229 offset:25600\n"
    "ds_read_b64_tr_b16 v[22:23], v231 offset:24576\n"
    "ds_read_b64_tr_b16 v[24:25], v231 offset:25600\n"
    "ds_read_b128 v[26:29], v223 offset:0\n"
    "ds_read_b64_tr_b16 v[34:35], v225 offset:32768\n"
    "ds_read_b64_tr_b16 v[36:37], v225 offset:33792\n"
    "ds_read_b64_tr_b16 v[38:39], v227 offset:32768\n"
    "ds_read_b64_tr_b16 v[40:41], v227 offset:33792\n"
    "s_waitcnt lgkmcnt(14)\n"
    "ds_read_b128 v[30:33], v221 offset:512\n"
    "s_waitcnt lgkmcnt(14)\n"
    "ds_read_b64_tr_b16 v[42:43], v229 offset:32768\n"
    "s_waitcnt lgkmcnt(14)\n"
    "ds_read_b64_tr_b16 v[44:45], v229 offset:33792\n"
    "s_waitcnt lgkmcnt(14)\n"
    "ds_read_b64_tr_b16 v[46:47], v231 offset:32768\n"
    "s_waitcnt lgkmcnt(14)\n"
    "ds_read_b64_tr_b16 v[48:49], v231 offset:33792\n"
    "s_waitcnt lgkmcnt(0)\n"
    "s_barrier\n"
    "v_mfma_f32_16x16x32_bf16 v[130:133], v[2:5], v[10:13], 0\n"
    "ds_read_b128 v[50:53], v221 offset:10240\n"
    "ds_read_b64_tr_b16 v[58:59], v225 offset:40960\n"
    "v_mfma_f32_16x16x32_bf16 v[134:137], v[6:9], v[10:13], 0\n"
    "ds_read_b64_tr_b16 v[60:61], v225 offset:41984\n"
    "ds_read_b64_tr_b16 v[62:63], v227 offset:40960\n"
    "v_mfma_f32_16x16x32_bf16 v[138:141], v[2:5], v[14:17], 0\n"
    "ds_read_b64_tr_b16 v[64:65], v227 offset:41984\n"
    "ds_read_b128 v[54:57], v223 offset:10752\n"
    "v_mfma_f32_16x16x32_bf16 v[142:145], v[6:9], v[14:17], 0\n"
    "ds_read_b64_tr_b16 v[66:67], v229 offset:40960\n"
    "ds_read_b64_tr_b16 v[68:69], v229 offset:41984\n"
    "v_mfma_f32_16x16x32_bf16 v[146:149], v[2:5], v[18:21], 0\n"
    "ds_read_b64_tr_b16 v[70:71], v231 offset:40960\n"
    "ds_read_b64_tr_b16 v[72:73], v231 offset:41984\n"
    "v_mfma_f32_16x16x32_bf16 v[150:153], v[6:9], v[18:21], 0\n"
    "ds_read_b128 v[74:77], v223 offset:10240\n"
    "v_mfma_f32_16x16x32_bf16 v[154:157], v[2:5], v[22:25], 0\n"
    "ds_read_b64_tr_b16 v[82:83], v225 offset:49152\n"
    "ds_read_b64_tr_b16 v[84:85], v225 offset:50176\n"
    "v_mfma_f32_16x16x32_bf16 v[158:161], v[6:9], v[22:25], 0\n"
    "ds_read_b64_tr_b16 v[86:87], v227 offset:49152\n"
    "ds_read_b64_tr_b16 v[88:89], v227 offset:50176\n"
    "v_mfma_f32_16x16x32_bf16 v[130:133], v[26:29], v[34:37], v[130:133]\n"
    "s_waitcnt lgkmcnt(14)\n"
    "ds_read_b128 v[78:81], v221 offset:10752\n"
    "s_waitcnt lgkmcnt(14)\n"
    "ds_read_b64_tr_b16 v[90:91], v229 offset:49152\n"
    "v_mfma_f32_16x16x32_bf16 v[134:137], v[30:33], v[34:37], v[134:137]\n"
    "s_waitcnt lgkmcnt(14)\n"
    "ds_read_b64_tr_b16 v[92:93], v229 offset:50176\n"
    "s_waitcnt lgkmcnt(14)\n"
    "ds_read_b64_tr_b16 v[94:95], v231 offset:49152\n"
    "v_mfma_f32_16x16x32_bf16 v[138:141], v[26:29], v[38:41], v[138:141]\n"
    "s_waitcnt lgkmcnt(14)\n"
    "ds_read_b64_tr_b16 v[96:97], v231 offset:50176\n"
    "v_mfma_f32_16x16x32_bf16 v[142:145], v[30:33], v[38:41], v[142:145]\n"
    "v_mfma_f32_16x16x32_bf16 v[146:149], v[26:29], v[42:45], v[146:149]\n"
    "v_mfma_f32_16x16x32_bf16 v[150:153], v[30:33], v[42:45], v[150:153]\n"
    "v_mfma_f32_16x16x32_bf16 v[154:157], v[26:29], v[46:49], v[154:157]\n"
    "v_mfma_f32_16x16x32_bf16 v[158:161], v[30:33], v[46:49], v[158:161]\n"
    "s_waitcnt lgkmcnt(0)\n"
    "s_barrier\n"
    "v_mfma_f32_16x16x32_bf16 v[130:133], v[50:53], v[58:61], v[130:133]\n"
    "ds_read_b128 v[2:5], v221 offset:20480\n"
    "ds_read_b64_tr_b16 v[10:11], v225 offset:57344\n"
    "v_mfma_f32_16x16x32_bf16 v[134:137], v[54:57], v[58:61], v[134:137]\n"
    "ds_read_b64_tr_b16 v[12:13], v225 offset:58368\n"
    "ds_read_b64_tr_b16 v[14:15], v227 offset:57344\n"
    "v_mfma_f32_16x16x32_bf16 v[138:141], v[50:53], v[62:65], v[138:141]\n"
    "ds_read_b64_tr_b16 v[16:17], v227 offset:58368\n"
    "ds_read_b128 v[6:9], v223 offset:20992\n"
    "v_mfma_f32_16x16x32_bf16 v[142:145], v[54:57], v[62:65], v[142:145]\n"
    "ds_read_b64_tr_b16 v[18:19], v229 offset:57344\n"
    "ds_read_b64_tr_b16 v[20:21], v229 offset:58368\n"
    "v_mfma_f32_16x16x32_bf16 v[146:149], v[50:53], v[66:69], v[146:149]\n"
    "ds_read_b64_tr_b16 v[22:23], v231 offset:57344\n"
    "ds_read_b64_tr_b16 v[24:25], v231 offset:58368\n"
    "v_mfma_f32_16x16x32_bf16 v[150:153], v[54:57], v[66:69], v[150:153]\n"
    "ds_read_b128 v[26:29], v223 offset:20480\n"
    "v_mfma_f32_16x16x32_bf16 v[154:157], v[50:53], v[70:73], v[154:157]\n"
    "ds_read_b64_tr_b16 v[34:35], v226 offset:0\n"
    "ds_read_b64_tr_b16 v[36:37], v226 offset:1024\n"
    "v_mfma_f32_16x16x32_bf16 v[158:161], v[54:57], v[70:73], v[158:161]\n"
    "ds_read_b64_tr_b16 v[38:39], v228 offset:0\n"
    "ds_read_b64_tr_b16 v[40:41], v228 offset:1024\n"
    "v_mfma_f32_16x16x32_bf16 v[130:133], v[74:77], v[82:85], v[130:133]\n"
    "s_waitcnt lgkmcnt(14)\n"
    "ds_read_b128 v[30:33], v221 offset:20992\n"
    "s_waitcnt lgkmcnt(14)\n"
    "ds_read_b64_tr_b16 v[42:43], v230 offset:0\n"
    "v_mfma_f32_16x16x32_bf16 v[134:137], v[78:81], v[82:85], v[134:137]\n"
    "s_waitcnt lgkmcnt(14)\n"
    "ds_read_b64_tr_b16 v[44:45], v230 offset:1024\n"
    "s_waitcnt lgkmcnt(14)\n"
    "ds_read_b64_tr_b16 v[46:47], v232 offset:0\n"
    "v_mfma_f32_16x16x32_bf16 v[138:141], v[74:77], v[86:89], v[138:141]\n"
    "s_waitcnt lgkmcnt(14)\n"
    "ds_read_b64_tr_b16 v[48:49], v232 offset:1024\n"
    "v_mfma_f32_16x16x32_bf16 v[142:145], v[78:81], v[86:89], v[142:145]\n"
    "v_mfma_f32_16x16x32_bf16 v[146:149], v[74:77], v[90:93], v[146:149]\n"
    "v_mfma_f32_16x16x32_bf16 v[150:153], v[78:81], v[90:93], v[150:153]\n"
    "v_mfma_f32_16x16x32_bf16 v[154:157], v[74:77], v[94:97], v[154:157]\n"
    "v_mfma_f32_16x16x32_bf16 v[158:161], v[78:81], v[94:97], v[158:161]\n"
    "s_waitcnt lgkmcnt(0)\n"
    "s_barrier\n"
    "v_mfma_f32_16x16x32_bf16 v[130:133], v[2:5], v[10:13], v[130:133]\n"
    "ds_read_b128 v[50:53], v221 offset:30720\n"
    "ds_read_b64_tr_b16 v[58:59], v226 offset:8192\n"
    "v_mfma_f32_16x16x32_bf16 v[134:137], v[6:9], v[10:13], v[134:137]\n"
    "ds_read_b64_tr_b16 v[60:61], v226 offset:9216\n"
    "ds_read_b64_tr_b16 v[62:63], v228 offset:8192\n"
    "v_mfma_f32_16x16x32_bf16 v[138:141], v[2:5], v[14:17], v[138:141]\n"
    "ds_read_b64_tr_b16 v[64:65], v228 offset:9216\n"
    "ds_read_b128 v[54:57], v223 offset:31232\n"
    "v_mfma_f32_16x16x32_bf16 v[142:145], v[6:9], v[14:17], v[142:145]\n"
    "ds_read_b64_tr_b16 v[66:67], v230 offset:8192\n"
    "ds_read_b64_tr_b16 v[68:69], v230 offset:9216\n"
    "v_mfma_f32_16x16x32_bf16 v[146:149], v[2:5], v[18:21], v[146:149]\n"
    "ds_read_b64_tr_b16 v[70:71], v232 offset:8192\n"
    "ds_read_b64_tr_b16 v[72:73], v232 offset:9216\n"
    "v_mfma_f32_16x16x32_bf16 v[150:153], v[6:9], v[18:21], v[150:153]\n"
    "ds_read_b128 v[74:77], v223 offset:30720\n"
    "v_mfma_f32_16x16x32_bf16 v[154:157], v[2:5], v[22:25], v[154:157]\n"
    "ds_read_b64_tr_b16 v[82:83], v226 offset:16384\n"
    "ds_read_b64_tr_b16 v[84:85], v226 offset:17408\n"
    "v_mfma_f32_16x16x32_bf16 v[158:161], v[6:9], v[22:25], v[158:161]\n"
    "ds_read_b64_tr_b16 v[86:87], v228 offset:16384\n"
    "ds_read_b64_tr_b16 v[88:89], v228 offset:17408\n"
    "v_mfma_f32_16x16x32_bf16 v[130:133], v[26:29], v[34:37], v[130:133]\n"
    "s_waitcnt lgkmcnt(14)\n"
    "ds_read_b128 v[78:81], v221 offset:31232\n"
    "s_waitcnt lgkmcnt(14)\n"
    "ds_read_b64_tr_b16 v[90:91], v230 offset:16384\n"
    "v_mfma_f32_16x16x32_bf16 v[134:137], v[30:33], v[34:37], v[134:137]\n"
    "s_waitcnt lgkmcnt(14)\n"
    "ds_read_b64_tr_b16 v[92:93], v230 offset:17408\n"
    "s_waitcnt lgkmcnt(14)\n"
    "ds_read_b64_tr_b16 v[94:95], v232 offset:16384\n"
    "v_mfma_f32_16x16x32_bf16 v[138:141], v[26:29], v[38:41], v[138:141]\n"
    "s_waitcnt lgkmcnt(14)\n"
    "ds_read_b64_tr_b16 v[96:97], v232 offset:17408\n"
    "v_mfma_f32_16x16x32_bf16 v[142:145], v[30:33], v[38:41], v[142:145]\n"
    "v_mfma_f32_16x16x32_bf16 v[146:149], v[26:29], v[42:45], v[146:149]\n"
    "v_mfma_f32_16x16x32_bf16 v[150:153], v[30:33], v[42:45], v[150:153]\n"
    "v_mfma_f32_16x16x32_bf16 v[154:157], v[26:29], v[46:49], v[154:157]\n"
    "v_mfma_f32_16x16x32_bf16 v[158:161], v[30:33], v[46:49], v[158:161]\n"
    "s_waitcnt lgkmcnt(0)\n"
    "s_barrier\n"
    "v_mfma_f32_16x16x32_bf16 v[130:133], v[50:53], v[58:61], v[130:133]\n"
    "ds_read_b128 v[2:5], v221 offset:40960\n"
    "ds_read_b64_tr_b16 v[10:11], v225 offset:16384\n"
    "v_mfma_f32_16x16x32_bf16 v[134:137], v[54:57], v[58:61], v[134:137]\n"
    "ds_read_b64_tr_b16 v[12:13], v225 offset:17408\n"
    "ds_read_b64_tr_b16 v[14:15], v227 offset:16384\n"
    "v_mfma_f32_16x16x32_bf16 v[138:141], v[50:53], v[62:65], v[138:141]\n"
    "ds_read_b64_tr_b16 v[16:17], v227 offset:17408\n"
    "ds_read_b128 v[6:9], v223 offset:41472\n"
    "v_mfma_f32_16x16x32_bf16 v[142:145], v[54:57], v[62:65], v[142:145]\n"
    "ds_read_b64_tr_b16 v[18:19], v229 offset:16384\n"
    "ds_read_b64_tr_b16 v[20:21], v229 offset:17408\n"
    "v_mfma_f32_16x16x32_bf16 v[146:149], v[50:53], v[66:69], v[146:149]\n"
    "ds_read_b64_tr_b16 v[22:23], v231 offset:16384\n"
    "ds_read_b64_tr_b16 v[24:25], v231 offset:17408\n"
    "v_mfma_f32_16x16x32_bf16 v[150:153], v[54:57], v[66:69], v[150:153]\n"
    "ds_read_b128 v[26:29], v223 offset:40960\n"
    "v_mfma_f32_16x16x32_bf16 v[154:157], v[50:53], v[70:73], v[154:157]\n"
    "ds_read_b64_tr_b16 v[34:35], v225 offset:24576\n"
    "ds_read_b64_tr_b16 v[36:37], v225 offset:25600\n"
    "v_mfma_f32_16x16x32_bf16 v[158:161], v[54:57], v[70:73], v[158:161]\n"
    "ds_read_b64_tr_b16 v[38:39], v227 offset:24576\n"
    "ds_read_b64_tr_b16 v[40:41], v227 offset:25600\n"
    "v_mfma_f32_16x16x32_bf16 v[130:133], v[74:77], v[82:85], v[130:133]\n"
    "s_waitcnt lgkmcnt(14)\n"
    "ds_read_b128 v[30:33], v221 offset:41472\n"
    "s_waitcnt lgkmcnt(14)\n"
    "ds_read_b64_tr_b16 v[42:43], v229 offset:24576\n"
    "v_mfma_f32_16x16x32_bf16 v[134:137], v[78:81], v[82:85], v[134:137]\n"
    "s_waitcnt lgkmcnt(14)\n"
    "ds_read_b64_tr_b16 v[44:45], v229 offset:25600\n"
    "s_waitcnt lgkmcnt(14)\n"
    "ds_read_b64_tr_b16 v[46:47], v231 offset:24576\n"
    "v_mfma_f32_16x16x32_bf16 v[138:141], v[74:77], v[86:89], v[138:141]\n"
    "s_waitcnt lgkmcnt(14)\n"
    "ds_read_b64_tr_b16 v[48:49], v231 offset:25600\n"
    "v_mfma_f32_16x16x32_bf16 v[142:145], v[78:81], v[86:89], v[142:145]\n"
    "v_mfma_f32_16x16x32_bf16 v[146:149], v[74:77], v[90:93], v[146:149]\n"
    "v_mfma_f32_16x16x32_bf16 v[150:153], v[78:81], v[90:93], v[150:153]\n"
    "v_mfma_f32_16x16x32_bf16 v[154:157], v[74:77], v[94:97], v[154:157]\n"
    "v_mfma_f32_16x16x32_bf16 v[158:161], v[78:81], v[94:97], v[158:161]\n"
    "s_waitcnt lgkmcnt(0)\n"
    "s_barrier\n"
    "v_mfma_f32_16x16x32_bf16 v[130:133], v[2:5], v[10:13], v[130:133]\n"
    "ds_read_b128 v[50:53], v221 offset:51200\n"
    "ds_read_b64_tr_b16 v[58:59], v225 offset:32768\n"
    "v_mfma_f32_16x16x32_bf16 v[134:137], v[6:9], v[10:13], v[134:137]\n"
    "ds_read_b64_tr_b16 v[60:61], v225 offset:33792\n"
    "ds_read_b64_tr_b16 v[62:63], v227 offset:32768\n"
    "v_mfma_f32_16x16x32_bf16 v[138:141], v[2:5], v[14:17], v[138:141]\n"
    "ds_read_b64_tr_b16 v[64:65], v227 offset:33792\n"
    "ds_read_b128 v[54:57], v223 offset:51712\n"
    "v_mfma_f32_16x16x32_bf16 v[142:145], v[6:9], v[14:17], v[142:145]\n"
    "ds_read_b64_tr_b16 v[66:67], v229 offset:32768\n"
    "ds_read_b64_tr_b16 v[68:69], v229 offset:33792\n"
    "v_mfma_f32_16x16x32_bf16 v[146:149], v[2:5], v[18:21], v[146:149]\n"
    "ds_read_b64_tr_b16 v[70:71], v231 offset:32768\n"
    "ds_read_b64_tr_b16 v[72:73], v231 offset:33792\n"
    "v_mfma_f32_16x16x32_bf16 v[150:153], v[6:9], v[18:21], v[150:153]\n"
    "ds_read_b128 v[74:77], v223 offset:51200\n"
    "v_mfma_f32_16x16x32_bf16 v[154:157], v[2:5], v[22:25], v[154:157]\n"
    "ds_read_b64_tr_b16 v[82:83], v225 offset:40960\n"
    "ds_read_b64_tr_b16 v[84:85], v225 offset:41984\n"
    "v_mfma_f32_16x16x32_bf16 v[158:161], v[6:9], v[22:25], v[158:161]\n"
    "ds_read_b64_tr_b16 v[86:87], v227 offset:40960\n"
    "ds_read_b64_tr_b16 v[88:89], v227 offset:41984\n"
    "v_mfma_f32_16x16x32_bf16 v[130:133], v[26:29], v[34:37], v[130:133]\n"
    "s_waitcnt lgkmcnt(14)\n"
    "ds_read_b128 v[78:81], v221 offset:51712\n"
    "s_waitcnt lgkmcnt(14)\n"
    "ds_read_b64_tr_b16 v[90:91], v229 offset:40960\n"
    "v_mfma_f32_16x16x32_bf16 v[134:137], v[30:33], v[34:37], v[134:137]\n"
    "s_waitcnt lgkmcnt(14)\n"
    "ds_read_b64_tr_b16 v[92:93], v229 offset:41984\n"
    "s_waitcnt lgkmcnt(14)\n"
    "ds_read_b64_tr_b16 v[94:95], v231 offset:40960\n"
    "v_mfma_f32_16x16x32_bf16 v[138:141], v[26:29], v[38:41], v[138:141]\n"
    "s_waitcnt lgkmcnt(14)\n"
    "ds_read_b64_tr_b16 v[96:97], v231 offset:41984\n"
    "v_mfma_f32_16x16x32_bf16 v[142:145], v[30:33], v[38:41], v[142:145]\n"
    "v_mfma_f32_16x16x32_bf16 v[146:149], v[26:29], v[42:45], v[146:149]\n"
    "v_mfma_f32_16x16x32_bf16 v[150:153], v[30:33], v[42:45], v[150:153]\n"
    "v_mfma_f32_16x16x32_bf16 v[154:157], v[26:29], v[46:49], v[154:157]\n"
    "v_mfma_f32_16x16x32_bf16 v[158:161], v[30:33], v[46:49], v[158:161]\n"
    "s_waitcnt lgkmcnt(0)\n"
    "s_barrier\n"
    "v_mfma_f32_16x16x32_bf16 v[130:133], v[50:53], v[58:61], v[130:133]\n"
    "ds_read_b128 v[2:5], v221 offset:61440\n"
    "ds_read_b64_tr_b16 v[10:11], v225 offset:49152\n"
    "v_mfma_f32_16x16x32_bf16 v[134:137], v[54:57], v[58:61], v[134:137]\n"
    "ds_read_b64_tr_b16 v[12:13], v225 offset:50176\n"
    "ds_read_b64_tr_b16 v[14:15], v227 offset:49152\n"
    "v_mfma_f32_16x16x32_bf16 v[138:141], v[50:53], v[62:65], v[138:141]\n"
    "ds_read_b64_tr_b16 v[16:17], v227 offset:50176\n"
    "ds_read_b128 v[6:9], v223 offset:61952\n"
    "v_mfma_f32_16x16x32_bf16 v[142:145], v[54:57], v[62:65], v[142:145]\n"
    "ds_read_b64_tr_b16 v[18:19], v229 offset:49152\n"
    "ds_read_b64_tr_b16 v[20:21], v229 offset:50176\n"
    "v_mfma_f32_16x16x32_bf16 v[146:149], v[50:53], v[66:69], v[146:149]\n"
    "ds_read_b64_tr_b16 v[22:23], v231 offset:49152\n"
    "ds_read_b64_tr_b16 v[24:25], v231 offset:50176\n"
    "v_mfma_f32_16x16x32_bf16 v[150:153], v[54:57], v[66:69], v[150:153]\n"
    "ds_read_b128 v[26:29], v223 offset:61440\n"
    "v_mfma_f32_16x16x32_bf16 v[154:157], v[50:53], v[70:73], v[154:157]\n"
    "ds_read_b64_tr_b16 v[34:35], v225 offset:57344\n"
    "ds_read_b64_tr_b16 v[36:37], v225 offset:58368\n"
    "v_mfma_f32_16x16x32_bf16 v[158:161], v[54:57], v[70:73], v[158:161]\n"
    "ds_read_b64_tr_b16 v[38:39], v227 offset:57344\n"
    "ds_read_b64_tr_b16 v[40:41], v227 offset:58368\n"
    "v_mfma_f32_16x16x32_bf16 v[130:133], v[74:77], v[82:85], v[130:133]\n"
    "s_waitcnt lgkmcnt(14)\n"
    "ds_read_b128 v[30:33], v221 offset:61952\n"
    "s_waitcnt lgkmcnt(14)\n"
    "ds_read_b64_tr_b16 v[42:43], v229 offset:57344\n"
    "v_mfma_f32_16x16x32_bf16 v[134:137], v[78:81], v[82:85], v[134:137]\n"
    "s_waitcnt lgkmcnt(14)\n"
    "ds_read_b64_tr_b16 v[44:45], v229 offset:58368\n"
    "s_waitcnt lgkmcnt(14)\n"
    "ds_read_b64_tr_b16 v[46:47], v231 offset:57344\n"
    "v_mfma_f32_16x16x32_bf16 v[138:141], v[74:77], v[86:89], v[138:141]\n"
    "s_waitcnt lgkmcnt(14)\n"
    "ds_read_b64_tr_b16 v[48:49], v231 offset:58368\n"
    "v_mfma_f32_16x16x32_bf16 v[142:145], v[78:81], v[86:89], v[142:145]\n"
    "v_mfma_f32_16x16x32_bf16 v[146:149], v[74:77], v[90:93], v[146:149]\n"
    "v_mfma_f32_16x16x32_bf16 v[150:153], v[78:81], v[90:93], v[150:153]\n"
    "v_mfma_f32_16x16x32_bf16 v[154:157], v[74:77], v[94:97], v[154:157]\n"
    "v_mfma_f32_16x16x32_bf16 v[158:161], v[78:81], v[94:97], v[158:161]\n"
    "s_waitcnt lgkmcnt(0)\n"
    "s_barrier\n"
    "v_mfma_f32_16x16x32_bf16 v[130:133], v[2:5], v[10:13], v[130:133]\n"
    "ds_read_b128 v[50:53], v222 offset:6144\n"
    "ds_read_b64_tr_b16 v[58:59], v226 offset:0\n"
    "v_mfma_f32_16x16x32_bf16 v[134:137], v[6:9], v[10:13], v[134:137]\n"
    "ds_read_b64_tr_b16 v[60:61], v226 offset:1024\n"
    "ds_read_b64_tr_b16 v[62:63], v228 offset:0\n"
    "v_mfma_f32_16x16x32_bf16 v[138:141], v[2:5], v[14:17], v[138:141]\n"
    "ds_read_b64_tr_b16 v[64:65], v228 offset:1024\n"
    "ds_read_b128 v[54:57], v224 offset:6656\n"
    "v_mfma_f32_16x16x32_bf16 v[142:145], v[6:9], v[14:17], v[142:145]\n"
    "ds_read_b64_tr_b16 v[66:67], v230 offset:0\n"
    "ds_read_b64_tr_b16 v[68:69], v230 offset:1024\n"
    "v_mfma_f32_16x16x32_bf16 v[146:149], v[2:5], v[18:21], v[146:149]\n"
    "ds_read_b64_tr_b16 v[70:71], v232 offset:0\n"
    "ds_read_b64_tr_b16 v[72:73], v232 offset:1024\n"
    "v_mfma_f32_16x16x32_bf16 v[150:153], v[6:9], v[18:21], v[150:153]\n"
    "ds_read_b128 v[74:77], v224 offset:6144\n"
    "v_mfma_f32_16x16x32_bf16 v[154:157], v[2:5], v[22:25], v[154:157]\n"
    "ds_read_b64_tr_b16 v[82:83], v226 offset:8192\n"
    "ds_read_b64_tr_b16 v[84:85], v226 offset:9216\n"
    "v_mfma_f32_16x16x32_bf16 v[158:161], v[6:9], v[22:25], v[158:161]\n"
    "ds_read_b64_tr_b16 v[86:87], v228 offset:8192\n"
    "ds_read_b64_tr_b16 v[88:89], v228 offset:9216\n"
    "v_mfma_f32_16x16x32_bf16 v[130:133], v[26:29], v[34:37], v[130:133]\n"
    "s_waitcnt lgkmcnt(14)\n"
    "ds_read_b128 v[78:81], v222 offset:6656\n"
    "s_waitcnt lgkmcnt(14)\n"
    "ds_read_b64_tr_b16 v[90:91], v230 offset:8192\n"
    "v_mfma_f32_16x16x32_bf16 v[134:137], v[30:33], v[34:37], v[134:137]\n"
    "s_waitcnt lgkmcnt(14)\n"
    "ds_read_b64_tr_b16 v[92:93], v230 offset:9216\n"
    "s_waitcnt lgkmcnt(14)\n"
    "ds_read_b64_tr_b16 v[94:95], v232 offset:8192\n"
    "v_mfma_f32_16x16x32_bf16 v[138:141], v[26:29], v[38:41], v[138:141]\n"
    "s_waitcnt lgkmcnt(14)\n"
    "ds_read_b64_tr_b16 v[96:97], v232 offset:9216\n"
    "v_mfma_f32_16x16x32_bf16 v[142:145], v[30:33], v[38:41], v[142:145]\n"
    "v_mfma_f32_16x16x32_bf16 v[146:149], v[26:29], v[42:45], v[146:149]\n"
    "v_mfma_f32_16x16x32_bf16 v[150:153], v[30:33], v[42:45], v[150:153]\n"
    "v_mfma_f32_16x16x32_bf16 v[154:157], v[26:29], v[46:49], v[154:157]\n"
    "v_mfma_f32_16x16x32_bf16 v[158:161], v[30:33], v[46:49], v[158:161]\n"
    "s_waitcnt lgkmcnt(0)\n"
    "s_barrier\n"
    "v_mfma_f32_16x16x32_bf16 v[130:133], v[50:53], v[58:61], v[130:133]\n"
    "ds_read_b128 v[2:5], v209 offset:0\n"
    "ds_read_b64_tr_b16 v[10:11], v226 offset:16384\n"
    "v_mfma_f32_16x16x32_bf16 v[134:137], v[54:57], v[58:61], v[134:137]\n"
    "ds_read_b64_tr_b16 v[12:13], v226 offset:17408\n"
    "ds_read_b64_tr_b16 v[14:15], v228 offset:16384\n"
    "v_mfma_f32_16x16x32_bf16 v[138:141], v[50:53], v[62:65], v[138:141]\n"
    "ds_read_b64_tr_b16 v[16:17], v228 offset:17408\n"
    "ds_read_b128 v[6:9], v218 offset:2048\n"
    "v_mfma_f32_16x16x32_bf16 v[142:145], v[54:57], v[62:65], v[142:145]\n"
    "ds_read_b64_tr_b16 v[18:19], v230 offset:16384\n"
    "ds_read_b64_tr_b16 v[20:21], v230 offset:17408\n"
    "v_mfma_f32_16x16x32_bf16 v[146:149], v[50:53], v[66:69], v[146:149]\n"
    "ds_read_b64_tr_b16 v[22:23], v232 offset:16384\n"
    "ds_read_b64_tr_b16 v[24:25], v232 offset:17408\n"
    "v_mfma_f32_16x16x32_bf16 v[150:153], v[54:57], v[66:69], v[150:153]\n"
    "ds_read_b128 v[26:29], v218 offset:0\n"
    "v_mfma_f32_16x16x32_bf16 v[154:157], v[50:53], v[70:73], v[154:157]\n"
    "ds_read_b64_tr_b16 v[34:35], v225 offset:16384\n"
    "ds_read_b64_tr_b16 v[36:37], v225 offset:17408\n"
    "v_mfma_f32_16x16x32_bf16 v[158:161], v[54:57], v[70:73], v[158:161]\n"
    "ds_read_b64_tr_b16 v[38:39], v227 offset:16384\n"
    "ds_read_b64_tr_b16 v[40:41], v227 offset:17408\n"
    "v_mfma_f32_16x16x32_bf16 v[130:133], v[74:77], v[82:85], v[130:133]\n"
    "s_waitcnt lgkmcnt(14)\n"
    "ds_read_b128 v[30:33], v209 offset:2048\n"
    "s_waitcnt lgkmcnt(14)\n"
    "ds_read_b64_tr_b16 v[42:43], v229 offset:16384\n"
    "v_mfma_f32_16x16x32_bf16 v[134:137], v[78:81], v[82:85], v[134:137]\n"
    "s_waitcnt lgkmcnt(14)\n"
    "ds_read_b64_tr_b16 v[44:45], v229 offset:17408\n"
    "s_waitcnt lgkmcnt(14)\n"
    "ds_read_b64_tr_b16 v[46:47], v231 offset:16384\n"
    "v_mfma_f32_16x16x32_bf16 v[138:141], v[74:77], v[86:89], v[138:141]\n"
    "s_waitcnt lgkmcnt(14)\n"
    "ds_read_b64_tr_b16 v[48:49], v231 offset:17408\n"
    "v_mfma_f32_16x16x32_bf16 v[142:145], v[78:81], v[86:89], v[142:145]\n"
    "v_mfma_f32_16x16x32_bf16 v[146:149], v[74:77], v[90:93], v[146:149]\n"
    "v_mfma_f32_16x16x32_bf16 v[150:153], v[78:81], v[90:93], v[150:153]\n"
    "v_mfma_f32_16x16x32_bf16 v[154:157], v[74:77], v[94:97], v[154:157]\n"
    "v_mfma_f32_16x16x32_bf16 v[158:161], v[78:81], v[94:97], v[158:161]\n"
    "s_waitcnt lgkmcnt(0)\n"
    "s_barrier\n"
    "s_waitcnt vmcnt(0)\n"
    "v_readfirstlane_b32 s48, v214\n"
    "v_readfirstlane_b32 s49, v215\n"
    "s_add_u32 s48, s48, 0x7f4a7c15\n"
    "s_addc_u32 s49, s49, 0x9e3779b9\n"
    "v_mov_b32 v187, s48\n"
    "v_mov_b32 v189, s49\n"
    "v_mov_b32 v191, s48\n"
    "v_mov_b32 v193, s49\n"
    "v_mov_b32 v214, s48\n"
    "v_mov_b32 v215, s49\n"
    "v_pk_mul_f32 v[50:51], v[130:131], v[130:131]\n"
    "v_pk_mul_f32 v[52:53], v[132:133], v[132:133]\n"
    "v_pk_mul_f32 v[54:55], v[134:135], v[134:135]\n"
    "v_pk_mul_f32 v[56:57], v[136:137], v[136:137]\n"
    "v_pk_mul_f32 v[58:59], v[138:139], v[138:139]\n"
    "v_pk_mul_f32 v[60:61], v[140:141], v[140:141]\n"
    "v_pk_mul_f32 v[62:63], v[142:143], v[142:143]\n"
    "v_pk_mul_f32 v[64:65], v[144:145], v[144:145]\n"
    "v_pk_mul_f32 v[66:67], v[146:147], v[146:147]\n"
    "v_pk_mul_f32 v[68:69], v[148:149], v[148:149]\n"
    "v_pk_mul_f32 v[70:71], v[150:151], v[150:151]\n"
    "v_pk_mul_f32 v[72:73], v[152:153], v[152:153]\n"
    "v_pk_mul_f32 v[74:75], v[154:155], v[154:155]\n"
    "v_pk_mul_f32 v[76:77], v[156:157], v[156:157]\n"
    "v_pk_mul_f32 v[78:79], v[158:159], v[158:159]\n"
    "v_pk_mul_f32 v[80:81], v[160:161], v[160:161]\n"
    "v_pk_fma_f32 v[50:51], v[50:51], v[240:241], v[238:239]\n"
    "v_pk_fma_f32 v[52:53], v[52:53], v[240:241], v[238:239]\n"
    "v_pk_fma_f32 v[54:55], v[54:55], v[240:241], v[238:239]\n"
    "v_pk_fma_f32 v[56:57], v[56:57], v[240:241], v[238:239]\n"
    "v_pk_fma_f32 v[58:59], v[58:59], v[240:241], v[238:239]\n"
    "v_pk_fma_f32 v[60:61], v[60:61], v[240:241], v[238:239]\n"
    "v_pk_fma_f32 v[62:63], v[62:63], v[240:241], v[238:239]\n"
    "v_pk_fma_f32 v[64:65], v[64:65], v[240:241], v[238:239]\n"
    "v_pk_fma_f32 v[66:67], v[66:67], v[240:241], v[238:239]\n"
    "v_pk_fma_f32 v[68:69], v[68:69], v[240:241], v[238:239]\n"
    "v_pk_fma_f32 v[70:71], v[70:71], v[240:241], v[238:239]\n"
    "v_pk_fma_f32 v[72:73], v[72:73], v[240:241], v[238:239]\n"
    "v_pk_fma_f32 v[74:75], v[74:75], v[240:241], v[238:239]\n"
    "v_pk_fma_f32 v[76:77], v[76:77], v[240:241], v[238:239]\n"
    "v_pk_fma_f32 v[78:79], v[78:79], v[240:241], v[238:239]\n"
    "v_pk_fma_f32 v[80:81], v[80:81], v[240:241], v[238:239]\n"
    "v_pk_mul_f32 v[50:51], v[50:51], v[130:131]\n"
    "v_pk_mul_f32 v[52:53], v[52:53], v[132:133]\n"
    "v_pk_mul_f32 v[54:55], v[54:55], v[134:135]\n"
    "v_pk_mul_f32 v[56:57], v[56:57], v[136:137]\n"
    "v_pk_mul_f32 v[58:59], v[58:59], v[138:139]\n"
    "v_pk_mul_f32 v[60:61], v[60:61], v[140:141]\n"
    "v_pk_mul_f32 v[62:63], v[62:63], v[142:143]\n"
    "v_pk_mul_f32 v[64:65], v[64:65], v[144:145]\n"
    "v_pk_mul_f32 v[66:67], v[66:67], v[146:147]\n"
    "v_pk_mul_f32 v[68:69], v[68:69], v[148:149]\n"
    "v_pk_mul_f32 v[70:71], v[70:71], v[150:151]\n"
    "v_pk_mul_f32 v[72:73], v[72:73], v[152:153]\n"
    "v_pk_mul_f32 v[74:75], v[74:75], v[154:155]\n"
    "v_pk_mul_f32 v[76:77], v[76:77], v[156:157]\n"
    "v_pk_mul_f32 v[78:79], v[78:79], v[158:159]\n"
    "v_pk_mul_f32 v[80:81], v[80:81], v[160:161]\n"
    "v_exp_f32 v50, v50\n"
    "v_exp_f32 v51, v51\n"
    "v_exp_f32 v52, v52\n"
    "v_exp_f32 v53, v53\n"
    "v_exp_f32 v54, v54\n"
    "v_exp_f32 v55, v55\n"
    "v_exp_f32 v56, v56\n"
    "v_exp_f32 v57, v57\n"
    "v_exp_f32 v58, v58\n"
    "v_exp_f32 v59, v59\n"
    "v_exp_f32 v60, v60\n"
    "v_exp_f32 v61, v61\n"
    "v_exp_f32 v62, v62\n"
    "v_exp_f32 v63, v63\n"
    "v_exp_f32 v64, v64\n"
    "v_exp_f32 v65, v65\n"
    "v_exp_f32 v66, v66\n"
    "v_exp_f32 v67, v67\n"
    "v_exp_f32 v68, v68\n"
    "v_exp_f32 v69, v69\n"
    "v_exp_f32 v70, v70\n"
    "v_exp_f32 v71, v71\n"
    "v_exp_f32 v72, v72\n"
    "v_exp_f32 v73, v73\n"
    "v_exp_f32 v74, v74\n"
    "v_exp_f32 v75, v75\n"
    "v_exp_f32 v76, v76\n"
    "v_exp_f32 v77, v77\n"
    "v_exp_f32 v78, v78\n"
    "v_exp_f32 v79, v79\n"
    "v_exp_f32 v80, v80\n"
    "v_exp_f32 v81, v81\n"
    "v_pk_add_f32 v[50:51], v[50:51], 1.0 op_sel_hi:[1,0]\n"
    "v_pk_add_f32 v[52:53], v[52:53], 1.0 op_sel_hi:[1,0]\n"
    "v_pk_add_f32 v[54:55], v[54:55], 1.0 op_sel_hi:[1,0]\n"
    "v_pk_add_f32 v[56:57], v[56:57], 1.0 op_sel_hi:[1,0]\n"
    "v_pk_add_f32 v[58:59], v[58:59], 1.0 op_sel_hi:[1,0]\n"
    "v_pk_add_f32 v[60:61], v[60:61], 1.0 op_sel_hi:[1,0]\n"
    "v_pk_add_f32 v[62:63], v[62:63], 1.0 op_sel_hi:[1,0]\n"
    "v_pk_add_f32 v[64:65], v[64:65], 1.0 op_sel_hi:[1,0]\n"
    "v_pk_add_f32 v[66:67], v[66:67], 1.0 op_sel_hi:[1,0]\n"
    "v_pk_add_f32 v[68:69], v[68:69], 1.0 op_sel_hi:[1,0]\n"
    "v_pk_add_f32 v[70:71], v[70:71], 1.0 op_sel_hi:[1,0]\n"
    "v_pk_add_f32 v[72:73], v[72:73], 1.0 op_sel_hi:[1,0]\n"
    "v_pk_add_f32 v[74:75], v[74:75], 1.0 op_sel_hi:[1,0]\n"
    "v_pk_add_f32 v[76:77], v[76:77], 1.0 op_sel_hi:[1,0]\n"
    "v_pk_add_f32 v[78:79], v[78:79], 1.0 op_sel_hi:[1,0]\n"
    "v_pk_add_f32 v[80:81], v[80:81], 1.0 op_sel_hi:[1,0]\n"
    "v_rcp_f32 v50, v50\n"
    "v_rcp_f32 v51, v51\n"
    "v_rcp_f32 v52, v52\n"
    "v_rcp_f32 v53, v53\n"
    "v_rcp_f32 v54, v54\n"
    "v_rcp_f32 v55, v55\n"
    "v_rcp_f32 v56, v56\n"
    "v_rcp_f32 v57, v57\n"
    "v_rcp_f32 v58, v58\n"
    "v_rcp_f32 v59, v59\n"
    "v_rcp_f32 v60, v60\n"
    "v_rcp_f32 v61, v61\n"
    "v_rcp_f32 v62, v62\n"
    "v_rcp_f32 v63, v63\n"
    "v_rcp_f32 v64, v64\n"
    "v_rcp_f32 v65, v65\n"
    "v_rcp_f32 v66, v66\n"
    "v_rcp_f32 v67, v67\n"
    "v_rcp_f32 v68, v68\n"
    "v_rcp_f32 v69, v69\n"
    "v_rcp_f32 v70, v70\n"
    "v_rcp_f32 v71, v71\n"
    "v_rcp_f32 v72, v72\n"
    "v_rcp_f32 v73, v73\n"
    "v_rcp_f32 v74, v74\n"
    "v_rcp_f32 v75, v75\n"
    "v_rcp_f32 v76, v76\n"
    "v_rcp_f32 v77, v77\n"
    "v_rcp_f32 v78, v78\n"
    "v_rcp_f32 v79, v79\n"
    "v_rcp_f32 v80, v80\n"
    "v_rcp_f32 v81, v81\n"
    "v_pk_mul_f32 v[74:75], v[74:75], s[96:97]\n"
    "v_pk_mul_f32 v[76:77], v[76:77], s[96:97]\n"
    "v_pk_mul_f32 v[78:79], v[78:79], s[96:97]\n"
    "v_pk_mul_f32 v[80:81], v[80:81], s[96:97]\n"
    "v_pk_mul_f32 v[130:131], v[130:131], v[50:51]\n"
    "v_pk_mul_f32 v[132:133], v[132:133], v[52:53]\n"
    "v_pk_mul_f32 v[134:135], v[134:135], v[54:55]\n"
    "v_pk_mul_f32 v[136:137], v[136:137], v[56:57]\n"
    "v_pk_mul_f32 v[138:139], v[138:139], v[58:59]\n"
    "v_pk_mul_f32 v[140:141], v[140:141], v[60:61]\n"
    "v_pk_mul_f32 v[142:143], v[142:143], v[62:63]\n"
    "v_pk_mul_f32 v[144:145], v[144:145], v[64:65]\n"
    "v_pk_mul_f32 v[146:147], v[146:147], v[66:67]\n"
    "v_pk_mul_f32 v[148:149], v[148:149], v[68:69]\n"
    "v_pk_mul_f32 v[150:151], v[150:151], v[70:71]\n"
    "v_pk_mul_f32 v[152:153], v[152:153], v[72:73]\n"
    "v_pk_mul_f32 v[154:155], v[154:155], v[74:75]\n"
    "v_pk_mul_f32 v[156:157], v[156:157], v[76:77]\n"
    "v_pk_mul_f32 v[158:159], v[158:159], v[78:79]\n"
    "v_pk_mul_f32 v[160:161], v[160:161], v[80:81]\n"
    "v_pk_mul_f32 v[178:179], v[130:131], v[130:131]\n"
    "v_mov_b32 v170, v130\n"
    "v_mov_b32 v171, v131\n"
    "v_pk_mul_f32 v[180:181], v[132:133], v[132:133]\n"
    "v_mov_b32 v172, v132\n"
    "v_mov_b32 v173, v133\n"
    "v_pk_mul_f32 v[182:183], v[134:135], v[134:135]\n"
    "v_mov_b32 v174, v134\n"
    "v_mov_b32 v175, v135\n"
    "v_pk_mul_f32 v[184:185], v[136:137], v[136:137]\n"
    "v_mov_b32 v176, v136\n"
    "v_mov_b32 v177, v137\n"
    "v_pk_add_f32 v[170:171], v[170:171], v[138:139]\n"
    "v_pk_fma_f32 v[178:179], v[138:139], v[138:139], v[178:179]\n"
    "v_pk_add_f32 v[172:173], v[172:173], v[140:141]\n"
    "v_pk_fma_f32 v[180:181], v[140:141], v[140:141], v[180:181]\n"
    "v_pk_add_f32 v[174:175], v[174:175], v[142:143]\n"
    "v_pk_fma_f32 v[182:183], v[142:143], v[142:143], v[182:183]\n"
    "v_pk_add_f32 v[176:177], v[176:177], v[144:145]\n"
    "v_pk_fma_f32 v[184:185], v[144:145], v[144:145], v[184:185]\n"
    "v_pk_add_f32 v[170:171], v[170:171], v[146:147]\n"
    "v_pk_fma_f32 v[178:179], v[146:147], v[146:147], v[178:179]\n"
    "v_pk_add_f32 v[172:173], v[172:173], v[148:149]\n"
    "v_pk_fma_f32 v[180:181], v[148:149], v[148:149], v[180:181]\n"
    "v_pk_add_f32 v[174:175], v[174:175], v[150:151]\n"
    "v_pk_fma_f32 v[182:183], v[150:151], v[150:151], v[182:183]\n"
    "v_pk_add_f32 v[176:177], v[176:177], v[152:153]\n"
    "v_pk_fma_f32 v[184:185], v[152:153], v[152:153], v[184:185]\n"
    "v_pk_add_f32 v[170:171], v[170:171], v[154:155]\n"
    "v_pk_fma_f32 v[178:179], v[154:155], v[154:155], v[178:179]\n"
    "v_pk_add_f32 v[172:173], v[172:173], v[156:157]\n"
    "v_pk_fma_f32 v[180:181], v[156:157], v[156:157], v[180:181]\n"
    "v_pk_add_f32 v[174:175], v[174:175], v[158:159]\n"
    "v_pk_fma_f32 v[182:183], v[158:159], v[158:159], v[182:183]\n"
    "v_pk_add_f32 v[176:177], v[176:177], v[160:161]\n"
    "v_pk_fma_f32 v[184:185], v[160:161], v[160:161], v[184:185]\n"
    "v_add_f32_dpp v50, v170, v170 row_ror:8 row_mask:0xf bank_mask:0x3\n"
    "v_add_f32_dpp v51, v171, v171 row_ror:8 row_mask:0xf bank_mask:0x3\n"
    "v_add_f32_dpp v52, v174, v174 row_ror:8 row_mask:0xf bank_mask:0x3\n"
    "v_add_f32_dpp v53, v175, v175 row_ror:8 row_mask:0xf bank_mask:0x3\n"
    "v_add_f32_dpp v54, v178, v178 row_ror:8 row_mask:0xf bank_mask:0x3\n"
    "v_add_f32_dpp v55, v179, v179 row_ror:8 row_mask:0xf bank_mask:0x3\n"
    "v_add_f32_dpp v56, v182, v182 row_ror:8 row_mask:0xf bank_mask:0x3\n"
    "v_add_f32_dpp v57, v183, v183 row_ror:8 row_mask:0xf bank_mask:0x3\n"
    "v_add_f32_dpp v50, v172, v172 row_ror:8 row_mask:0xf bank_mask:0xc\n"
    "v_add_f32_dpp v51, v173, v173 row_ror:8 row_mask:0xf bank_mask:0xc\n"
    "v_add_f32_dpp v52, v176, v176 row_ror:8 row_mask:0xf bank_mask:0xc\n"
    "v_add_f32_dpp v53, v177, v177 row_ror:8 row_mask:0xf bank_mask:0xc\n"
    "v_add_f32_dpp v54, v180, v180 row_ror:8 row_mask:0xf bank_mask:0xc\n"
    "v_add_f32_dpp v55, v181, v181 row_ror:8 row_mask:0xf bank_mask:0xc\n"
    "v_add_f32_dpp v56, v184, v184 row_ror:8 row_mask:0xf bank_mask:0xc\n"
    "v_add_f32_dpp v57, v185, v185 row_ror:8 row_mask:0xf bank_mask:0xc\n"
    "v_add_f32_dpp v186, v50, v50 row_half_mirror row_mask:0xf bank_mask:0x5\n"
    "v_add_f32_dpp v190, v52, v52 row_half_mirror row_mask:0xf bank_mask:0x5\n"
    "v_add_f32_dpp v188, v54, v54 row_half_mirror row_mask:0xf bank_mask:0x5\n"
    "v_add_f32_dpp v192, v56, v56 row_half_mirror row_mask:0xf bank_mask:0x5\n"
    "v_add_f32_dpp v186, v51, v51 row_half_mirror row_mask:0xf bank_mask:0xa\n"
    "v_add_f32_dpp v190, v53, v53 row_half_mirror row_mask:0xf bank_mask:0xa\n"
    "v_add_f32_dpp v188, v55, v55 row_half_mirror row_mask:0xf bank_mask:0xa\n"
    "v_add_f32_dpp v192, v57, v57 row_half_mirror row_mask:0xf bank_mask:0xa\n"
    "v_add_f32_dpp v186, v186, v186 quad_perm:[1,0,3,2] row_mask:0xf bank_mask:0xf\n"
    "v_add_f32_dpp v190, v190, v190 quad_perm:[1,0,3,2] row_mask:0xf bank_mask:0xf\n"
    "v_add_f32_dpp v188, v188, v188 quad_perm:[1,0,3,2] row_mask:0xf bank_mask:0xf\n"
    "v_add_f32_dpp v192, v192, v192 quad_perm:[1,0,3,2] row_mask:0xf bank_mask:0xf\n"
    "v_add_f32_dpp v186, v186, v186 quad_perm:[2,3,0,1] row_mask:0xf bank_mask:0xf\n"
    "v_add_f32_dpp v190, v190, v190 quad_perm:[2,3,0,1] row_mask:0xf bank_mask:0xf\n"
    "v_add_f32_dpp v188, v188, v188 quad_perm:[2,3,0,1] row_mask:0xf bank_mask:0xf\n"
    "v_add_f32_dpp v192, v192, v192 quad_perm:[2,3,0,1] row_mask:0xf bank_mask:0xf\n"
    "s_lshl_b32 s50, s46, 3\n"
    "s_add_u32 s50, s50, s47\n"
    "s_lshl_b32 s50, s50, 1\n"
    "s_add_u32 s50, s50, s55\n"
    "s_lshl_b32 s50, s50, 10\n"
    "s_add_u32 s50, s50, 0x10000\n"
    "s_mov_b32 exec_lo, 0x11111111\n"
    "s_mov_b32 exec_hi, 0x11111111\n"
    "buffer_store_dwordx4 v[186:189], v236, s[36:39], s50 offen offset:0 sc1\n"
    "buffer_store_dwordx4 v[190:193], v236, s[36:39], s50 offen offset:64 sc1\n"
    "s_mov_b64 exec, -1\n"
    "s_barrier\n"
    "v_mfma_f32_16x16x32_bf16 v[98:101], v[10:13], v[2:5], 0\n"
    "ds_read_b128 v[50:53], v209 offset:10240\n"
    "ds_read_b64_tr_b16 v[58:59], v225 offset:24576\n"
    "v_mfma_f32_16x16x32_bf16 v[102:105], v[10:13], v[6:9], 0\n"
    "ds_read_b64_tr_b16 v[60:61], v225 offset:25600\n"
    "ds_read_b64_tr_b16 v[62:63], v227 offset:24576\n"
    "v_mfma_f32_16x16x32_bf16 v[106:109], v[14:17], v[2:5], 0\n"
    "ds_read_b64_tr_b16 v[64:65], v227 offset:25600\n"
    "ds_read_b128 v[54:57], v218 offset:12288\n"
    "v_mfma_f32_16x16x32_bf16 v[110:113], v[14:17], v[6:9], 0\n"
    "ds_read_b64_tr_b16 v[66:67], v229 offset:24576\n"
    "ds_read_b64_tr_b16 v[68:69], v229 offset:25600\n"
    "v_mfma_f32_16x16x32_bf16 v[114:117], v[18:21], v[2:5], 0\n"
    "ds_read_b64_tr_b16 v[70:71], v231 offset:24576\n"
    "ds_read_b64_tr_b16 v[72:73], v231 offset:25600\n"
    "v_mfma_f32_16x16x32_bf16 v[118:121], v[18:21], v[6:9], 0\n"
    "ds_read_b128 v[74:77], v218 offset:10240\n"
    "s_lshl_b32 s51, s56, 12\n"
    "v_mfma_f32_16x16x32_bf16 v[122:125], v[22:25], v[2:5], 0\n"
    "ds_read_b64_tr_b16 v[82:83], v225 offset:32768\n"
    "ds_read_b64_tr_b16 v[84:85], v225 offset:33792\n"
    "v_mfma_f32_16x16x32_bf16 v[126:129], v[22:25], v[6:9], 0\n"
    "ds_read_b64_tr_b16 v[86:87], v227 offset:32768\n"
    "ds_read_b64_tr_b16 v[88:89], v227 offset:33792\n"
    "v_mfma_f32_16x16x32_bf16 v[98:101], v[34:37], v[26:29], v[98:101]\n"
    "s_waitcnt lgkmcnt(14)\n"
    "ds_read_b128 v[78:81], v209 offset:12288\n"
    "s_waitcnt lgkmcnt(14)\n"
    "ds_read_b64_tr_b16 v[90:91], v229 offset:32768\n"
    "v_mfma_f32_16x16x32_bf16 v[102:105], v[34:37], v[30:33], v[102:105]\n"
    "s_waitcnt lgkmcnt(14)\n"
    "ds_read_b64_tr_b16 v[92:93], v229 offset:33792\n"
    "s_waitcnt lgkmcnt(14)\n"
    "ds_read_b64_tr_b16 v[94:95], v231 offset:32768\n"
    "v_mfma_f32_16x16x32_bf16 v[106:109], v[38:41], v[26:29], v[106:109]\n"
    "s_waitcnt lgkmcnt(14)\n"
    "ds_read_b64_tr_b16 v[96:97], v231 offset:33792\n"
    "v_subrev_u32 v221, s51, v209\n"
    "v_mfma_f32_16x16x32_bf16 v[110:113], v[38:41], v[30:33], v[110:113]\n"
    "v_add_u32 v222, 0x10000, v221\n"
    "s_lshl_b32 s51, s56, 12\n"
    "v_mfma_f32_16x16x32_bf16 v[114:117], v[42:45], v[26:29], v[114:117]\n"
    "v_subrev_u32 v223, s51, v218\n"
    "v_add_u32 v224, 0x10000, v223\n"
    "v_mfma_f32_16x16x32_bf16 v[118:121], v[42:45], v[30:33], v[118:121]\n"
    "v_bfe_u32 v206, v0, 3, 2\n"
    "v_xor_b32 v206, s45, v206\n"
    "v_mfma_f32_16x16x32_bf16 v[122:125], v[46:49], v[26:29], v[122:125]\n"
    "v_lshlrev_b32 v207, 3, v205\n"
    "v_lshl_or_b32 v206, v206, 5, v207\n"
    "v_mfma_f32_16x16x32_bf16 v[126:129], v[46:49], v[30:33], v[126:129]\n"
    "v_lshl_or_b32 v206, v204, 7, v206\n"
    "s_movk_i32 s51, 3072\n"
    "s_waitcnt lgkmcnt(0)\n"
    "s_barrier\n"
    "v_mfma_f32_16x16x32_bf16 v[98:101], v[58:61], v[50:53], v[98:101]\n"
    "ds_read_b128 v[2:5], v209 offset:20480\n"
    "ds_read_b64_tr_b16 v[10:11], v225 offset:40960\n"
    "v_mfma_f32_16x16x32_bf16 v[102:105], v[58:61], v[54:57], v[102:105]\n"
    "ds_read_b64_tr_b16 v[12:13], v225 offset:41984\n"
    "ds_read_b64_tr_b16 v[14:15], v227 offset:40960\n"
    "v_mfma_f32_16x16x32_bf16 v[106:109], v[62:65], v[50:53], v[106:109]\n"
    "ds_read_b64_tr_b16 v[16:17], v227 offset:41984\n"
    "ds_read_b128 v[6:9], v218 offset:22528\n"
    "v_mfma_f32_16x16x32_bf16 v[110:113], v[62:65], v[54:57], v[110:113]\n"
    "ds_read_b64_tr_b16 v[18:19], v229 offset:40960\n"
    "ds_read_b64_tr_b16 v[20:21], v229 offset:41984\n"
    "v_mfma_f32_16x16x32_bf16 v[114:117], v[66:69], v[50:53], v[114:117]\n"
    "ds_read_b64_tr_b16 v[22:23], v231 offset:40960\n"
    "ds_read_b64_tr_b16 v[24:25], v231 offset:41984\n"
    "v_mfma_f32_16x16x32_bf16 v[118:121], v[66:69], v[54:57], v[118:121]\n"
    "ds_read_b128 v[26:29], v218 offset:20480\n"
    "v_mad_u32_u24 v208, v203, s51, v206\n"
    "v_mfma_f32_16x16x32_bf16 v[122:125], v[70:73], v[50:53], v[122:125]\n"
    "ds_read_b64_tr_b16 v[34:35], v225 offset:49152\n"
    "ds_read_b64_tr_b16 v[36:37], v225 offset:50176\n"
    "v_mfma_f32_16x16x32_bf16 v[126:129], v[70:73], v[54:57], v[126:129]\n"
    "ds_read_b64_tr_b16 v[38:39], v227 offset:49152\n"
    "ds_read_b64_tr_b16 v[40:41], v227 offset:50176\n"
    "v_mfma_f32_16x16x32_bf16 v[98:101], v[82:85], v[74:77], v[98:101]\n"
    "s_waitcnt lgkmcnt(14)\n"
    "ds_read_b128 v[30:33], v209 offset:22528\n"
    "s_waitcnt lgkmcnt(14)\n"
    "ds_read_b64_tr_b16 v[42:43], v229 offset:49152\n"
    "v_mfma_f32_16x16x32_bf16 v[102:105], v[82:85], v[78:81], v[102:105]\n"
    "s_waitcnt lgkmcnt(14)\n"
    "ds_read_b64_tr_b16 v[44:45], v229 offset:50176\n"
    "s_waitcnt lgkmcnt(14)\n"
    "ds_read_b64_tr_b16 v[46:47], v231 offset:49152\n"
    "v_mfma_f32_16x16x32_bf16 v[106:109], v[86:89], v[74:77], v[106:109]\n"
    "s_waitcnt lgkmcnt(14)\n"
    "ds_read_b64_tr_b16 v[48:49], v231 offset:50176\n"
    "v_add_u32 v237, 0x10000, v208\n"
    "v_mfma_f32_16x16x32_bf16 v[110:113], v[86:89], v[78:81], v[110:113]\n"
    "v_add_u32 v242, 0x20000, v208\n"
    "v_mfma_f32_16x16x32_bf16 v[114:117], v[90:93], v[74:77], v[114:117]\n"
    "v_mfma_f32_16x16x32_bf16 v[118:121], v[90:93], v[78:81], v[118:121]\n"
    "v_mfma_f32_16x16x32_bf16 v[122:125], v[94:97], v[74:77], v[122:125]\n"
    "v_mfma_f32_16x16x32_bf16 v[126:129], v[94:97], v[78:81], v[126:129]\n"
    "s_waitcnt lgkmcnt(0)\n"
    "s_barrier\n"
    "v_mfma_f32_16x16x32_bf16 v[98:101], v[10:13], v[2:5], v[98:101]\n"
    "ds_read_b128 v[50:53], v209 offset:30720\n"
    "ds_read_b64_tr_b16 v[58:59], v225 offset:57344\n"
    "v_mfma_f32_16x16x32_bf16 v[102:105], v[10:13], v[6:9], v[102:105]\n"
    "ds_read_b64_tr_b16 v[60:61], v225 offset:58368\n"
    "ds_read_b64_tr_b16 v[62:63], v227 offset:57344\n"
    "v_mfma_f32_16x16x32_bf16 v[106:109], v[14:17], v[2:5], v[106:109]\n"
    "ds_read_b64_tr_b16 v[64:65], v227 offset:58368\n"
    "ds_read_b128 v[54:57], v218 offset:32768\n"
    "v_mfma_f32_16x16x32_bf16 v[110:113], v[14:17], v[6:9], v[110:113]\n"
    "ds_read_b64_tr_b16 v[66:67], v229 offset:57344\n"
    "ds_read_b64_tr_b16 v[68:69], v229 offset:58368\n"
    "v_mfma_f32_16x16x32_bf16 v[114:117], v[18:21], v[2:5], v[114:117]\n"
    "ds_read_b64_tr_b16 v[70:71], v231 offset:57344\n"
    "ds_read_b64_tr_b16 v[72:73], v231 offset:58368\n"
    "v_mfma_f32_16x16x32_bf16 v[118:121], v[18:21], v[6:9], v[118:121]\n"
    "ds_read_b128 v[74:77], v218 offset:30720\n"
    "v_mfma_f32_16x16x32_bf16 v[122:125], v[22:25], v[2:5], v[122:125]\n"
    "ds_read_b64_tr_b16 v[82:83], v226 offset:0\n"
    "ds_read_b64_tr_b16 v[84:85], v226 offset:1024\n"
    "v_mfma_f32_16x16x32_bf16 v[126:129], v[22:25], v[6:9], v[126:129]\n"
    "ds_read_b64_tr_b16 v[86:87], v228 offset:0\n"
    "ds_read_b64_tr_b16 v[88:89], v228 offset:1024\n"
    "v_mfma_f32_16x16x32_bf16 v[98:101], v[34:37], v[26:29], v[98:101]\n"
    "s_waitcnt lgkmcnt(14)\n"
    "ds_read_b128 v[78:81], v209 offset:32768\n"
    "s_waitcnt lgkmcnt(14)\n"
    "ds_read_b64_tr_b16 v[90:91], v230 offset:0\n"
    "v_mfma_f32_16x16x32_bf16 v[102:105], v[34:37], v[30:33], v[102:105]\n"
    "s_waitcnt lgkmcnt(14)\n"
    "ds_read_b64_tr_b16 v[92:93], v230 offset:1024\n"
    "s_waitcnt lgkmcnt(14)\n"
    "ds_read_b64_tr_b16 v[94:95], v232 offset:0\n"
    "v_mfma_f32_16x16x32_bf16 v[106:109], v[38:41], v[26:29], v[106:109]\n"
    "s_waitcnt lgkmcnt(14)\n"
    "ds_read_b64_tr_b16 v[96:97], v232 offset:1024\n"
    "v_mfma_f32_16x16x32_bf16 v[110:113], v[38:41], v[30:33], v[110:113]\n"
    "v_mfma_f32_16x16x32_bf16 v[114:117], v[42:45], v[26:29], v[114:117]\n"
    "v_mfma_f32_16x16x32_bf16 v[118:121], v[42:45], v[30:33], v[118:121]\n"
    "v_mfma_f32_16x16x32_bf16 v[122:125], v[46:49], v[26:29], v[122:125]\n"
    "v_mfma_f32_16x16x32_bf16 v[126:129], v[46:49], v[30:33], v[126:129]\n"
    "s_waitcnt lgkmcnt(0)\n"
    "s_barrier\n"
    "v_mfma_f32_16x16x32_bf16 v[98:101], v[58:61], v[50:53], v[98:101]\n"
    "ds_read_b128 v[2:5], v209 offset:40960\n"
    "ds_read_b64_tr_b16 v[10:11], v226 offset:8192\n"
    "v_mfma_f32_16x16x32_bf16 v[102:105], v[58:61], v[54:57], v[102:105]\n"
    "ds_read_b64_tr_b16 v[12:13], v226 offset:9216\n"
    "ds_read_b64_tr_b16 v[14:15], v228 offset:8192\n"
    "v_mfma_f32_16x16x32_bf16 v[106:109], v[62:65], v[50:53], v[106:109]\n"
    "ds_read_b64_tr_b16 v[16:17], v228 offset:9216\n"
    "ds_read_b128 v[6:9], v218 offset:43008\n"
    "v_mfma_f32_16x16x32_bf16 v[110:113], v[62:65], v[54:57], v[110:113]\n"
    "ds_read_b64_tr_b16 v[18:19], v230 offset:8192\n"
    "ds_read_b64_tr_b16 v[20:21], v230 offset:9216\n"
    "v_mfma_f32_16x16x32_bf16 v[114:117], v[66:69], v[50:53], v[114:117]\n"
    "ds_read_b64_tr_b16 v[22:23], v232 offset:8192\n"
    "ds_read_b64_tr_b16 v[24:25], v232 offset:9216\n"
    "v_mfma_f32_16x16x32_bf16 v[118:121], v[66:69], v[54:57], v[118:121]\n"
    "ds_read_b128 v[26:29], v218 offset:40960\n"
    "v_mfma_f32_16x16x32_bf16 v[122:125], v[70:73], v[50:53], v[122:125]\n"
    "ds_read_b64_tr_b16 v[34:35], v226 offset:16384\n"
    "ds_read_b64_tr_b16 v[36:37], v226 offset:17408\n"
    "v_mfma_f32_16x16x32_bf16 v[126:129], v[70:73], v[54:57], v[126:129]\n"
    "ds_read_b64_tr_b16 v[38:39], v228 offset:16384\n"
    "ds_read_b64_tr_b16 v[40:41], v228 offset:17408\n"
    "v_mfma_f32_16x16x32_bf16 v[98:101], v[82:85], v[74:77], v[98:101]\n"
    "s_waitcnt lgkmcnt(14)\n"
    "ds_read_b128 v[30:33], v209 offset:43008\n"
    "s_waitcnt lgkmcnt(14)\n"
    "ds_read_b64_tr_b16 v[42:43], v230 offset:16384\n"
    "v_mfma_f32_16x16x32_bf16 v[102:105], v[82:85], v[78:81], v[102:105]\n"
    "s_waitcnt lgkmcnt(14)\n"
    "ds_read_b64_tr_b16 v[44:45], v230 offset:17408\n"
    "s_waitcnt lgkmcnt(14)\n"
    "ds_read_b64_tr_b16 v[46:47], v232 offset:16384\n"
    "v_mfma_f32_16x16x32_bf16 v[106:109], v[86:89], v[74:77], v[106:109]\n"
    "s_waitcnt lgkmcnt(14)\n"
    "ds_read_b64_tr_b16 v[48:49], v232 offset:17408\n"
    "v_mfma_f32_16x16x32_bf16 v[110:113], v[86:89], v[78:81], v[110:113]\n"
    "v_mfma_f32_16x16x32_bf16 v[114:117], v[90:93], v[74:77], v[114:117]\n"
    "v_mfma_f32_16x16x32_bf16 v[118:121], v[90:93], v[78:81], v[118:121]\n"
    "v_mfma_f32_16x16x32_bf16 v[122:125], v[94:97], v[74:77], v[122:125]\n"
    "v_mfma_f32_16x16x32_bf16 v[126:129], v[94:97], v[78:81], v[126:129]\n"
    "s_waitcnt lgkmcnt(0)\n"
    "s_barrier\n"
    "v_mfma_f32_16x16x32_bf16 v[98:101], v[10:13], v[2:5], v[98:101]\n"
    "ds_read_b128 v[50:53], v209 offset:51200\n"
    "ds_read_b64_tr_b16 v[58:59], v225 offset:16384\n"
    "v_mfma_f32_16x16x32_bf16 v[102:105], v[10:13], v[6:9], v[102:105]\n"
    "ds_read_b64_tr_b16 v[60:61], v225 offset:17408\n"
    "ds_read_b64_tr_b16 v[62:63], v227 offset:16384\n"
    "v_mfma_f32_16x16x32_bf16 v[106:109], v[14:17], v[2:5], v[106:109]\n"
    "ds_read_b64_tr_b16 v[64:65], v227 offset:17408\n"
    "ds_read_b128 v[54:57], v218 offset:53248\n"
    "v_mfma_f32_16x16x32_bf16 v[110:113], v[14:17], v[6:9], v[110:113]\n"
    "ds_read_b64_tr_b16 v[66:67], v229 offset:16384\n"
    "ds_read_b64_tr_b16 v[68:69], v229 offset:17408\n"
    "v_mfma_f32_16x16x32_bf16 v[114:117], v[18:21], v[2:5], v[114:117]\n"
    "ds_read_b64_tr_b16 v[70:71], v231 offset:16384\n"
    "ds_read_b64_tr_b16 v[72:73], v231 offset:17408\n"
    "v_mfma_f32_16x16x32_bf16 v[118:121], v[18:21], v[6:9], v[118:121]\n"
    "ds_read_b128 v[74:77], v218 offset:51200\n"
    "v_mfma_f32_16x16x32_bf16 v[122:125], v[22:25], v[2:5], v[122:125]\n"
    "ds_read_b64_tr_b16 v[82:83], v225 offset:24576\n"
    "ds_read_b64_tr_b16 v[84:85], v225 offset:25600\n"
    "v_mfma_f32_16x16x32_bf16 v[126:129], v[22:25], v[6:9], v[126:129]\n"
    "ds_read_b64_tr_b16 v[86:87], v227 offset:24576\n"
    "ds_read_b64_tr_b16 v[88:89], v227 offset:25600\n"
    "v_mfma_f32_16x16x32_bf16 v[98:101], v[34:37], v[26:29], v[98:101]\n"
    "s_waitcnt lgkmcnt(14)\n"
    "ds_read_b128 v[78:81], v209 offset:53248\n"
    "s_waitcnt lgkmcnt(14)\n"
    "ds_read_b64_tr_b16 v[90:91], v229 offset:24576\n"
    "v_mfma_f32_16x16x32_bf16 v[102:105], v[34:37], v[30:33], v[102:105]\n"
    "s_waitcnt lgkmcnt(14)\n"
    "ds_read_b64_tr_b16 v[92:93], v229 offset:25600\n"
    "s_waitcnt lgkmcnt(14)\n"
    "ds_read_b64_tr_b16 v[94:95], v231 offset:24576\n"
    "v_mfma_f32_16x16x32_bf16 v[106:109], v[38:41], v[26:29], v[106:109]\n"
    "s_waitcnt lgkmcnt(14)\n"
    "ds_read_b64_tr_b16 v[96:97], v231 offset:25600\n"
    "v_mfma_f32_16x16x32_bf16 v[110:113], v[38:41], v[30:33], v[110:113]\n"
    "v_mfma_f32_16x16x32_bf16 v[114:117], v[42:45], v[26:29], v[114:117]\n"
    "v_mfma_f32_16x16x32_bf16 v[118:121], v[42:45], v[30:33], v[118:121]\n"
    "v_mfma_f32_16x16x32_bf16 v[122:125], v[46:49], v[26:29], v[122:125]\n"
    "v_mfma_f32_16x16x32_bf16 v[126:129], v[46:49], v[30:33], v[126:129]\n"
    "s_waitcnt lgkmcnt(0)\n"
    "s_barrier\n"
    "v_mfma_f32_16x16x32_bf16 v[98:101], v[58:61], v[50:53], v[98:101]\n"
    "ds_read_b128 v[2:5], v209 offset:61440\n"
    "ds_read_b64_tr_b16 v[10:11], v225 offset:32768\n"
    "v_mfma_f32_16x16x32_bf16 v[102:105], v[58:61], v[54:57], v[102:105]\n"
    "ds_read_b64_tr_b16 v[12:13], v225 offset:33792\n"
    "ds_read_b64_tr_b16 v[14:15], v227 offset:32768\n"
    "v_mfma_f32_16x16x32_bf16 v[106:109], v[62:65], v[50:53], v[106:109]\n"
    "ds_read_b64_tr_b16 v[16:17], v227 offset:33792\n"
    "ds_read_b128 v[6:9], v218 offset:63488\n"
    "v_mfma_f32_16x16x32_bf16 v[110:113], v[62:65], v[54:57], v[110:113]\n"
    "ds_read_b64_tr_b16 v[18:19], v229 offset:32768\n"
    "ds_read_b64_tr_b16 v[20:21], v229 offset:33792\n"
    "v_mfma_f32_16x16x32_bf16 v[114:117], v[66:69], v[50:53], v[114:117]\n"
    "ds_read_b64_tr_b16 v[22:23], v231 offset:32768\n"
    "ds_read_b64_tr_b16 v[24:25], v231 offset:33792\n"
    "v_mfma_f32_16x16x32_bf16 v[118:121], v[66:69], v[54:57], v[118:121]\n"
    "ds_read_b128 v[26:29], v218 offset:61440\n"
    "v_mfma_f32_16x16x32_bf16 v[122:125], v[70:73], v[50:53], v[122:125]\n"
    "ds_read_b64_tr_b16 v[34:35], v225 offset:40960\n"
    "ds_read_b64_tr_b16 v[36:37], v225 offset:41984\n"
    "v_mfma_f32_16x16x32_bf16 v[126:129], v[70:73], v[54:57], v[126:129]\n"
    "ds_read_b64_tr_b16 v[38:39], v227 offset:40960\n"
    "ds_read_b64_tr_b16 v[40:41], v227 offset:41984\n"
    "v_mfma_f32_16x16x32_bf16 v[98:101], v[82:85], v[74:77], v[98:101]\n"
    "s_waitcnt lgkmcnt(14)\n"
    "ds_read_b128 v[30:33], v209 offset:63488\n"
    "s_waitcnt lgkmcnt(14)\n"
    "ds_read_b64_tr_b16 v[42:43], v229 offset:40960\n"
    "v_mfma_f32_16x16x32_bf16 v[102:105], v[82:85], v[78:81], v[102:105]\n"
    "s_waitcnt lgkmcnt(14)\n"
    "ds_read_b64_tr_b16 v[44:45], v229 offset:41984\n"
    "s_waitcnt lgkmcnt(14)\n"
    "ds_read_b64_tr_b16 v[46:47], v231 offset:40960\n"
    "v_mfma_f32_16x16x32_bf16 v[106:109], v[86:89], v[74:77], v[106:109]\n"
    "s_waitcnt lgkmcnt(14)\n"
    "ds_read_b64_tr_b16 v[48:49], v231 offset:41984\n"
    "v_mfma_f32_16x16x32_bf16 v[110:113], v[86:89], v[78:81], v[110:113]\n"
    "v_mfma_f32_16x16x32_bf16 v[114:117], v[90:93], v[74:77], v[114:117]\n"
    "v_mfma_f32_16x16x32_bf16 v[118:121], v[90:93], v[78:81], v[118:121]\n"
    "v_mfma_f32_16x16x32_bf16 v[122:125], v[94:97], v[74:77], v[122:125]\n"
    "v_mfma_f32_16x16x32_bf16 v[126:129], v[94:97], v[78:81], v[126:129]\n"
    "s_waitcnt lgkmcnt(0)\n"
    "s_barrier\n"
    "v_mfma_f32_16x16x32_bf16 v[98:101], v[10:13], v[2:5], v[98:101]\n"
    "ds_read_b128 v[50:53], v216 offset:6144\n"
    "ds_read_b64_tr_b16 v[58:59], v225 offset:49152\n"
    "v_mfma_f32_16x16x32_bf16 v[102:105], v[10:13], v[6:9], v[102:105]\n"
    "ds_read_b64_tr_b16 v[60:61], v225 offset:50176\n"
    "ds_read_b64_tr_b16 v[62:63], v227 offset:49152\n"
    "v_mfma_f32_16x16x32_bf16 v[106:109], v[14:17], v[2:5], v[106:109]\n"
    "ds_read_b64_tr_b16 v[64:65], v227 offset:50176\n"
    "ds_read_b128 v[54:57], v219 offset:8192\n"
    "v_mfma_f32_16x16x32_bf16 v[110:113], v[14:17], v[6:9], v[110:113]\n"
    "ds_read_b64_tr_b16 v[66:67], v229 offset:49152\n"
    "ds_read_b64_tr_b16 v[68:69], v229 offset:50176\n"
    "v_mfma_f32_16x16x32_bf16 v[114:117], v[18:21], v[2:5], v[114:117]\n"
    "ds_read_b64_tr_b16 v[70:71], v231 offset:49152\n"
    "ds_read_b64_tr_b16 v[72:73], v231 offset:50176\n"
    "v_mfma_f32_16x16x32_bf16 v[118:121], v[18:21], v[6:9], v[118:121]\n"
    "ds_read_b128 v[74:77], v219 offset:6144\n"
    "v_mfma_f32_16x16x32_bf16 v[122:125], v[22:25], v[2:5], v[122:125]\n"
    "ds_read_b64_tr_b16 v[82:83], v225 offset:57344\n"
    "ds_read_b64_tr_b16 v[84:85], v225 offset:58368\n"
    "v_mfma_f32_16x16x32_bf16 v[126:129], v[22:25], v[6:9], v[126:129]\n"
    "ds_read_b64_tr_b16 v[86:87], v227 offset:57344\n"
    "ds_read_b64_tr_b16 v[88:89], v227 offset:58368\n"
    "v_mfma_f32_16x16x32_bf16 v[98:101], v[34:37], v[26:29], v[98:101]\n"
    "s_waitcnt lgkmcnt(14)\n"
    "ds_read_b128 v[78:81], v216 offset:8192\n"
    "s_waitcnt lgkmcnt(14)\n"
    "ds_read_b64_tr_b16 v[90:91], v229 offset:57344\n"
    "v_mfma_f32_16x16x32_bf16 v[102:105], v[34:37], v[30:33], v[102:105]\n"
    "s_waitcnt lgkmcnt(14)\n"
    "ds_read_b64_tr_b16 v[92:93], v229 offset:58368\n"
    "s_waitcnt lgkmcnt(14)\n"
    "ds_read_b64_tr_b16 v[94:95], v231 offset:57344\n"
    "v_mfma_f32_16x16x32_bf16 v[106:109], v[38:41], v[26:29], v[106:109]\n"
    "s_waitcnt lgkmcnt(14)\n"
    "ds_read_b64_tr_b16 v[96:97], v231 offset:58368\n"
    "v_mfma_f32_16x16x32_bf16 v[110:113], v[38:41], v[30:33], v[110:113]\n"
    "v_mfma_f32_16x16x32_bf16 v[114:117], v[42:45], v[26:29], v[114:117]\n"
    "v_mfma_f32_16x16x32_bf16 v[118:121], v[42:45], v[30:33], v[118:121]\n"
    "v_mfma_f32_16x16x32_bf16 v[122:125], v[46:49], v[26:29], v[122:125]\n"
    "v_mfma_f32_16x16x32_bf16 v[126:129], v[46:49], v[30:33], v[126:129]\n"
    "s_waitcnt lgkmcnt(0)\n"
    "s_barrier\n"
    "v_mfma_f32_16x16x32_bf16 v[98:101], v[58:61], v[50:53], v[98:101]\n"
    "ds_read_b128 v[2:5], v221 offset:0\n"
    "v_mfma_f32_16x16x32_bf16 v[102:105], v[58:61], v[54:57], v[102:105]\n"
    "ds_read_b64_tr_b16 v[22:23], v242 offset:0\n"
    "v_mfma_f32_16x16x32_bf16 v[106:109], v[62:65], v[50:53], v[106:109]\n"
    "ds_read_b64_tr_b16 v[24:25], v242 offset:512\n"
    "v_mfma_f32_16x16x32_bf16 v[110:113], v[62:65], v[54:57], v[110:113]\n"
    "ds_read_b128 v[6:9], v223 offset:2048\n"
    "v_mfma_f32_16x16x32_bf16 v[114:117], v[66:69], v[50:53], v[114:117]\n"
    "ds_read_b64_tr_b16 v[26:27], v242 offset:1024\n"
    "v_mfma_f32_16x16x32_bf16 v[118:121], v[66:69], v[54:57], v[118:121]\n"
    "ds_read_b128 v[10:13], v221 offset:4096\n"
    "v_mfma_f32_16x16x32_bf16 v[122:125], v[70:73], v[50:53], v[122:125]\n"
    "ds_read_b64_tr_b16 v[28:29], v242 offset:1536\n"
    "v_mfma_f32_16x16x32_bf16 v[126:129], v[70:73], v[54:57], v[126:129]\n"
    "ds_read_b128 v[14:17], v223 offset:6144\n"
    "v_mfma_f32_16x16x32_bf16 v[98:101], v[82:85], v[74:77], v[98:101]\n"
    "ds_read_b64_tr_b16 v[30:31], v242 offset:2048\n"
    "v_mfma_f32_16x16x32_bf16 v[102:105], v[82:85], v[78:81], v[102:105]\n"
    "ds_read_b128 v[18:21], v221 offset:8192\n"
    "v_mfma_f32_16x16x32_bf16 v[106:109], v[86:89], v[74:77], v[106:109]\n"
    "ds_read_b64_tr_b16 v[32:33], v242 offset:2560\n"
    "v_mfma_f32_16x16x32_bf16 v[110:113], v[86:89], v[78:81], v[110:113]\n"
    "v_mfma_f32_16x16x32_bf16 v[114:117], v[90:93], v[74:77], v[114:117]\n"
    "v_mfma_f32_16x16x32_bf16 v[118:121], v[90:93], v[78:81], v[118:121]\n"
    "v_mfma_f32_16x16x32_bf16 v[122:125], v[94:97], v[74:77], v[122:125]\n"
    "v_mfma_f32_16x16x32_bf16 v[126:129], v[94:97], v[78:81], v[126:129]\n"
    "s_waitcnt lgkmcnt(0)\n"
    "s_barrier\n"
    "ds_read_b128 v[34:37], v235 offset:2048\n"
    "ds_read_b128 v[42:45], v235 offset:2304\n"
    "ds_read_b128 v[38:41], v235 offset:2064\n"
    "ds_read_b128 v[46:49], v235 offset:2320\n"
    "s_xor_b32 s40, s47, 0\n"
    "s_mul_i32 s50, s40, 458752\n"
    "s_mul_i32 s51, s46, 14336\n"
    "s_add_u32 s50, s50, s51\n"
    "s_mul_i32 s51, s56, 7168\n"
    "s_add_u32 s50, s50, s51\n"
    "s_add_u32 s50, s50, 0x100000\n"
    "s_waitcnt lgkmcnt(0)\n"
    "v_pk_fma_f32 v[130:131], v[130:131], v[34:35], v[42:43]\n"
    "v_pk_fma_f32 v[132:133], v[132:133], v[36:37], v[44:45]\n"
    "v_pk_fma_f32 v[134:135], v[134:135], v[38:39], v[46:47]\n"
    "v_pk_fma_f32 v[136:137], v[136:137], v[40:41], v[48:49]\n"
    "v_cvt_pk_bf16_f32 v50, v130, v131\n"
    "v_cvt_pk_bf16_f32 v51, v132, v133\n"
    "v_cvt_pk_bf16_f32 v52, v134, v135\n"
    "v_cvt_pk_bf16_f32 v53, v136, v137\n"
    "s_lshl_b32 s51, s53, 10\n"
    "s_add_u32 s51, s51, s50\n"
    "buffer_store_dwordx4 v[50:53], v234, s[36:39], s51 offen sc1\n"
    "v_pk_fma_f32 v[138:139], v[138:139], v[34:35], v[42:43]\n"
    "v_pk_fma_f32 v[140:141], v[140:141], v[36:37], v[44:45]\n"
    "v_pk_fma_f32 v[142:143], v[142:143], v[38:39], v[46:47]\n"
    "v_pk_fma_f32 v[144:145], v[144:145], v[40:41], v[48:49]\n"
    "v_cvt_pk_bf16_f32 v54, v138, v139\n"
    "v_cvt_pk_bf16_f32 v55, v140, v141\n"
    "v_cvt_pk_bf16_f32 v56, v142, v143\n"
    "v_cvt_pk_bf16_f32 v57, v144, v145\n"
    "s_lshl_b32 s51, s54, 10\n"
    "s_add_u32 s51, s51, s50\n"
    "buffer_store_dwordx4 v[54:57], v234, s[36:39], s51 offen sc1\n"
    "v_pk_fma_f32 v[146:147], v[146:147], v[34:35], v[42:43]\n"
    "v_pk_fma_f32 v[148:149], v[148:149], v[36:37], v[44:45]\n"
    "v_pk_fma_f32 v[150:151], v[150:151], v[38:39], v[46:47]\n"
    "v_pk_fma_f32 v[152:153], v[152:153], v[40:41], v[48:49]\n"
    "v_cvt_pk_bf16_f32 v58, v146, v147\n"
    "v_cvt_pk_bf16_f32 v59, v148, v149\n"
    "v_cvt_pk_bf16_f32 v60, v150, v151\n"
    "v_cvt_pk_bf16_f32 v61, v152, v153\n"
    "s_lshl_b32 s51, s60, 10\n"
    "s_add_u32 s51, s51, s50\n"
    "buffer_store_dwordx4 v[58:61], v234, s[36:39], s51 offen sc1\n"
    "v_pk_fma_f32 v[154:155], v[154:155], v[34:35], v[42:43]\n"
    "v_pk_fma_f32 v[156:157], v[156:157], v[36:37], v[44:45]\n"
    "v_pk_fma_f32 v[158:159], v[158:159], v[38:39], v[46:47]\n"
    "v_pk_fma_f32 v[160:161], v[160:161], v[40:41], v[48:49]\n"
    "v_cvt_pk_bf16_f32 v62, v154, v155\n"
    "v_cvt_pk_bf16_f32 v63, v156, v157\n"
    "v_cvt_pk_bf16_f32 v64, v158, v159\n"
    "v_cvt_pk_bf16_f32 v65, v160, v161\n"
    "s_lshl_b32 s51, s61, 10\n"
    "s_add_u32 s51, s51, s50\n"
    "buffer_store_dwordx4 v[62:65], v234, s[36:39], s51 offen sc1\n"
    "s_barrier\n"
    "v_mfma_f32_16x16x32_bf16 v[130:133], v[22:25], v[2:5], 0\n"
    "ds_read_b128 v[34:37], v223 offset:0\n"
    "ds_read_b64_tr_b16 v[54:55], v242 offset:12288\n"
    "v_mfma_f32_16x16x32_bf16 v[146:149], v[26:29], v[2:5], 0\n"
    "ds_read_b64_tr_b16 v[56:57], v242 offset:12800\n"
    "v_lshlrev_b32 v206, 2, v203\n"
    "v_mfma_f32_16x16x32_bf16 v[166:169], v[2:5], v[30:33], 0\n"
    "ds_read_b128 v[38:41], v221 offset:2048\n"
    "v_sub_u32 v206, v202, v206\n"
    "v_mfma_f32_16x16x32_bf16 v[134:137], v[22:25], v[6:9], 0\n"
    "ds_read_b64_tr_b16 v[58:59], v242 offset:13312\n"
    "v_subrev_u32 v207, 0, v206\n"
    "v_mfma_f32_16x16x32_bf16 v[150:153], v[26:29], v[6:9], 0\n"
    "ds_read_b128 v[42:45], v223 offset:4096\n"
    "ds_read_b64_tr_b16 v[60:61], v242 offset:13824\n"
    "v_mfma_f32_16x16x32_bf16 v[170:173], v[6:9], v[30:33], 0\n"
    "ds_read_b128 v[46:49], v221 offset:6144\n"
    "v_and_b32 v208, 0x80000001, v207\n"
    "v_mfma_f32_16x16x32_bf16 v[138:141], v[22:25], v[10:13], 0\n"
    "ds_read_b64_tr_b16 v[62:63], v242 offset:14336\n"
    "v_mov_b32 v244, 0xf149f2ca\n"
    "v_mfma_f32_16x16x32_bf16 v[154:157], v[26:29], v[10:13], 0\n"
    "ds_read_b128 v[50:53], v223 offset:8192\n"
    "v_cmp_eq_u32 vcc, 0, v208\n"
    "v_mfma_f32_16x16x32_bf16 v[174:177], v[10:13], v[30:33], 0\n"
    "ds_read_b64_tr_b16 v[64:65], v242 offset:14848\n"
    "v_add_u32 v207, 1, v207\n"
    "v_mfma_f32_16x16x32_bf16 v[142:145], v[22:25], v[14:17], 0\n"
    "v_mov_b32 v226, 0xf149f2ca\n"
    "v_cndmask_b32 v244, v244, 0, vcc\n"
    "v_mfma_f32_16x16x32_bf16 v[158:161], v[26:29], v[14:17], 0\n"
    "v_and_b32 v208, 0x80000001, v207\n"
    "v_cmp_eq_u32 vcc, 0x80000001, v208\n"
    "v_mfma_f32_16x16x32_bf16 v[178:181], v[14:17], v[30:33], 0\n"
    "s_nop 0\n"
    "v_cndmask_b32 v226, v226, 0, vcc\n"
    "v_mfma_f32_16x16x32_bf16 v[162:165], v[26:29], v[18:21], 0\n"
    "v_min_f32 v248, s3, v226\n"
    "v_subrev_u32 v207, 1, v206\n"
    "v_mfma_f32_16x16x32_bf16 v[182:185], v[18:21], v[30:33], 0\n"
    "v_and_b32 v208, 0x80000001, v207\n"
    "v_mov_b32 v245, 0xf149f2ca\n"
    "s_waitcnt lgkmcnt(0)\n"
    "s_barrier\n"
    "v_mfma_f32_16x16x32_bf16 v[130:133], v[54:57], v[34:37], v[130:133]\n"
    "ds_read_b128 v[2:5], v221 offset:10240\n"
    "ds_read_b64_tr_b16 v[22:23], v237 offset:16384\n"
    "v_mfma_f32_16x16x32_bf16 v[146:149], v[58:61], v[34:37], v[146:149]\n"
    "ds_read_b64_tr_b16 v[24:25], v237 offset:16896\n"
    "v_cmp_eq_u32 vcc, 0, v208\n"
    "v_mfma_f32_16x16x32_bf16 v[166:169], v[34:37], v[62:65], v[166:169]\n"
    "ds_read_b128 v[6:9], v223 offset:12288\n"
    "v_add_u32 v207, 1, v207\n"
    "v_mfma_f32_16x16x32_bf16 v[134:137], v[54:57], v[38:41], v[134:137]\n"
    "ds_read_b64_tr_b16 v[26:27], v237 offset:17408\n"
    "v_mov_b32 v227, 0xf149f2ca\n"
    "v_mfma_f32_16x16x32_bf16 v[150:153], v[58:61], v[38:41], v[150:153]\n"
    "ds_read_b128 v[10:13], v221 offset:14336\n"
    "ds_read_b64_tr_b16 v[28:29], v237 offset:17920\n"
    "v_mfma_f32_16x16x32_bf16 v[170:173], v[38:41], v[62:65], v[170:173]\n"
    "ds_read_b128 v[14:17], v223 offset:16384\n"
    "v_cndmask_b32 v245, v245, 0, vcc\n"
    "v_mfma_f32_16x16x32_bf16 v[138:141], v[54:57], v[42:45], v[138:141]\n"
    "ds_read_b64_tr_b16 v[30:31], v237 offset:18432\n"
    "v_and_b32 v208, 0x80000001, v207\n"
    "v_mfma_f32_16x16x32_bf16 v[154:157], v[58:61], v[42:45], v[154:157]\n"
    "ds_read_b128 v[18:21], v221 offset:18432\n"
    "v_cmp_eq_u32 vcc, 0x80000001, v208\n"
    "v_mfma_f32_16x16x32_bf16 v[174:177], v[42:45], v[62:65], v[174:177]\n"
    "ds_read_b64_tr_b16 v[32:33], v237 offset:18944\n"
    "s_nop 0\n"
    "v_mfma_f32_16x16x32_bf16 v[142:145], v[54:57], v[46:49], v[142:145]\n"
    "v_cndmask_b32 v227, v227, 0, vcc\n"
    "v_min_f32 v249, s3, v227\n"
    "v_mfma_f32_16x16x32_bf16 v[158:161], v[58:61], v[46:49], v[158:161]\n"
    "v_subrev_u32 v207, 2, v206\n"
    "v_and_b32 v208, 0x80000001, v207\n"
    "v_mfma_f32_16x16x32_bf16 v[178:181], v[46:49], v[62:65], v[178:181]\n"
    "v_mov_b32 v246, 0xf149f2ca\n"
    "v_cmp_eq_u32 vcc, 0, v208\n"
    "v_mfma_f32_16x16x32_bf16 v[162:165], v[58:61], v[50:53], v[162:165]\n"
    "v_add_u32 v207, 1, v207\n"
    "v_mov_b32 v228, 0xf149f2ca\n"
    "v_mfma_f32_16x16x32_bf16 v[182:185], v[50:53], v[62:65], v[182:185]\n"
    "v_cndmask_b32 v246, v246, 0, vcc\n"
    "v_and_b32 v208, 0x80000001, v207\n"
    "s_lshl_b32 s50, s40, 10\n"
    "s_lshl_b32 s51, s46, 5\n"
    "s_add_u32 s50, s50, s51\n"
    "s_lshl_b32 s51, s45, 3\n"
    "s_add_u32 s50, s50, s51\n"
    "s_add_u32 s50, s50, 0x1000\n"
    "s_waitcnt vmcnt(0)\n"
    "s_mov_b64 exec, 1\n"
    "buffer_store_dwordx2 v[214:215], off, s[36:39], s50 sc1\n"
    "s_mov_b64 exec, -1\n"
    "s_waitcnt lgkmcnt(0)\n"
    "v_mfma_f32_16x16x32_bf16 v[130:133], v[22:25], v[2:5], v[130:133]\n"
    "ds_read_b128 v[34:37], v223 offset:10240\n"
    "ds_read_b64_tr_b16 v[54:55], v237 offset:28672\n"
    "v_mfma_f32_16x16x32_bf16 v[146:149], v[26:29], v[2:5], v[146:149]\n"
    "ds_read_b64_tr_b16 v[56:57], v237 offset:29184\n"
    "v_cmp_eq_u32 vcc, 0x80000001, v208\n"
    "v_mfma_f32_16x16x32_bf16 v[166:169], v[2:5], v[30:33], v[166:169]\n"
    "ds_read_b128 v[38:41], v221 offset:12288\n"
    "s_nop 0\n"
    "v_mfma_f32_16x16x32_bf16 v[134:137], v[22:25], v[6:9], v[134:137]\n"
    "ds_read_b64_tr_b16 v[58:59], v237 offset:29696\n"
    "v_cndmask_b32 v228, v228, 0, vcc\n"
    "v_mfma_f32_16x16x32_bf16 v[150:153], v[26:29], v[6:9], v[150:153]\n"
    "ds_read_b128 v[42:45], v223 offset:14336\n"
    "ds_read_b64_tr_b16 v[60:61], v237 offset:30208\n"
    "v_mfma_f32_16x16x32_bf16 v[170:173], v[6:9], v[30:33], v[170:173]\n"
    "ds_read_b128 v[46:49], v221 offset:16384\n"
    "v_min_f32 v250, s3, v228\n"
    "v_mfma_f32_16x16x32_bf16 v[138:141], v[22:25], v[10:13], v[138:141]\n"
    "ds_read_b64_tr_b16 v[62:63], v237 offset:30720\n"
    "v_subrev_u32 v207, 3, v206\n"
    "v_mfma_f32_16x16x32_bf16 v[154:157], v[26:29], v[10:13], v[154:157]\n"
    "ds_read_b128 v[50:53], v223 offset:18432\n"
    "v_and_b32 v208, 0x80000001, v207\n"
    "v_mfma_f32_16x16x32_bf16 v[174:177], v[10:13], v[30:33], v[174:177]\n"
    "ds_read_b64_tr_b16 v[64:65], v237 offset:31232\n"
    "v_mov_b32 v247, 0xf149f2ca\n"
    "v_mfma_f32_16x16x32_bf16 v[142:145], v[22:25], v[14:17], v[142:145]\n"
    "v_cmp_eq_u32 vcc, 0, v208\n"
    "v_add_u32 v207, 1, v207\n"
    "v_mfma_f32_16x16x32_bf16 v[158:161], v[26:29], v[14:17], v[158:161]\n"
    "v_mov_b32 v229, 0xf149f2ca\n"
    "v_cndmask_b32 v247, v247, 0, vcc\n"
    "v_mfma_f32_16x16x32_bf16 v[178:181], v[14:17], v[30:33], v[178:181]\n"
    "v_and_b32 v208, 0x80000001, v207\n"
    "v_cmp_eq_u32 vcc, 0x80000001, v208\n"
    "v_mfma_f32_16x16x32_bf16 v[162:165], v[26:29], v[18:21], v[162:165]\n"
    "s_nop 0\n"
    "v_cndmask_b32 v229, v229, 0, vcc\n"
    "v_mfma_f32_16x16x32_bf16 v[182:185], v[18:21], v[30:33], v[182:185]\n"
    "v_min_f32 v251, s3, v229\n"
    "s_lshl_b32 s62, s58, 10\n"
    "s_waitcnt lgkmcnt(0)\n"
    "s_barrier\n"
    "v_mfma_f32_16x16x32_bf16 v[130:133], v[54:57], v[34:37], v[130:133]\n"
    "ds_read_b128 v[2:5], v221 offset:20480\n"
    "ds_read_b64_tr_b16 v[22:23], v237 offset:40960\n"
    "v_mfma_f32_16x16x32_bf16 v[146:149], v[58:61], v[34:37], v[146:149]\n"
    "ds_read_b64_tr_b16 v[24:25], v237 offset:41472\n"
    "v_add_u32 v225, s62, v234\n"
    "v_mfma_f32_16x16x32_bf16 v[166:169], v[34:37], v[62:65], v[166:169]\n"
    "ds_read_b128 v[6:9], v223 offset:22528\n"
    "v_add_u32 v230, 0x10000, v225\n"
    "v_mfma_f32_16x16x32_bf16 v[134:137], v[54:57], v[38:41], v[134:137]\n"
    "ds_read_b64_tr_b16 v[26:27], v237 offset:41984\n"
    "v_add_u32 v231, 0x20000, v225\n"
    "v_mfma_f32_16x16x32_bf16 v[150:153], v[58:61], v[38:41], v[150:153]\n"
    "ds_read_b128 v[10:13], v221 offset:24576\n"
    "ds_read_b64_tr_b16 v[28:29], v237 offset:42496\n"
    "v_mfma_f32_16x16x32_bf16 v[170:173], v[38:41], v[62:65], v[170:173]\n"
    "ds_read_b128 v[14:17], v223 offset:26624\n"
    "s_lshl_b32 s62, s46, 7\n"
    "s_lshl_b32 s63, s56, 6\n"
    "v_mfma_f32_16x16x32_bf16 v[138:141], v[54:57], v[42:45], v[138:141]\n"
    "ds_read_b64_tr_b16 v[30:31], v237 offset:43008\n"
    "s_add_u32 s62, s62, s63\n"
    "v_mfma_f32_16x16x32_bf16 v[154:157], v[58:61], v[42:45], v[154:157]\n"
    "ds_read_b128 v[18:21], v221 offset:28672\n"
    "v_lshlrev_b32 v206, 1, v202\n"
    "v_mfma_f32_16x16x32_bf16 v[174:177], v[42:45], v[62:65], v[174:177]\n"
    "ds_read_b64_tr_b16 v[32:33], v237 offset:43520\n"
    "buffer_load_ushort v200, v206, s[24:27], s62 offen offset:0\n"
    "v_mfma_f32_16x16x32_bf16 v[142:145], v[54:57], v[46:49], v[142:145]\n"
    "buffer_load_ushort v201, v206, s[24:27], s62 offen offset:32\n"
    "v_mfma_f32_16x16x32_bf16 v[158:161], v[58:61], v[46:49], v[158:161]\n"
    "v_mfma_f32_16x16x32_bf16 v[178:181], v[46:49], v[62:65], v[178:181]\n"
    "v_mfma_f32_16x16x32_bf16 v[162:165], v[58:61], v[50:53], v[162:165]\n"
    "v_mfma_f32_16x16x32_bf16 v[182:185], v[50:53], v[62:65], v[182:185]\n"
    "s_waitcnt lgkmcnt(0)\n"
    "v_mfma_f32_16x16x32_bf16 v[130:133], v[22:25], v[2:5], v[130:133]\n"
    "ds_read_b128 v[34:37], v223 offset:20480\n"
    "ds_read_b64_tr_b16 v[54:55], v237 offset:53248\n"
    "v_mfma_f32_16x16x32_bf16 v[146:149], v[26:29], v[2:5], v[146:149]\n"
    "ds_read_b64_tr_b16 v[56:57], v237 offset:53760\n"
    "v_mfma_f32_16x16x32_bf16 v[166:169], v[2:5], v[30:33], v[166:169]\n"
    "ds_read_b128 v[38:41], v221 offset:22528\n"
    "v_mfma_f32_16x16x32_bf16 v[134:137], v[22:25], v[6:9], v[134:137]\n"
    "ds_read_b64_tr_b16 v[58:59], v237 offset:54272\n"
    "v_mfma_f32_16x16x32_bf16 v[150:153], v[26:29], v[6:9], v[150:153]\n"
    "ds_read_b128 v[42:45], v223 offset:24576\n"
    "ds_read_b64_tr_b16 v[60:61], v237 offset:54784\n"
    "v_mfma_f32_16x16x32_bf16 v[170:173], v[6:9], v[30:33], v[170:173]\n"
    "ds_read_b128 v[46:49], v221 offset:26624\n"
    "v_mfma_f32_16x16x32_bf16 v[138:141], v[22:25], v[10:13], v[138:141]\n"
    "ds_read_b64_tr_b16 v[62:63], v237 offset:55296\n"
    "v_mfma_f32_16x16x32_bf16 v[154:157], v[26:29], v[10:13], v[154:157]\n"
    "ds_read_b128 v[50:53], v223 offset:28672\n"
    "v_mfma_f32_16x16x32_bf16 v[174:177], v[10:13], v[30:33], v[174:177]\n"
    "ds_read_b64_tr_b16 v[64:65], v237 offset:55808\n"
    "v_mfma_f32_16x16x32_bf16 v[142:145], v[22:25], v[14:17], v[142:145]\n"
    "v_mfma_f32_16x16x32_bf16 v[158:161], v[26:29], v[14:17], v[158:161]\n"
    "v_mfma_f32_16x16x32_bf16 v[178:181], v[14:17], v[30:33], v[178:181]\n"
    "v_mfma_f32_16x16x32_bf16 v[162:165], v[26:29], v[18:21], v[162:165]\n"
    "v_mfma_f32_16x16x32_bf16 v[182:185], v[18:21], v[30:33], v[182:185]\n"
    "s_waitcnt lgkmcnt(0)\n"
    "s_barrier\n"
    "v_mfma_f32_16x16x32_bf16 v[130:133], v[54:57], v[34:37], v[130:133]\n"
    "ds_read_b128 v[2:5], v221 offset:30720\n"
    "ds_read_b64_tr_b16 v[22:23], v242 offset:0\n"
    "v_mfma_f32_16x16x32_bf16 v[146:149], v[58:61], v[34:37], v[146:149]\n"
    "ds_read_b64_tr_b16 v[24:25], v242 offset:512\n"
    "v_mfma_f32_16x16x32_bf16 v[166:169], v[34:37], v[62:65], v[166:169]\n"
    "ds_read_b128 v[6:9], v223 offset:32768\n"
    "v_mfma_f32_16x16x32_bf16 v[134:137], v[54:57], v[38:41], v[134:137]\n"
    "ds_read_b64_tr_b16 v[26:27], v242 offset:1024\n"
    "v_mfma_f32_16x16x32_bf16 v[150:153], v[58:61], v[38:41], v[150:153]\n"
    "ds_read_b128 v[10:13], v221 offset:34816\n"
    "ds_read_b64_tr_b16 v[28:29], v242 offset:1536\n"
    "v_mfma_f32_16x16x32_bf16 v[170:173], v[38:41], v[62:65], v[170:173]\n"
    "ds_read_b128 v[14:17], v223 offset:36864\n"
    "v_mfma_f32_16x16x32_bf16 v[138:141], v[54:57], v[42:45], v[138:141]\n"
    "ds_read_b64_tr_b16 v[30:31], v242 offset:2048\n"
    "v_mfma_f32_16x16x32_bf16 v[154:157], v[58:61], v[42:45], v[154:157]\n"
    "ds_read_b128 v[18:21], v221 offset:38912\n"
    "v_mfma_f32_16x16x32_bf16 v[174:177], v[42:45], v[62:65], v[174:177]\n"
    "ds_read_b64_tr_b16 v[32:33], v242 offset:2560\n"
    "v_mfma_f32_16x16x32_bf16 v[142:145], v[54:57], v[46:49], v[142:145]\n"
    "v_mfma_f32_16x16x32_bf16 v[158:161], v[58:61], v[46:49], v[158:161]\n"
    "v_mfma_f32_16x16x32_bf16 v[178:181], v[46:49], v[62:65], v[178:181]\n"
    "v_mfma_f32_16x16x32_bf16 v[162:165], v[58:61], v[50:53], v[162:165]\n"
    "v_mfma_f32_16x16x32_bf16 v[182:185], v[50:53], v[62:65], v[182:185]\n"
    "s_waitcnt lgkmcnt(0)\n"
    "v_mfma_f32_16x16x32_bf16 v[130:133], v[22:25], v[2:5], v[130:133]\n"
    "ds_read_b128 v[34:37], v223 offset:30720\n"
    "ds_read_b64_tr_b16 v[54:55], v242 offset:12288\n"
    "v_mfma_f32_16x16x32_bf16 v[146:149], v[26:29], v[2:5], v[146:149]\n"
    "ds_read_b64_tr_b16 v[56:57], v242 offset:12800\n"
    "v_mfma_f32_16x16x32_bf16 v[166:169], v[2:5], v[30:33], v[166:169]\n"
    "ds_read_b128 v[38:41], v221 offset:32768\n"
    "v_mfma_f32_16x16x32_bf16 v[134:137], v[22:25], v[6:9], v[134:137]\n"
    "ds_read_b64_tr_b16 v[58:59], v242 offset:13312\n"
    "v_mfma_f32_16x16x32_bf16 v[150:153], v[26:29], v[6:9], v[150:153]\n"
    "ds_read_b128 v[42:45], v223 offset:34816\n"
    "ds_read_b64_tr_b16 v[60:61], v242 offset:13824\n"
    "v_mfma_f32_16x16x32_bf16 v[170:173], v[6:9], v[30:33], v[170:173]\n"
    "ds_read_b128 v[46:49], v221 offset:36864\n"
    "v_mfma_f32_16x16x32_bf16 v[138:141], v[22:25], v[10:13], v[138:141]\n"
    "ds_read_b64_tr_b16 v[62:63], v242 offset:14336\n"
    "v_mfma_f32_16x16x32_bf16 v[154:157], v[26:29], v[10:13], v[154:157]\n"
    "ds_read_b128 v[50:53], v223 offset:38912\n"
    "v_mfma_f32_16x16x32_bf16 v[174:177], v[10:13], v[30:33], v[174:177]\n"
    "ds_read_b64_tr_b16 v[64:65], v242 offset:14848\n"
    "v_mfma_f32_16x16x32_bf16 v[142:145], v[22:25], v[14:17], v[142:145]\n"
    "v_mfma_f32_16x16x32_bf16 v[158:161], v[26:29], v[14:17], v[158:161]\n"
    "v_mfma_f32_16x16x32_bf16 v[178:181], v[14:17], v[30:33], v[178:181]\n"
    "v_mfma_f32_16x16x32_bf16 v[162:165], v[26:29], v[18:21], v[162:165]\n"
    "v_mfma_f32_16x16x32_bf16 v[182:185], v[18:21], v[30:33], v[182:185]\n"
    "s_waitcnt lgkmcnt(0)\n"
    "s_barrier\n"
    "v_mfma_f32_16x16x32_bf16 v[130:133], v[54:57], v[34:37], v[130:133]\n"
    "ds_read_b128 v[2:5], v221 offset:40960\n"
    "ds_read_b64_tr_b16 v[22:23], v237 offset:16384\n"
    "v_mfma_f32_16x16x32_bf16 v[146:149], v[58:61], v[34:37], v[146:149]\n"
    "ds_read_b64_tr_b16 v[24:25], v237 offset:16896\n"
    "v_mfma_f32_16x16x32_bf16 v[166:169], v[34:37], v[62:65], v[166:169]\n"
    "ds_read_b128 v[6:9], v223 offset:43008\n"
    "v_mfma_f32_16x16x32_bf16 v[134:137], v[54:57], v[38:41], v[134:137]\n"
    "ds_read_b64_tr_b16 v[26:27], v237 offset:17408\n"
    "v_mfma_f32_16x16x32_bf16 v[150:153], v[58:61], v[38:41], v[150:153]\n"
    "ds_read_b128 v[10:13], v221 offset:45056\n"
    "ds_read_b64_tr_b16 v[28:29], v237 offset:17920\n"
    "v_mfma_f32_16x16x32_bf16 v[170:173], v[38:41], v[62:65], v[170:173]\n"
    "ds_read_b128 v[14:17], v223 offset:47104\n"
    "v_mfma_f32_16x16x32_bf16 v[138:141], v[54:57], v[42:45], v[138:141]\n"
    "ds_read_b64_tr_b16 v[30:31], v237 offset:18432\n"
    "v_mfma_f32_16x16x32_bf16 v[154:157], v[58:61], v[42:45], v[154:157]\n"
    "ds_read_b128 v[18:21], v221 offset:49152\n"
    "v_mfma_f32_16x16x32_bf16 v[174:177], v[42:45], v[62:65], v[174:177]\n"
    "ds_read_b64_tr_b16 v[32:33], v237 offset:18944\n"
    "v_mfma_f32_16x16x32_bf16 v[142:145], v[54:57], v[46:49], v[142:145]\n"
    "v_mfma_f32_16x16x32_bf16 v[158:161], v[58:61], v[46:49], v[158:161]\n"
    "v_mfma_f32_16x16x32_bf16 v[178:181], v[46:49], v[62:65], v[178:181]\n"
    "v_mfma_f32_16x16x32_bf16 v[162:165], v[58:61], v[50:53], v[162:165]\n"
    "v_mfma_f32_16x16x32_bf16 v[182:185], v[50:53], v[62:65], v[182:185]\n"
    "s_waitcnt lgkmcnt(0)\n"
    "v_mfma_f32_16x16x32_bf16 v[130:133], v[22:25], v[2:5], v[130:133]\n"
    "ds_read_b128 v[34:37], v223 offset:40960\n"
    "ds_read_b64_tr_b16 v[54:55], v237 offset:28672\n"
    "v_mfma_f32_16x16x32_bf16 v[146:149], v[26:29], v[2:5], v[146:149]\n"
    "ds_read_b64_tr_b16 v[56:57], v237 offset:29184\n"
    "v_mfma_f32_16x16x32_bf16 v[166:169], v[2:5], v[30:33], v[166:169]\n"
    "ds_read_b128 v[38:41], v221 offset:43008\n"
    "v_mfma_f32_16x16x32_bf16 v[134:137], v[22:25], v[6:9], v[134:137]\n"
    "ds_read_b64_tr_b16 v[58:59], v237 offset:29696\n"
    "v_mfma_f32_16x16x32_bf16 v[150:153], v[26:29], v[6:9], v[150:153]\n"
    "ds_read_b128 v[42:45], v223 offset:45056\n"
    "ds_read_b64_tr_b16 v[60:61], v237 offset:30208\n"
    "v_mfma_f32_16x16x32_bf16 v[170:173], v[6:9], v[30:33], v[170:173]\n"
    "ds_read_b128 v[46:49], v221 offset:47104\n"
    "v_mfma_f32_16x16x32_bf16 v[138:141], v[22:25], v[10:13], v[138:141]\n"
    "ds_read_b64_tr_b16 v[62:63], v237 offset:30720\n"
    "v_mfma_f32_16x16x32_bf16 v[154:157], v[26:29], v[10:13], v[154:157]\n"
    "ds_read_b128 v[50:53], v223 offset:49152\n"
    "v_mfma_f32_16x16x32_bf16 v[174:177], v[10:13], v[30:33], v[174:177]\n"
    "ds_read_b64_tr_b16 v[64:65], v237 offset:31232\n"
    "v_mfma_f32_16x16x32_bf16 v[142:145], v[22:25], v[14:17], v[142:145]\n"
    "v_mfma_f32_16x16x32_bf16 v[158:161], v[26:29], v[14:17], v[158:161]\n"
    "v_mfma_f32_16x16x32_bf16 v[178:181], v[14:17], v[30:33], v[178:181]\n"
    "v_mfma_f32_16x16x32_bf16 v[162:165], v[26:29], v[18:21], v[162:165]\n"
    "v_mfma_f32_16x16x32_bf16 v[182:185], v[18:21], v[30:33], v[182:185]\n"
    "s_waitcnt lgkmcnt(0)\n"
    "s_barrier\n"
    "v_mfma_f32_16x16x32_bf16 v[130:133], v[54:57], v[34:37], v[130:133]\n"
    "ds_read_b128 v[2:5], v221 offset:51200\n"
    "ds_read_b64_tr_b16 v[22:23], v237 offset:40960\n"
    "v_mfma_f32_16x16x32_bf16 v[146:149], v[58:61], v[34:37], v[146:149]\n"
    "ds_read_b64_tr_b16 v[24:25], v237 offset:41472\n"
    "v_mfma_f32_16x16x32_bf16 v[166:169], v[34:37], v[62:65], v[166:169]\n"
    "ds_read_b128 v[6:9], v223 offset:53248\n"
    "v_mfma_f32_16x16x32_bf16 v[134:137], v[54:57], v[38:41], v[134:137]\n"
    "ds_read_b64_tr_b16 v[26:27], v237 offset:41984\n"
    "v_mfma_f32_16x16x32_bf16 v[150:153], v[58:61], v[38:41], v[150:153]\n"
    "ds_read_b128 v[10:13], v221 offset:55296\n"
    "ds_read_b64_tr_b16 v[28:29], v237 offset:42496\n"
    "v_mfma_f32_16x16x32_bf16 v[170:173], v[38:41], v[62:65], v[170:173]\n"
    "ds_read_b128 v[14:17], v223 offset:57344\n"
    "v_mfma_f32_16x16x32_bf16 v[138:141], v[54:57], v[42:45], v[138:141]\n"
    "ds_read_b64_tr_b16 v[30:31], v237 offset:43008\n"
    "v_mfma_f32_16x16x32_bf16 v[154:157], v[58:61], v[42:45], v[154:157]\n"
    "ds_read_b128 v[18:21], v221 offset:59392\n"
    "v_mfma_f32_16x16x32_bf16 v[174:177], v[42:45], v[62:65], v[174:177]\n"
    "ds_read_b64_tr_b16 v[32:33], v237 offset:43520\n"
    "v_mfma_f32_16x16x32_bf16 v[142:145], v[54:57], v[46:49], v[142:145]\n"
    "v_mfma_f32_16x16x32_bf16 v[158:161], v[58:61], v[46:49], v[158:161]\n"
    "v_mfma_f32_16x16x32_bf16 v[178:181], v[46:49], v[62:65], v[178:181]\n"
    "v_mfma_f32_16x16x32_bf16 v[162:165], v[58:61], v[50:53], v[162:165]\n"
    "v_mfma_f32_16x16x32_bf16 v[182:185], v[50:53], v[62:65], v[182:185]\n"
    "s_waitcnt lgkmcnt(0)\n"
    "v_mfma_f32_16x16x32_bf16 v[130:133], v[22:25], v[2:5], v[130:133]\n"
    "ds_read_b128 v[34:37], v223 offset:51200\n"
    "ds_read_b64_tr_b16 v[54:55], v237 offset:53248\n"
    "v_mfma_f32_16x16x32_bf16 v[146:149], v[26:29], v[2:5], v[146:149]\n"
    "ds_read_b64_tr_b16 v[56:57], v237 offset:53760\n"
    "v_mfma_f32_16x16x32_bf16 v[166:169], v[2:5], v[30:33], v[166:169]\n"
    "ds_read_b128 v[38:41], v221 offset:53248\n"
    "v_mfma_f32_16x16x32_bf16 v[134:137], v[22:25], v[6:9], v[134:137]\n"
    "ds_read_b64_tr_b16 v[58:59], v237 offset:54272\n"
    "v_mfma_f32_16x16x32_bf16 v[150:153], v[26:29], v[6:9], v[150:153]\n"
    "ds_read_b128 v[42:45], v223 offset:55296\n"
    "ds_read_b64_tr_b16 v[60:61], v237 offset:54784\n"
    "v_mfma_f32_16x16x32_bf16 v[170:173], v[6:9], v[30:33], v[170:173]\n"
    "ds_read_b128 v[46:49], v221 offset:57344\n"
    "v_mfma_f32_16x16x32_bf16 v[138:141], v[22:25], v[10:13], v[138:141]\n"
    "ds_read_b64_tr_b16 v[62:63], v237 offset:55296\n"
    "v_mfma_f32_16x16x32_bf16 v[154:157], v[26:29], v[10:13], v[154:157]\n"
    "ds_read_b128 v[50:53], v223 offset:59392\n"
    "v_mfma_f32_16x16x32_bf16 v[174:177], v[10:13], v[30:33], v[174:177]\n"
    "ds_read_b64_tr_b16 v[64:65], v237 offset:55808\n"
    "v_mfma_f32_16x16x32_bf16 v[142:145], v[22:25], v[14:17], v[142:145]\n"
    "v_mfma_f32_16x16x32_bf16 v[158:161], v[26:29], v[14:17], v[158:161]\n"
    "v_mfma_f32_16x16x32_bf16 v[178:181], v[14:17], v[30:33], v[178:181]\n"
    "v_mfma_f32_16x16x32_bf16 v[162:165], v[26:29], v[18:21], v[162:165]\n"
    "v_mfma_f32_16x16x32_bf16 v[182:185], v[18:21], v[30:33], v[182:185]\n"
    "s_waitcnt lgkmcnt(0)\n"
    "s_barrier\n"
    "v_mfma_f32_16x16x32_bf16 v[130:133], v[54:57], v[34:37], v[130:133]\n"
    "ds_read_b128 v[2:5], v221 offset:61440\n"
    "ds_read_b64_tr_b16 v[22:23], v242 offset:0\n"
    "v_mfma_f32_16x16x32_bf16 v[146:149], v[58:61], v[34:37], v[146:149]\n"
    "ds_read_b64_tr_b16 v[24:25], v242 offset:512\n"
    "v_mfma_f32_16x16x32_bf16 v[166:169], v[34:37], v[62:65], v[166:169]\n"
    "ds_read_b128 v[6:9], v223 offset:63488\n"
    "v_mfma_f32_16x16x32_bf16 v[134:137], v[54:57], v[38:41], v[134:137]\n"
    "ds_read_b64_tr_b16 v[26:27], v242 offset:1024\n"
    "v_mfma_f32_16x16x32_bf16 v[150:153], v[58:61], v[38:41], v[150:153]\n"
    "ds_read_b128 v[10:13], v222 offset:0\n"
    "ds_read_b64_tr_b16 v[28:29], v242 offset:1536\n"
    "v_mfma_f32_16x16x32_bf16 v[170:173], v[38:41], v[62:65], v[170:173]\n"
    "ds_read_b128 v[14:17], v224 offset:2048\n"
    "v_mfma_f32_16x16x32_bf16 v[138:141], v[54:57], v[42:45], v[138:141]\n"
    "ds_read_b64_tr_b16 v[30:31], v242 offset:2048\n"
    "v_mfma_f32_16x16x32_bf16 v[154:157], v[58:61], v[42:45], v[154:157]\n"
    "ds_read_b128 v[18:21], v222 offset:4096\n"
    "v_mfma_f32_16x16x32_bf16 v[174:177], v[42:45], v[62:65], v[174:177]\n"
    "ds_read_b64_tr_b16 v[32:33], v242 offset:2560\n"
    "v_mfma_f32_16x16x32_bf16 v[142:145], v[54:57], v[46:49], v[142:145]\n"
    "v_mfma_f32_16x16x32_bf16 v[158:161], v[58:61], v[46:49], v[158:161]\n"
    "v_mfma_f32_16x16x32_bf16 v[178:181], v[46:49], v[62:65], v[178:181]\n"
    "v_mfma_f32_16x16x32_bf16 v[162:165], v[58:61], v[50:53], v[162:165]\n"
    "v_mfma_f32_16x16x32_bf16 v[182:185], v[50:53], v[62:65], v[182:185]\n"
    "s_waitcnt lgkmcnt(0)\n"
    "v_mfma_f32_16x16x32_bf16 v[130:133], v[22:25], v[2:5], v[130:133]\n"
    "ds_read_b128 v[34:37], v223 offset:61440\n"
    "ds_read_b64_tr_b16 v[54:55], v242 offset:12288\n"
    "v_mfma_f32_16x16x32_bf16 v[146:149], v[26:29], v[2:5], v[146:149]\n"
    "ds_read_b64_tr_b16 v[56:57], v242 offset:12800\n"
    "v_mfma_f32_16x16x32_bf16 v[166:169], v[2:5], v[30:33], v[166:169]\n"
    "ds_read_b128 v[38:41], v221 offset:63488\n"
    "v_mfma_f32_16x16x32_bf16 v[134:137], v[22:25], v[6:9], v[134:137]\n"
    "ds_read_b64_tr_b16 v[58:59], v242 offset:13312\n"
    "v_mfma_f32_16x16x32_bf16 v[150:153], v[26:29], v[6:9], v[150:153]\n"
    "ds_read_b128 v[42:45], v224 offset:0\n"
    "ds_read_b64_tr_b16 v[60:61], v242 offset:13824\n"
    "v_mfma_f32_16x16x32_bf16 v[170:173], v[6:9], v[30:33], v[170:173]\n"
    "ds_read_b128 v[46:49], v222 offset:2048\n"
    "v_mfma_f32_16x16x32_bf16 v[138:141], v[22:25], v[10:13], v[138:141]\n"
    "ds_read_b64_tr_b16 v[62:63], v242 offset:14336\n"
    "v_mfma_f32_16x16x32_bf16 v[154:157], v[26:29], v[10:13], v[154:157]\n"
    "ds_read_b128 v[50:53], v224 offset:4096\n"
    "v_mfma_f32_16x16x32_bf16 v[174:177], v[10:13], v[30:33], v[174:177]\n"
    "ds_read_b64_tr_b16 v[64:65], v242 offset:14848\n"
    "v_mfma_f32_16x16x32_bf16 v[142:145], v[22:25], v[14:17], v[142:145]\n"
    "v_mfma_f32_16x16x32_bf16 v[158:161], v[26:29], v[14:17], v[158:161]\n"
    "v_mfma_f32_16x16x32_bf16 v[178:181], v[14:17], v[30:33], v[178:181]\n"
    "v_mfma_f32_16x16x32_bf16 v[162:165], v[26:29], v[18:21], v[162:165]\n"
    "v_mfma_f32_16x16x32_bf16 v[182:185], v[18:21], v[30:33], v[182:185]\n"
    "s_waitcnt lgkmcnt(0)\n"
    "s_barrier\n"
    "v_mfma_f32_16x16x32_bf16 v[130:133], v[54:57], v[34:37], v[130:133]\n"
    "ds_read_b128 v[2:5], v222 offset:6144\n"
    "ds_read_b64_tr_b16 v[22:23], v237 offset:16384\n"
    "v_mfma_f32_16x16x32_bf16 v[146:149], v[58:61], v[34:37], v[146:149]\n"
    "ds_read_b64_tr_b16 v[24:25], v237 offset:16896\n"
    "v_mfma_f32_16x16x32_bf16 v[166:169], v[34:37], v[62:65], v[166:169]\n"
    "ds_read_b128 v[6:9], v224 offset:8192\n"
    "v_mfma_f32_16x16x32_bf16 v[134:137], v[54:57], v[38:41], v[134:137]\n"
    "ds_read_b64_tr_b16 v[26:27], v237 offset:17408\n"
    "v_mfma_f32_16x16x32_bf16 v[150:153], v[58:61], v[38:41], v[150:153]\n"
    "ds_read_b128 v[10:13], v222 offset:10240\n"
    "ds_read_b64_tr_b16 v[28:29], v237 offset:17920\n"
    "v_mfma_f32_16x16x32_bf16 v[170:173], v[38:41], v[62:65], v[170:173]\n"
    "ds_read_b128 v[14:17], v224 offset:12288\n"
    "v_mfma_f32_16x16x32_bf16 v[138:141], v[54:57], v[42:45], v[138:141]\n"
    "ds_read_b64_tr_b16 v[30:31], v237 offset:18432\n"
    "v_mfma_f32_16x16x32_bf16 v[154:157], v[58:61], v[42:45], v[154:157]\n"
    "ds_read_b128 v[18:21], v222 offset:14336\n"
    "v_mfma_f32_16x16x32_bf16 v[174:177], v[42:45], v[62:65], v[174:177]\n"
    "ds_read_b64_tr_b16 v[32:33], v237 offset:18944\n"
    "v_mfma_f32_16x16x32_bf16 v[142:145], v[54:57], v[46:49], v[142:145]\n"
    "v_mfma_f32_16x16x32_bf16 v[158:161], v[58:61], v[46:49], v[158:161]\n"
    "v_mfma_f32_16x16x32_bf16 v[178:181], v[46:49], v[62:65], v[178:181]\n"
    "v_mfma_f32_16x16x32_bf16 v[162:165], v[58:61], v[50:53], v[162:165]\n"
    "v_mfma_f32_16x16x32_bf16 v[182:185], v[50:53], v[62:65], v[182:185]\n"
    "s_waitcnt lgkmcnt(0)\n"
    "s_barrier\n"
    "v_mfma_f32_16x16x32_bf16 v[130:133], v[22:25], v[2:5], v[130:133]\n"
    "ds_read_b128 v[34:37], v224 offset:6144\n"
    "ds_read_b64_tr_b16 v[54:55], v237 offset:28672\n"
    "v_mfma_f32_16x16x32_bf16 v[146:149], v[26:29], v[2:5], v[146:149]\n"
    "ds_read_b64_tr_b16 v[56:57], v237 offset:29184\n"
    "v_mfma_f32_16x16x32_bf16 v[166:169], v[2:5], v[30:33], v[166:169]\n"
    "ds_read_b128 v[38:41], v222 offset:8192\n"
    "v_mfma_f32_16x16x32_bf16 v[134:137], v[22:25], v[6:9], v[134:137]\n"
    "ds_read_b64_tr_b16 v[58:59], v237 offset:29696\n"
    "v_mfma_f32_16x16x32_bf16 v[150:153], v[26:29], v[6:9], v[150:153]\n"
    "ds_read_b128 v[42:45], v224 offset:10240\n"
    "ds_read_b64_tr_b16 v[60:61], v237 offset:30208\n"
    "v_mfma_f32_16x16x32_bf16 v[170:173], v[6:9], v[30:33], v[170:173]\n"
    "ds_read_b128 v[46:49], v222 offset:12288\n"
    "v_mfma_f32_16x16x32_bf16 v[138:141], v[22:25], v[10:13], v[138:141]\n"
    "ds_read_b64_tr_b16 v[62:63], v237 offset:30720\n"
    "v_mfma_f32_16x16x32_bf16 v[154:157], v[26:29], v[10:13], v[154:157]\n"
    "ds_read_b128 v[50:53], v224 offset:14336\n"
    "v_mfma_f32_16x16x32_bf16 v[174:177], v[10:13], v[30:33], v[174:177]\n"
    "ds_read_b64_tr_b16 v[64:65], v237 offset:31232\n"
    "v_mfma_f32_16x16x32_bf16 v[142:145], v[22:25], v[14:17], v[142:145]\n"
    "v_mfma_f32_16x16x32_bf16 v[158:161], v[26:29], v[14:17], v[158:161]\n"
    "v_mfma_f32_16x16x32_bf16 v[178:181], v[14:17], v[30:33], v[178:181]\n"
    "v_mfma_f32_16x16x32_bf16 v[162:165], v[26:29], v[18:21], v[162:165]\n"
    "v_mfma_f32_16x16x32_bf16 v[182:185], v[18:21], v[30:33], v[182:185]\n"
    "s_waitcnt lgkmcnt(0)\n"
    "s_barrier\n"
    "s_waitcnt vmcnt(0)\n"
    "v_mfma_f32_16x16x32_bf16 v[130:133], v[54:57], v[34:37], v[130:133]\n"
    "v_mfma_f32_16x16x32_bf16 v[134:137], v[54:57], v[38:41], v[134:137]\n"
    "v_mfma_f32_16x16x32_bf16 v[138:141], v[54:57], v[42:45], v[138:141]\n"
    "v_mfma_f32_16x16x32_bf16 v[142:145], v[54:57], v[46:49], v[142:145]\n"
    "v_mfma_f32_16x16x32_bf16 v[146:149], v[58:61], v[34:37], v[146:149]\n"
    "v_mfma_f32_16x16x32_bf16 v[150:153], v[58:61], v[38:41], v[150:153]\n"
    "v_mfma_f32_16x16x32_bf16 v[154:157], v[58:61], v[42:45], v[154:157]\n"
    "v_lshlrev_b32 v200, 16, v200\n"
    "v_lshlrev_b32 v201, 16, v201\n"
    "v_mfma_f32_16x16x32_bf16 v[158:161], v[58:61], v[46:49], v[158:161]\n"
    "v_cvt_pk_bf16_f32 v70, v130, v131\n"
    "v_cvt_pk_bf16_f32 v71, v132, v133\n"
    "v_mfma_f32_16x16x32_bf16 v[162:165], v[58:61], v[50:53], v[162:165]\n"
    "v_cvt_pk_bf16_f32 v72, v134, v135\n"
    "v_cvt_pk_bf16_f32 v73, v136, v137\n"
    "v_mfma_f32_16x16x32_bf16 v[166:169], v[34:37], v[62:65], v[166:169]\n"
    "v_cvt_pk_bf16_f32 v74, v138, v139\n"
    "v_cvt_pk_bf16_f32 v75, v140, v141\n"
    "v_mfma_f32_16x16x32_bf16 v[170:173], v[38:41], v[62:65], v[170:173]\n"
    "v_cvt_pk_bf16_f32 v76, v142, v143\n"
    "v_cvt_pk_bf16_f32 v77, v144, v145\n"
    "v_mfma_f32_16x16x32_bf16 v[174:177], v[42:45], v[62:65], v[174:177]\n"
    "v_mfma_f32_16x16x32_bf16 v[178:181], v[46:49], v[62:65], v[178:181]\n"
    "v_mfma_f32_16x16x32_bf16 v[182:185], v[50:53], v[62:65], v[182:185]\n"
    "v_cvt_pk_bf16_f32 v80, v146, v147\n"
    "v_cvt_pk_bf16_f32 v81, v148, v149\n"
    "v_cvt_pk_bf16_f32 v82, v150, v151\n"
    "v_cvt_pk_bf16_f32 v83, v152, v153\n"
    "v_cvt_pk_bf16_f32 v84, v154, v155\n"
    "v_cvt_pk_bf16_f32 v85, v156, v157\n"
    "v_cvt_pk_bf16_f32 v86, v158, v159\n"
    "v_cvt_pk_bf16_f32 v87, v160, v161\n"
    "v_cvt_pk_bf16_f32 v78, v162, v163\n"
    "v_cvt_pk_bf16_f32 v79, v164, v165\n"
    "v_cvt_pk_bf16_f32 v90, v166, v167\n"
    "v_cvt_pk_bf16_f32 v91, v168, v169\n"
    "v_cvt_pk_bf16_f32 v92, v170, v171\n"
    "v_cvt_pk_bf16_f32 v93, v172, v173\n"
    "v_cvt_pk_bf16_f32 v94, v174, v175\n"
    "v_cvt_pk_bf16_f32 v95, v176, v177\n"
    "v_cvt_pk_bf16_f32 v96, v178, v179\n"
    "v_cvt_pk_bf16_f32 v97, v180, v181\n"
    "v_cvt_pk_bf16_f32 v88, v182, v183\n"
    "v_cvt_pk_bf16_f32 v89, v184, v185\n"
    "v_mfma_f32_16x16x16_bf16 v[130:133], v[78:79], v[70:71], v[248:251]\n"
    "v_mfma_f32_16x16x16_bf16 v[146:149], v[80:81], v[70:71], v[244:247]\n"
    "v_mfma_f32_16x16x16_bf16 v[134:137], v[80:81], v[72:73], v[226:229]\n"
    "v_mfma_f32_16x16x16_bf16 v[150:153], v[82:83], v[72:73], v[244:247]\n"
    "v_mfma_f32_16x16x16_bf16 v[138:141], v[82:83], v[74:75], v[226:229]\n"
    "v_mfma_f32_16x16x16_bf16 v[154:157], v[84:85], v[74:75], v[244:247]\n"
    "v_mfma_f32_16x16x16_bf16 v[142:145], v[84:85], v[76:77], v[226:229]\n"
    "v_mfma_f32_16x16x16_bf16 v[158:161], v[86:87], v[76:77], v[244:247]\n"
    "v_mov_b32 v62, 0x3eb8aa3b\n"
    "v_mov_b32 v63, 0x3eb8aa3b\n"
    "v_max3_f32 v50, v130, v131, v132\n"
    "v_max3_f32 v52, v134, v135, v136\n"
    "s_nop 0\n"
    "v_max3_f32 v54, v138, v139, v140\n"
    "s_nop 0\n"
    "v_max3_f32 v56, v142, v143, v144\n"
    "v_max3_f32 v50, v50, v133, v146\n"
    "v_max3_f32 v52, v52, v137, v150\n"
    "v_max3_f32 v54, v54, v141, v154\n"
    "v_max3_f32 v56, v56, v145, v158\n"
    "v_max3_f32 v50, v50, v147, v148\n"
    "v_max3_f32 v52, v52, v151, v152\n"
    "v_max3_f32 v54, v54, v155, v156\n"
    "v_max3_f32 v56, v56, v159, v160\n"
    "v_max_f32 v50, v50, v149\n"
    "v_max_f32 v52, v52, v153\n"
    "v_max_f32 v54, v54, v157\n"
    "v_max_f32 v56, v56, v161\n"
    "v_mov_b32 v58, v50\n"
    "v_mov_b32 v59, v52\n"
    "v_mov_b32 v60, v54\n"
    "v_mov_b32 v61, v56\n"
    "v_permlane16_swap_b32 v50, v58\n"
    "v_permlane16_swap_b32 v52, v59\n"
    "v_permlane16_swap_b32 v54, v60\n"
    "v_permlane16_swap_b32 v56, v61\n"
    "v_max_f32 v50, v50, v58\n"
    "v_max_f32 v52, v52, v59\n"
    "v_max_f32 v54, v54, v60\n"
    "v_max_f32 v56, v56, v61\n"
    "v_mov_b32 v58, v50\n"
    "v_mov_b32 v59, v52\n"
    "v_mov_b32 v60, v54\n"
    "v_mov_b32 v61, v56\n"
    "v_permlane32_swap_b32 v50, v58\n"
    "v_permlane32_swap_b32 v52, v59\n"
    "v_permlane32_swap_b32 v54, v60\n"
    "v_permlane32_swap_b32 v56, v61\n"
    "v_max_f32 v50, v50, v58\n"
    "v_max_f32 v52, v52, v59\n"
    "v_max_f32 v54, v54, v60\n"
    "v_max_f32 v56, v56, v61\n"
    "v_mul_f32 v50, 0xbeb8aa3b, v50\n"
    "v_mul_f32 v52, 0xbeb8aa3b, v52\n"
    "v_mul_f32 v54, 0xbeb8aa3b, v54\n"
    "v_mul_f32 v56, 0xbeb8aa3b, v56\n"
    "v_pk_fma_f32 v[130:131], v[130:131], v[62:63], v[50:51] op_sel_hi:[1,1,0]\n"
    "v_pk_fma_f32 v[132:133], v[132:133], v[62:63], v[50:51] op_sel_hi:[1,1,0]\n"
    "v_pk_fma_f32 v[146:147], v[146:147], v[62:63], v[50:51] op_sel_hi:[1,1,0]\n"
    "v_pk_fma_f32 v[148:149], v[148:149], v[62:63], v[50:51] op_sel_hi:[1,1,0]\n"
    "v_pk_fma_f32 v[134:135], v[134:135], v[62:63], v[52:53] op_sel_hi:[1,1,0]\n"
    "v_pk_fma_f32 v[136:137], v[136:137], v[62:63], v[52:53] op_sel_hi:[1,1,0]\n"
    "v_pk_fma_f32 v[150:151], v[150:151], v[62:63], v[52:53] op_sel_hi:[1,1,0]\n"
    "v_pk_fma_f32 v[152:153], v[152:153], v[62:63], v[52:53] op_sel_hi:[1,1,0]\n"
    "v_pk_fma_f32 v[138:139], v[138:139], v[62:63], v[54:55] op_sel_hi:[1,1,0]\n"
    "v_pk_fma_f32 v[140:141], v[140:141], v[62:63], v[54:55] op_sel_hi:[1,1,0]\n"
    "v_pk_fma_f32 v[154:155], v[154:155], v[62:63], v[54:55] op_sel_hi:[1,1,0]\n"
    "v_pk_fma_f32 v[156:157], v[156:157], v[62:63], v[54:55] op_sel_hi:[1,1,0]\n"
    "v_pk_fma_f32 v[142:143], v[142:143], v[62:63], v[56:57] op_sel_hi:[1,1,0]\n"
    "v_pk_fma_f32 v[144:145], v[144:145], v[62:63], v[56:57] op_sel_hi:[1,1,0]\n"
    "v_pk_fma_f32 v[158:159], v[158:159], v[62:63], v[56:57] op_sel_hi:[1,1,0]\n"
    "v_pk_fma_f32 v[160:161], v[160:161], v[62:63], v[56:57] op_sel_hi:[1,1,0]\n"
    "v_exp_f32 v130, v130\n"
    "v_exp_f32 v131, v131\n"
    "v_exp_f32 v132, v132\n"
    "v_exp_f32 v133, v133\n"
    "v_exp_f32 v146, v146\n"
    "v_exp_f32 v147, v147\n"
    "v_exp_f32 v148, v148\n"
    "v_exp_f32 v149, v149\n"
    "v_exp_f32 v134, v134\n"
    "v_exp_f32 v135, v135\n"
    "v_exp_f32 v136, v136\n"
    "v_exp_f32 v137, v137\n"
    "v_exp_f32 v150, v150\n"
    "v_exp_f32 v151, v151\n"
    "v_exp_f32 v152, v152\n"
    "v_exp_f32 v153, v153\n"
    "v_exp_f32 v138, v138\n"
    "v_exp_f32 v139, v139\n"
    "v_exp_f32 v140, v140\n"
    "v_exp_f32 v141, v141\n"
    "v_exp_f32 v154, v154\n"
    "v_exp_f32 v155, v155\n"
    "v_exp_f32 v156, v156\n"
    "v_exp_f32 v157, v157\n"
    "v_exp_f32 v142, v142\n"
    "v_exp_f32 v143, v143\n"
    "v_exp_f32 v144, v144\n"
    "v_exp_f32 v145, v145\n"
    "v_exp_f32 v158, v158\n"
    "v_exp_f32 v159, v159\n"
    "v_exp_f32 v160, v160\n"
    "v_exp_f32 v161, v161\n"
    "v_cvt_pk_bf16_f32 v2, v130, v131\n"
    "v_cvt_pk_bf16_f32 v3, v132, v133\n"
    "v_cvt_pk_bf16_f32 v4, v146, v147\n"
    "v_cvt_pk_bf16_f32 v5, v148, v149\n"
    "v_cvt_pk_bf16_f32 v6, v134, v135\n"
    "v_cvt_pk_bf16_f32 v7, v136, v137\n"
    "v_cvt_pk_bf16_f32 v8, v150, v151\n"
    "v_cvt_pk_bf16_f32 v9, v152, v153\n"
    "v_cvt_pk_bf16_f32 v10, v138, v139\n"
    "v_cvt_pk_bf16_f32 v11, v140, v141\n"
    "v_cvt_pk_bf16_f32 v12, v154, v155\n"
    "v_cvt_pk_bf16_f32 v13, v156, v157\n"
    "v_cvt_pk_bf16_f32 v14, v142, v143\n"
    "v_cvt_pk_bf16_f32 v15, v144, v145\n"
    "v_cvt_pk_bf16_f32 v16, v158, v159\n"
    "v_cvt_pk_bf16_f32 v17, v160, v161\n"
    "v_mfma_f32_16x16x32_bf16 v[18:21], v[210:213], v[2:5], 0\n"
    "v_mfma_f32_16x16x32_bf16 v[22:25], v[210:213], v[6:9], 0\n"
    "v_mfma_f32_16x16x32_bf16 v[26:29], v[210:213], v[10:13], 0\n"
    "v_mfma_f32_16x16x32_bf16 v[30:33], v[210:213], v[14:17], 0\n"
    "v_mfma_f32_16x16x32_bf16 v[166:169], v[88:91], v[2:5], 0\n"
    "v_mfma_f32_16x16x32_bf16 v[170:173], v[90:93], v[6:9], 0\n"
    "v_mfma_f32_16x16x32_bf16 v[174:177], v[92:95], v[10:13], 0\n"
    "v_mfma_f32_16x16x32_bf16 v[178:181], v[94:97], v[14:17], 0\n"
    "s_lshl_b32 s62, s46, 16\n"
    "s_lshl_b32 s63, s47, 13\n"
    "s_add_u32 s62, s62, s63\n"
    "s_add_u32 s62, s62, s52\n"
    "s_add_u32 s62, s62, 0x900000\n"
    "v_rcp_f32 v50, v18\n"
    "v_rcp_f32 v52, v22\n"
    "v_rcp_f32 v54, v26\n"
    "v_rcp_f32 v56, v30\n"
    "v_pk_mul_f32 v[166:167], v[166:167], v[50:51] op_sel_hi:[1,0]\n"
    "v_pk_mul_f32 v[168:169], v[168:169], v[50:51] op_sel_hi:[1,0]\n"
    "v_pk_mul_f32 v[170:171], v[170:171], v[52:53] op_sel_hi:[1,0]\n"
    "v_pk_mul_f32 v[172:173], v[172:173], v[52:53] op_sel_hi:[1,0]\n"
    "v_pk_mul_f32 v[174:175], v[174:175], v[54:55] op_sel_hi:[1,0]\n"
    "v_pk_mul_f32 v[176:177], v[176:177], v[54:55] op_sel_hi:[1,0]\n"
    "v_pk_mul_f32 v[178:179], v[178:179], v[56:57] op_sel_hi:[1,0]\n"
    "v_pk_mul_f32 v[180:181], v[180:181], v[56:57] op_sel_hi:[1,0]\n"
    "v_cvt_pk_bf16_f32 v78, v166, v167\n"
    "v_cvt_pk_bf16_f32 v79, v168, v169\n"
    "buffer_store_dwordx2 v[78:79], v233, s[36:39], s62 offen offset:0 sc1\n"
    "v_cvt_pk_bf16_f32 v80, v170, v171\n"
    "v_cvt_pk_bf16_f32 v81, v172, v173\n"
    "buffer_store_dwordx2 v[80:81], v233, s[36:39], s62 offen offset:512 sc1\n"
    "v_cvt_pk_bf16_f32 v82, v174, v175\n"
    "v_cvt_pk_bf16_f32 v83, v176, v177\n"
    "buffer_store_dwordx2 v[82:83], v233, s[36:39], s62 offen offset:1024 sc1\n"
    "v_cvt_pk_bf16_f32 v84, v178, v179\n"
    "v_cvt_pk_bf16_f32 v85, v180, v181\n"
    "buffer_store_dwordx2 v[84:85], v233, s[36:39], s62 offen offset:1536 sc1\n"
    "s_lshl_b32 s51, s55, 13\n"
    "v_add_u32 v210, s51, v209\n"
    "v_add_u32 v211, 0x10000, v210\n"
    "v_add_u32 v212, s51, v218\n"
    "v_add_u32 v213, 0x10000, v212\n"
    "v_add_u32 v221, 0x10000, v234\n"
    "v_add_u32 v222, 0x20000, v234\n"
    "v_mul_f32 v90, s96, v200\n"
    "v_mov_b32 v82, v200\n"
    "v_mov_b32 v83, v200\n"
    "v_mov_b32 v84, v200\n"
    "v_mov_b32 v85, v200\n"
    "v_mul_f32 v94, s96, v201\n"
    "v_mov_b32 v86, v201\n"
    "v_mov_b32 v87, v201\n"
    "v_mov_b32 v88, v201\n"
    "v_mov_b32 v89, v201\n"
    "v_mov_b32 v91, v90\n"
    "v_mov_b32 v92, v90\n"
    "v_mov_b32 v93, v90\n"
    "v_mov_b32 v95, v94\n"
    "v_mov_b32 v96, v94\n"
    "v_mov_b32 v97, v94\n"
    "s_barrier\n"
    "ds_read_b128 v[18:21], v209 offset:0\n"
    "ds_read_b128 v[2:5], v230 offset:45056\n"
    "ds_read_b128 v[22:25], v218 offset:2048\n"
    "ds_read_b128 v[6:9], v230 offset:46080\n"
    "ds_read_b128 v[26:29], v209 offset:8192\n"
    "ds_read_b128 v[10:13], v230 offset:47104\n"
    "ds_read_b128 v[30:33], v218 offset:10240\n"
    "ds_read_b128 v[14:17], v221 offset:48128\n"
    "ds_read_b128 v[34:37], v210 offset:0\n"
    "ds_read_b128 v[38:41], v212 offset:2048\n"
    "s_waitcnt lgkmcnt(0)\n"
    "v_mfma_f32_16x16x32_bf16 v[162:165], v[2:5], v[26:29], 0\n"
    "ds_read_b128 v[58:61], v218 offset:0\n"
    "ds_read_b128 v[42:45], v230 offset:52224\n"
    "v_mfma_f32_16x16x32_bf16 v[166:169], v[2:5], v[30:33], 0\n"
    "ds_read_b128 v[62:65], v209 offset:2048\n"
    "v_mfma_f32_16x16x32_bf16 v[170:173], v[6:9], v[26:29], 0\n"
    "ds_read_b128 v[46:49], v230 offset:53248\n"
    "v_mfma_f32_16x16x32_bf16 v[174:177], v[6:9], v[30:33], 0\n"
    "ds_read_b128 v[66:69], v218 offset:8192\n"
    "v_mfma_f32_16x16x32_bf16 v[178:181], v[10:13], v[26:29], 0\n"
    "ds_read_b128 v[50:53], v230 offset:54272\n"
    "v_mfma_f32_16x16x32_bf16 v[182:185], v[10:13], v[30:33], 0\n"
    "ds_read_b128 v[70:73], v209 offset:10240\n"
    "v_mfma_f32_16x16x32_bf16 v[154:157], v[14:17], v[34:37], v[90:93]\n"
    "ds_read_b128 v[54:57], v221 offset:55296\n"
    "v_mfma_f32_16x16x32_bf16 v[158:161], v[14:17], v[38:41], v[94:97]\n"
    "ds_read_b128 v[74:77], v212 offset:0\n"
    "v_mfma_f32_16x16x32_bf16 v[130:133], v[2:5], v[18:21], v[82:85]\n"
    "ds_read_b128 v[78:81], v210 offset:2048\n"
    "v_mfma_f32_16x16x32_bf16 v[134:137], v[2:5], v[22:25], v[86:89]\n"
    "v_mfma_f32_16x16x32_bf16 v[138:141], v[6:9], v[18:21], v[82:85]\n"
    "v_mfma_f32_16x16x32_bf16 v[142:145], v[6:9], v[22:25], v[86:89]\n"
    "v_mfma_f32_16x16x32_bf16 v[146:149], v[10:13], v[18:21], v[82:85]\n"
    "v_mfma_f32_16x16x32_bf16 v[150:153], v[10:13], v[22:25], v[86:89]\n"
    "s_waitcnt lgkmcnt(0)\n"
    "s_barrier\n"
    "v_mfma_f32_16x16x32_bf16 v[162:165], v[42:45], v[66:69], v[162:165]\n"
    "ds_read_b128 v[18:21], v209 offset:16384\n"
    "ds_read_b128 v[2:5], v230 offset:59392\n"
    "v_mfma_f32_16x16x32_bf16 v[166:169], v[42:45], v[70:73], v[166:169]\n"
    "ds_read_b128 v[22:25], v218 offset:18432\n"
    "s_lshl_b32 s63, s55, 12\n"
    "s_lshl_b32 s62, s56, 10\n"
    "v_mfma_f32_16x16x32_bf16 v[170:173], v[46:49], v[66:69], v[170:173]\n"
    "ds_read_b128 v[6:9], v230 offset:60416\n"
    "s_add_u32 s62, s62, s63\n"
    "v_mfma_f32_16x16x32_bf16 v[174:177], v[46:49], v[70:73], v[174:177]\n"
    "ds_read_b128 v[26:29], v209 offset:24576\n"
    "v_add_u32 v223, s62, v233\n"
    "v_mfma_f32_16x16x32_bf16 v[178:181], v[50:53], v[66:69], v[178:181]\n"
    "ds_read_b128 v[10:13], v230 offset:61440\n"
    "v_add_u32 v224, 0x10000, v223\n"
    "v_mfma_f32_16x16x32_bf16 v[182:185], v[50:53], v[70:73], v[182:185]\n"
    "ds_read_b128 v[30:33], v218 offset:26624\n"
    "v_add_u32 v226, 0x20000, v223\n"
    "v_mfma_f32_16x16x32_bf16 v[154:157], v[54:57], v[74:77], v[154:157]\n"
    "ds_read_b128 v[14:17], v221 offset:62464\n"
    "v_bfe_u32 v206, v0, 3, 2\n"
    "v_mfma_f32_16x16x32_bf16 v[158:161], v[54:57], v[78:81], v[158:161]\n"
    "ds_read_b128 v[34:37], v210 offset:16384\n"
    "v_bfe_u32 v207, v0, 2, 4\n"
    "v_mfma_f32_16x16x32_bf16 v[130:133], v[42:45], v[58:61], v[130:133]\n"
    "ds_read_b128 v[38:41], v212 offset:18432\n"
    "v_lshlrev_b32 v207, 7, v207\n"
    "v_mfma_f32_16x16x32_bf16 v[134:137], v[42:45], v[62:65], v[134:137]\n"
    "v_lshl_or_b32 v207, v205, 3, v207\n"
    "v_lshl_or_b32 v207, v206, 5, v207\n"
    "v_mfma_f32_16x16x32_bf16 v[138:141], v[46:49], v[58:61], v[138:141]\n"
    "v_add_u32 v207, s63, v207\n"
    "v_xor_b32 v227, 0, v207\n"
    "v_mfma_f32_16x16x32_bf16 v[142:145], v[46:49], v[62:65], v[142:145]\n"
    "v_add_u32 v228, 0x10000, v227\n"
    "v_add_u32 v229, 0x20000, v227\n"
    "v_mfma_f32_16x16x32_bf16 v[146:149], v[50:53], v[58:61], v[146:149]\n"
    "v_xor_b32 v232, 32, v207\n"
    "v_add_u32 v235, 0x10000, v232\n"
    "v_mfma_f32_16x16x32_bf16 v[150:153], v[50:53], v[62:65], v[150:153]\n"
    "v_add_u32 v236, 0x20000, v232\n"
    "v_xor_b32 v237, 64, v207\n"
    "s_waitcnt lgkmcnt(0)\n"
    "v_mfma_f32_16x16x32_bf16 v[162:165], v[2:5], v[26:29], v[162:165]\n"
    "ds_read_b128 v[58:61], v218 offset:16384\n"
    "ds_read_b128 v[42:45], v231 offset:1024\n"
    "v_mfma_f32_16x16x32_bf16 v[166:169], v[2:5], v[30:33], v[166:169]\n"
    "ds_read_b128 v[62:65], v209 offset:18432\n"
    "v_add_u32 v242, 0x10000, v237\n"
    "v_mfma_f32_16x16x32_bf16 v[170:173], v[6:9], v[26:29], v[170:173]\n"
    "ds_read_b128 v[46:49], v231 offset:2048\n"
    "v_add_u32 v243, 0x20000, v237\n"
    "v_mfma_f32_16x16x32_bf16 v[174:177], v[6:9], v[30:33], v[174:177]\n"
    "ds_read_b128 v[66:69], v218 offset:24576\n"
    "v_xor_b32 v244, 96, v207\n"
    "v_mfma_f32_16x16x32_bf16 v[178:181], v[10:13], v[26:29], v[178:181]\n"
    "ds_read_b128 v[50:53], v231 offset:3072\n"
    "v_add_u32 v245, 0x10000, v244\n"
    "v_mfma_f32_16x16x32_bf16 v[182:185], v[10:13], v[30:33], v[182:185]\n"
    "ds_read_b128 v[70:73], v209 offset:26624\n"
    "v_add_u32 v246, 0x20000, v244\n"
    "v_mfma_f32_16x16x32_bf16 v[154:157], v[14:17], v[34:37], v[154:157]\n"
    "ds_read_b128 v[54:57], v222 offset:4096\n"
    "s_lshl_b32 s62, s55, 14\n"
    "s_lshl_b32 s63, s56, 13\n"
    "v_mfma_f32_16x16x32_bf16 v[158:161], v[14:17], v[38:41], v[158:161]\n"
    "ds_read_b128 v[74:77], v212 offset:16384\n"
    "s_add_u32 s62, s62, s63\n"
    "v_mfma_f32_16x16x32_bf16 v[130:133], v[2:5], v[18:21], v[130:133]\n"
    "ds_read_b128 v[78:81], v210 offset:18432\n"
    "v_xor_b32 v206, v203, v202\n"
    "v_mfma_f32_16x16x32_bf16 v[134:137], v[2:5], v[22:25], v[134:137]\n"
    "v_lshlrev_b32 v206, 4, v206\n"
    "v_lshl_or_b32 v206, v202, 8, v206\n"
    "v_mfma_f32_16x16x32_bf16 v[138:141], v[6:9], v[18:21], v[138:141]\n"
    "v_add_u32 v206, s62, v206\n"
    "v_xor_b32 v198, 0, v206\n"
    "v_mfma_f32_16x16x32_bf16 v[142:145], v[6:9], v[22:25], v[142:145]\n"
    "v_xor_b32 v199, 64, v206\n"
    "v_xor_b32 v200, 128, v206\n"
    "v_mfma_f32_16x16x32_bf16 v[146:149], v[10:13], v[18:21], v[146:149]\n"
    "v_xor_b32 v201, 192, v206\n"
    "v_bfe_u32 v247, v0, 3, 6\n"
    "v_mfma_f32_16x16x32_bf16 v[150:153], v[10:13], v[22:25], v[150:153]\n"
    "v_and_b32 v206, 7, v0\n"
    "v_lshlrev_b32 v206, 4, v206\n"
    "s_waitcnt lgkmcnt(0)\n"
    "s_barrier\n"
    "v_mfma_f32_16x16x32_bf16 v[162:165], v[42:45], v[66:69], v[162:165]\n"
    "ds_read_b128 v[18:21], v209 offset:32768\n"
    "ds_read_b128 v[2:5], v231 offset:8192\n"
    "v_mfma_f32_16x16x32_bf16 v[166:169], v[42:45], v[70:73], v[166:169]\n"
    "ds_read_b128 v[22:25], v218 offset:34816\n"
    "v_lshl_or_b32 v247, v247, 10, v206\n"
    "v_mfma_f32_16x16x32_bf16 v[170:173], v[46:49], v[66:69], v[170:173]\n"
    "ds_read_b128 v[6:9], v231 offset:9216\n"
    "s_lshl_b32 s62, s46, 16\n"
    "s_add_u32 s62, s62, s57\n"
    "v_mfma_f32_16x16x32_bf16 v[174:177], v[46:49], v[70:73], v[174:177]\n"
    "ds_read_b128 v[26:29], v209 offset:40960\n"
    "v_add_u32 v247, s62, v247\n"
    "v_mfma_f32_16x16x32_bf16 v[178:181], v[50:53], v[66:69], v[178:181]\n"
    "ds_read_b128 v[10:13], v231 offset:10240\n"
    "v_bfe_u32 v248, v0, 3, 6\n"
    "v_mfma_f32_16x16x32_bf16 v[182:185], v[50:53], v[70:73], v[182:185]\n"
    "ds_read_b128 v[30:33], v218 offset:43008\n"
    "v_and_b32 v249, 7, v0\n"
    "v_mfma_f32_16x16x32_bf16 v[154:157], v[54:57], v[74:77], v[154:157]\n"
    "ds_read_b128 v[14:17], v222 offset:11264\n"
    "v_lshlrev_b32 v249, 1, v249\n"
    "v_mfma_f32_16x16x32_bf16 v[158:161], v[54:57], v[78:81], v[158:161]\n"
    "ds_read_b128 v[34:37], v210 offset:32768\n"
    "v_and_b32 v250, 15, v248\n"
    "v_mfma_f32_16x16x32_bf16 v[130:133], v[42:45], v[58:61], v[130:133]\n"
    "ds_read_b128 v[38:41], v212 offset:34816\n"
    "v_xor_b32 v249, v249, v250\n"
    "v_mfma_f32_16x16x32_bf16 v[134:137], v[42:45], v[62:65], v[134:137]\n"
    "v_lshlrev_b32 v249, 4, v249\n"
    "v_lshl_or_b32 v248, v248, 8, v249\n"
    "v_mfma_f32_16x16x32_bf16 v[138:141], v[46:49], v[58:61], v[138:141]\n"
    "v_xor_b32 v249, 16, v248\n"
    "buffer_load_dwordx4 v[194:197], v247, s[4:7], 0 offen\n"
    "v_mfma_f32_16x16x32_bf16 v[142:145], v[46:49], v[62:65], v[142:145]\n"
    "v_mul_f32 v82, v98, v98\n"
    "v_mul_f32 v83, v99, v99\n"
    "v_mfma_f32_16x16x32_bf16 v[146:149], v[50:53], v[58:61], v[146:149]\n"
    "v_mul_f32 v84, v100, v100\n"
    "v_mul_f32 v85, v101, v101\n"
    "v_mfma_f32_16x16x32_bf16 v[150:153], v[50:53], v[62:65], v[150:153]\n"
    "v_mul_f32 v86, v102, v102\n"
    "v_mul_f32 v87, v103, v103\n"
    "s_waitcnt lgkmcnt(0)\n"
    "v_mfma_f32_16x16x32_bf16 v[162:165], v[2:5], v[26:29], v[162:165]\n"
    "ds_read_b128 v[58:61], v218 offset:32768\n"
    "ds_read_b128 v[42:45], v231 offset:15360\n"
    "v_mfma_f32_16x16x32_bf16 v[166:169], v[2:5], v[30:33], v[166:169]\n"
    "ds_read_b128 v[62:65], v209 offset:34816\n"
    "v_mul_f32 v88, v104, v104\n"
    "v_mfma_f32_16x16x32_bf16 v[170:173], v[6:9], v[26:29], v[170:173]\n"
    "ds_read_b128 v[46:49], v231 offset:16384\n"
    "v_mul_f32 v89, v105, v105\n"
    "v_mfma_f32_16x16x32_bf16 v[174:177], v[6:9], v[30:33], v[174:177]\n"
    "ds_read_b128 v[66:69], v218 offset:40960\n"
    "v_fma_f32 v82, v82, v240, v238\n"
    "v_mfma_f32_16x16x32_bf16 v[178:181], v[10:13], v[26:29], v[178:181]\n"
    "ds_read_b128 v[50:53], v231 offset:17408\n"
    "v_fma_f32 v83, v83, v240, v238\n"
    "v_mfma_f32_16x16x32_bf16 v[182:185], v[10:13], v[30:33], v[182:185]\n"
    "ds_read_b128 v[70:73], v209 offset:43008\n"
    "v_fma_f32 v84, v84, v240, v238\n"
    "v_mfma_f32_16x16x32_bf16 v[154:157], v[14:17], v[34:37], v[154:157]\n"
    "ds_read_b128 v[54:57], v222 offset:18432\n"
    "v_fma_f32 v85, v85, v240, v238\n"
    "v_mfma_f32_16x16x32_bf16 v[158:161], v[14:17], v[38:41], v[158:161]\n"
    "ds_read_b128 v[74:77], v212 offset:32768\n"
    "v_fma_f32 v86, v86, v240, v238\n"
    "v_mfma_f32_16x16x32_bf16 v[130:133], v[2:5], v[18:21], v[130:133]\n"
    "ds_read_b128 v[78:81], v210 offset:34816\n"
    "v_fma_f32 v87, v87, v240, v238\n"
    "v_mfma_f32_16x16x32_bf16 v[134:137], v[2:5], v[22:25], v[134:137]\n"
    "v_fma_f32 v88, v88, v240, v238\n"
    "v_fma_f32 v89, v89, v240, v238\n"
    "v_mfma_f32_16x16x32_bf16 v[138:141], v[6:9], v[18:21], v[138:141]\n"
    "v_mul_f32 v82, v82, v98\n"
    "v_mul_f32 v83, v83, v99\n"
    "v_mfma_f32_16x16x32_bf16 v[142:145], v[6:9], v[22:25], v[142:145]\n"
    "v_mul_f32 v84, v84, v100\n"
    "v_mul_f32 v85, v85, v101\n"
    "v_mfma_f32_16x16x32_bf16 v[146:149], v[10:13], v[18:21], v[146:149]\n"
    "v_mul_f32 v86, v86, v102\n"
    "v_mul_f32 v87, v87, v103\n"
    "v_mfma_f32_16x16x32_bf16 v[150:153], v[10:13], v[22:25], v[150:153]\n"
    "v_mul_f32 v88, v88, v104\n"
    "v_mul_f32 v89, v89, v105\n"
    "s_waitcnt lgkmcnt(0)\n"
    "s_barrier\n"
    "v_mfma_f32_16x16x32_bf16 v[162:165], v[42:45], v[66:69], v[162:165]\n"
    "ds_read_b128 v[18:21], v209 offset:49152\n"
    "ds_read_b128 v[2:5], v230 offset:16384\n"
    "v_mfma_f32_16x16x32_bf16 v[166:169], v[42:45], v[70:73], v[166:169]\n"
    "ds_read_b128 v[22:25], v218 offset:51200\n"
    "v_mfma_f32_16x16x32_bf16 v[170:173], v[46:49], v[66:69], v[170:173]\n"
    "ds_read_b128 v[6:9], v230 offset:17408\n"
    "v_exp_f32 v82, v82\n"
    "v_mfma_f32_16x16x32_bf16 v[174:177], v[46:49], v[70:73], v[174:177]\n"
    "ds_read_b128 v[26:29], v209 offset:57344\n"
    "v_mfma_f32_16x16x32_bf16 v[178:181], v[50:53], v[66:69], v[178:181]\n"
    "ds_read_b128 v[10:13], v230 offset:18432\n"
    "v_exp_f32 v83, v83\n"
    "v_mfma_f32_16x16x32_bf16 v[182:185], v[50:53], v[70:73], v[182:185]\n"
    "ds_read_b128 v[30:33], v218 offset:59392\n"
    "v_mfma_f32_16x16x32_bf16 v[154:157], v[54:57], v[74:77], v[154:157]\n"
    "ds_read_b128 v[14:17], v221 offset:19456\n"
    "v_exp_f32 v84, v84\n"
    "v_mfma_f32_16x16x32_bf16 v[158:161], v[54:57], v[78:81], v[158:161]\n"
    "ds_read_b128 v[34:37], v210 offset:49152\n"
    "v_mfma_f32_16x16x32_bf16 v[130:133], v[42:45], v[58:61], v[130:133]\n"
    "ds_read_b128 v[38:41], v212 offset:51200\n"
    "v_exp_f32 v85, v85\n"
    "v_mfma_f32_16x16x32_bf16 v[134:137], v[42:45], v[62:65], v[134:137]\n"
    "v_exp_f32 v86, v86\n"
    "v_mfma_f32_16x16x32_bf16 v[138:141], v[46:49], v[58:61], v[138:141]\n"
    "v_exp_f32 v87, v87\n"
    "v_mfma_f32_16x16x32_bf16 v[142:145], v[46:49], v[62:65], v[142:145]\n"
    "v_exp_f32 v88, v88\n"
    "v_mfma_f32_16x16x32_bf16 v[146:149], v[50:53], v[58:61], v[146:149]\n"
    "v_exp_f32 v89, v89\n"
    "v_mfma_f32_16x16x32_bf16 v[150:153], v[50:53], v[62:65], v[150:153]\n"
    "v_add_f32 v82, 1.0, v82\n"
    "v_add_f32 v83, 1.0, v83\n"
    "s_waitcnt lgkmcnt(0)\n"
    "v_mfma_f32_16x16x32_bf16 v[162:165], v[2:5], v[26:29], v[162:165]\n"
    "ds_read_b128 v[58:61], v218 offset:49152\n"
    "ds_read_b128 v[42:45], v230 offset:23552\n"
    "v_mfma_f32_16x16x32_bf16 v[166:169], v[2:5], v[30:33], v[166:169]\n"
    "ds_read_b128 v[62:65], v209 offset:51200\n"
    "v_add_f32 v84, 1.0, v84\n"
    "v_mfma_f32_16x16x32_bf16 v[170:173], v[6:9], v[26:29], v[170:173]\n"
    "ds_read_b128 v[46:49], v230 offset:24576\n"
    "v_add_f32 v85, 1.0, v85\n"
    "v_mfma_f32_16x16x32_bf16 v[174:177], v[6:9], v[30:33], v[174:177]\n"
    "ds_read_b128 v[66:69], v218 offset:57344\n"
    "v_add_f32 v86, 1.0, v86\n"
    "v_mfma_f32_16x16x32_bf16 v[178:181], v[10:13], v[26:29], v[178:181]\n"
    "ds_read_b128 v[50:53], v230 offset:25600\n"
    "v_add_f32 v87, 1.0, v87\n"
    "v_mfma_f32_16x16x32_bf16 v[182:185], v[10:13], v[30:33], v[182:185]\n"
    "ds_read_b128 v[70:73], v209 offset:59392\n"
    "v_add_f32 v88, 1.0, v88\n"
    "v_mfma_f32_16x16x32_bf16 v[154:157], v[14:17], v[34:37], v[154:157]\n"
    "ds_read_b128 v[54:57], v221 offset:26624\n"
    "v_add_f32 v89, 1.0, v89\n"
    "v_mfma_f32_16x16x32_bf16 v[158:161], v[14:17], v[38:41], v[158:161]\n"
    "ds_read_b128 v[74:77], v212 offset:49152\n"
    "v_mfma_f32_16x16x32_bf16 v[130:133], v[2:5], v[18:21], v[130:133]\n"
    "ds_read_b128 v[78:81], v210 offset:51200\n"
    "v_rcp_f32 v82, v82\n"
    "v_mfma_f32_16x16x32_bf16 v[134:137], v[2:5], v[22:25], v[134:137]\n"
    "v_rcp_f32 v83, v83\n"
    "v_mfma_f32_16x16x32_bf16 v[138:141], v[6:9], v[18:21], v[138:141]\n"
    "v_rcp_f32 v84, v84\n"
    "v_mfma_f32_16x16x32_bf16 v[142:145], v[6:9], v[22:25], v[142:145]\n"
    "v_rcp_f32 v85, v85\n"
    "v_mfma_f32_16x16x32_bf16 v[146:149], v[10:13], v[18:21], v[146:149]\n"
    "v_rcp_f32 v86, v86\n"
    "v_mfma_f32_16x16x32_bf16 v[150:153], v[10:13], v[22:25], v[150:153]\n"
    "v_rcp_f32 v87, v87\n"
    "s_waitcnt lgkmcnt(0)\n"
    "s_barrier\n"
    "v_mfma_f32_16x16x32_bf16 v[162:165], v[42:45], v[66:69], v[162:165]\n"
    "ds_read_b128 v[18:21], v216 offset:0\n"
    "ds_read_b128 v[2:5], v230 offset:30720\n"
    "v_mfma_f32_16x16x32_bf16 v[166:169], v[42:45], v[70:73], v[166:169]\n"
    "ds_read_b128 v[22:25], v219 offset:2048\n"
    "v_mfma_f32_16x16x32_bf16 v[170:173], v[46:49], v[66:69], v[170:173]\n"
    "ds_read_b128 v[6:9], v230 offset:31744\n"
    "v_rcp_f32 v88, v88\n"
    "v_mfma_f32_16x16x32_bf16 v[174:177], v[46:49], v[70:73], v[174:177]\n"
    "ds_read_b128 v[26:29], v216 offset:8192\n"
    "v_mfma_f32_16x16x32_bf16 v[178:181], v[50:53], v[66:69], v[178:181]\n"
    "ds_read_b128 v[10:13], v230 offset:32768\n"
    "v_rcp_f32 v89, v89\n"
    "v_mfma_f32_16x16x32_bf16 v[182:185], v[50:53], v[70:73], v[182:185]\n"
    "ds_read_b128 v[30:33], v219 offset:10240\n"
    "v_mul_f32 v98, v98, v82\n"
    "v_mfma_f32_16x16x32_bf16 v[154:157], v[54:57], v[74:77], v[154:157]\n"
    "ds_read_b128 v[14:17], v221 offset:33792\n"
    "v_mul_f32 v99, v99, v83\n"
    "v_mfma_f32_16x16x32_bf16 v[158:161], v[54:57], v[78:81], v[158:161]\n"
    "ds_read_b128 v[34:37], v211 offset:0\n"
    "v_mul_f32 v100, v100, v84\n"
    "v_mfma_f32_16x16x32_bf16 v[130:133], v[42:45], v[58:61], v[130:133]\n"
    "ds_read_b128 v[38:41], v213 offset:2048\n"
    "v_mul_f32 v101, v101, v85\n"
    "v_mfma_f32_16x16x32_bf16 v[134:137], v[42:45], v[62:65], v[134:137]\n"
    "v_mul_f32 v102, v102, v86\n"
    "v_mul_f32 v103, v103, v87\n"
    "v_mfma_f32_16x16x32_bf16 v[138:141], v[46:49], v[58:61], v[138:141]\n"
    "v_mul_f32 v104, v104, v88\n"
    "v_mul_f32 v105, v105, v89\n"
    "v_mfma_f32_16x16x32_bf16 v[142:145], v[46:49], v[62:65], v[142:145]\n"
    "v_mul_f32 v82, v106, v106\n"
    "v_mul_f32 v83, v107, v107\n"
    "v_mfma_f32_16x16x32_bf16 v[146:149], v[50:53], v[58:61], v[146:149]\n"
    "v_mul_f32 v84, v108, v108\n"
    "v_mul_f32 v85, v109, v109\n"
    "v_mfma_f32_16x16x32_bf16 v[150:153], v[50:53], v[62:65], v[150:153]\n"
    "v_mul_f32 v86, v110, v110\n"
    "v_mul_f32 v87, v111, v111\n"
    "s_waitcnt lgkmcnt(0)\n"
    "v_mfma_f32_16x16x32_bf16 v[162:165], v[2:5], v[26:29], v[162:165]\n"
    "ds_read_b128 v[58:61], v219 offset:0\n"
    "ds_read_b128 v[42:45], v230 offset:37888\n"
    "v_mfma_f32_16x16x32_bf16 v[166:169], v[2:5], v[30:33], v[166:169]\n"
    "ds_read_b128 v[62:65], v216 offset:2048\n"
    "v_mul_f32 v88, v112, v112\n"
    "v_mfma_f32_16x16x32_bf16 v[170:173], v[6:9], v[26:29], v[170:173]\n"
    "ds_read_b128 v[46:49], v230 offset:38912\n"
    "v_mul_f32 v89, v113, v113\n"
    "v_mfma_f32_16x16x32_bf16 v[174:177], v[6:9], v[30:33], v[174:177]\n"
    "ds_read_b128 v[66:69], v219 offset:8192\n"
    "v_fma_f32 v82, v82, v240, v238\n"
    "v_mfma_f32_16x16x32_bf16 v[178:181], v[10:13], v[26:29], v[178:181]\n"
    "ds_read_b128 v[50:53], v230 offset:39936\n"
    "v_fma_f32 v83, v83, v240, v238\n"
    "v_mfma_f32_16x16x32_bf16 v[182:185], v[10:13], v[30:33], v[182:185]\n"
    "ds_read_b128 v[70:73], v216 offset:10240\n"
    "v_fma_f32 v84, v84, v240, v238\n"
    "v_mfma_f32_16x16x32_bf16 v[154:157], v[14:17], v[34:37], v[154:157]\n"
    "ds_read_b128 v[54:57], v221 offset:40960\n"
    "v_fma_f32 v85, v85, v240, v238\n"
    "v_mfma_f32_16x16x32_bf16 v[158:161], v[14:17], v[38:41], v[158:161]\n"
    "ds_read_b128 v[74:77], v213 offset:0\n"
    "v_fma_f32 v86, v86, v240, v238\n"
    "v_mfma_f32_16x16x32_bf16 v[130:133], v[2:5], v[18:21], v[130:133]\n"
    "ds_read_b128 v[78:81], v211 offset:2048\n"
    "v_fma_f32 v87, v87, v240, v238\n"
    "v_mfma_f32_16x16x32_bf16 v[134:137], v[2:5], v[22:25], v[134:137]\n"
    "v_fma_f32 v88, v88, v240, v238\n"
    "v_fma_f32 v89, v89, v240, v238\n"
    "v_mfma_f32_16x16x32_bf16 v[138:141], v[6:9], v[18:21], v[138:141]\n"
    "v_mul_f32 v82, v82, v106\n"
    "v_mul_f32 v83, v83, v107\n"
    "v_mfma_f32_16x16x32_bf16 v[142:145], v[6:9], v[22:25], v[142:145]\n"
    "v_mul_f32 v84, v84, v108\n"
    "v_mul_f32 v85, v85, v109\n"
    "v_mfma_f32_16x16x32_bf16 v[146:149], v[10:13], v[18:21], v[146:149]\n"
    "v_mul_f32 v86, v86, v110\n"
    "v_mul_f32 v87, v87, v111\n"
    "v_mfma_f32_16x16x32_bf16 v[150:153], v[10:13], v[22:25], v[150:153]\n"
    "v_mul_f32 v88, v88, v112\n"
    "v_mul_f32 v89, v89, v113\n"
    "s_waitcnt lgkmcnt(0)\n"
    "s_barrier\n"
    "v_mfma_f32_16x16x32_bf16 v[162:165], v[42:45], v[66:69], v[162:165]\n"
    "ds_read_b128 v[18:21], v209 offset:0\n"
    "ds_read_b128 v[2:5], v230 offset:45056\n"
    "v_mfma_f32_16x16x32_bf16 v[166:169], v[42:45], v[70:73], v[166:169]\n"
    "ds_read_b128 v[22:25], v218 offset:2048\n"
    "v_mfma_f32_16x16x32_bf16 v[170:173], v[46:49], v[66:69], v[170:173]\n"
    "ds_read_b128 v[6:9], v230 offset:46080\n"
    "v_exp_f32 v82, v82\n"
    "v_mfma_f32_16x16x32_bf16 v[174:177], v[46:49], v[70:73], v[174:177]\n"
    "ds_read_b128 v[26:29], v209 offset:8192\n"
    "v_mfma_f32_16x16x32_bf16 v[178:181], v[50:53], v[66:69], v[178:181]\n"
    "ds_read_b128 v[10:13], v230 offset:47104\n"
    "v_exp_f32 v83, v83\n"
    "v_mfma_f32_16x16x32_bf16 v[182:185], v[50:53], v[70:73], v[182:185]\n"
    "ds_read_b128 v[30:33], v218 offset:10240\n"
    "v_mfma_f32_16x16x32_bf16 v[154:157], v[54:57], v[74:77], v[154:157]\n"
    "ds_read_b128 v[14:17], v221 offset:48128\n"
    "v_exp_f32 v84, v84\n"
    "v_mfma_f32_16x16x32_bf16 v[158:161], v[54:57], v[78:81], v[158:161]\n"
    "ds_read_b128 v[34:37], v210 offset:0\n"
    "v_mfma_f32_16x16x32_bf16 v[130:133], v[42:45], v[58:61], v[130:133]\n"
    "ds_read_b128 v[38:41], v212 offset:2048\n"
    "v_exp_f32 v85, v85\n"
    "v_mfma_f32_16x16x32_bf16 v[134:137], v[42:45], v[62:65], v[134:137]\n"
    "v_exp_f32 v86, v86\n"
    "v_mfma_f32_16x16x32_bf16 v[138:141], v[46:49], v[58:61], v[138:141]\n"
    "v_exp_f32 v87, v87\n"
    "v_mfma_f32_16x16x32_bf16 v[142:145], v[46:49], v[62:65], v[142:145]\n"
    "v_exp_f32 v88, v88\n"
    "v_mfma_f32_16x16x32_bf16 v[146:149], v[50:53], v[58:61], v[146:149]\n"
    "v_exp_f32 v89, v89\n"
    "v_mfma_f32_16x16x32_bf16 v[150:153], v[50:53], v[62:65], v[150:153]\n"
    "v_add_f32 v82, 1.0, v82\n"
    "v_add_f32 v83, 1.0, v83\n"
    "s_waitcnt lgkmcnt(0)\n"
    "v_mfma_f32_16x16x32_bf16 v[162:165], v[2:5], v[26:29], v[162:165]\n"
    "ds_read_b128 v[58:61], v218 offset:0\n"
    "ds_read_b128 v[42:45], v230 offset:52224\n"
    "v_mfma_f32_16x16x32_bf16 v[166:169], v[2:5], v[30:33], v[166:169]\n"
    "ds_read_b128 v[62:65], v209 offset:2048\n"
    "v_add_f32 v84, 1.0, v84\n"
    "v_mfma_f32_16x16x32_bf16 v[170:173], v[6:9], v[26:29], v[170:173]\n"
    "ds_read_b128 v[46:49], v230 offset:53248\n"
    "v_add_f32 v85, 1.0, v85\n"
    "v_mfma_f32_16x16x32_bf16 v[174:177], v[6:9], v[30:33], v[174:177]\n"
    "ds_read_b128 v[66:69], v218 offset:8192\n"
    "v_add_f32 v86, 1.0, v86\n"
    "v_mfma_f32_16x16x32_bf16 v[178:181], v[10:13], v[26:29], v[178:181]\n"
    "ds_read_b128 v[50:53], v230 offset:54272\n"
    "v_add_f32 v87, 1.0, v87\n"
    "v_mfma_f32_16x16x32_bf16 v[182:185], v[10:13], v[30:33], v[182:185]\n"
    "ds_read_b128 v[70:73], v209 offset:10240\n"
    "v_add_f32 v88, 1.0, v88\n"
    "v_mfma_f32_16x16x32_bf16 v[154:157], v[14:17], v[34:37], v[154:157]\n"
    "ds_read_b128 v[54:57], v221 offset:55296\n"
    "v_add_f32 v89, 1.0, v89\n"
    "v_mfma_f32_16x16x32_bf16 v[158:161], v[14:17], v[38:41], v[158:161]\n"
    "ds_read_b128 v[74:77], v212 offset:0\n"
    "v_mfma_f32_16x16x32_bf16 v[130:133], v[2:5], v[18:21], v[130:133]\n"
    "ds_read_b128 v[78:81], v210 offset:2048\n"
    "v_rcp_f32 v82, v82\n"
    "v_mfma_f32_16x16x32_bf16 v[134:137], v[2:5], v[22:25], v[134:137]\n"
    "v_rcp_f32 v83, v83\n"
    "v_mfma_f32_16x16x32_bf16 v[138:141], v[6:9], v[18:21], v[138:141]\n"
    "v_rcp_f32 v84, v84\n"
    "v_mfma_f32_16x16x32_bf16 v[142:145], v[6:9], v[22:25], v[142:145]\n"
    "v_rcp_f32 v85, v85\n"
    "v_mfma_f32_16x16x32_bf16 v[146:149], v[10:13], v[18:21], v[146:149]\n"
    "v_rcp_f32 v86, v86\n"
    "v_mfma_f32_16x16x32_bf16 v[150:153], v[10:13], v[22:25], v[150:153]\n"
    "v_rcp_f32 v87, v87\n"
    "s_waitcnt lgkmcnt(0)\n"
    "s_barrier\n"
    "v_mfma_f32_16x16x32_bf16 v[162:165], v[42:45], v[66:69], v[162:165]\n"
    "ds_read_b128 v[18:21], v209 offset:16384\n"
    "ds_read_b128 v[2:5], v230 offset:59392\n"
    "v_mfma_f32_16x16x32_bf16 v[166:169], v[42:45], v[70:73], v[166:169]\n"
    "ds_read_b128 v[22:25], v218 offset:18432\n"
    "v_mfma_f32_16x16x32_bf16 v[170:173], v[46:49], v[66:69], v[170:173]\n"
    "ds_read_b128 v[6:9], v230 offset:60416\n"
    "v_rcp_f32 v88, v88\n"
    "v_mfma_f32_16x16x32_bf16 v[174:177], v[46:49], v[70:73], v[174:177]\n"
    "ds_read_b128 v[26:29], v209 offset:24576\n"
    "v_mfma_f32_16x16x32_bf16 v[178:181], v[50:53], v[66:69], v[178:181]\n"
    "ds_read_b128 v[10:13], v230 offset:61440\n"
    "v_rcp_f32 v89, v89\n"
    "v_mfma_f32_16x16x32_bf16 v[182:185], v[50:53], v[70:73], v[182:185]\n"
    "ds_read_b128 v[30:33], v218 offset:26624\n"
    "v_mul_f32 v106, v106, v82\n"
    "v_mfma_f32_16x16x32_bf16 v[154:157], v[54:57], v[74:77], v[154:157]\n"
    "ds_read_b128 v[14:17], v221 offset:62464\n"
    "v_mul_f32 v107, v107, v83\n"
    "v_mfma_f32_16x16x32_bf16 v[158:161], v[54:57], v[78:81], v[158:161]\n"
    "ds_read_b128 v[34:37], v210 offset:16384\n"
    "v_mul_f32 v108, v108, v84\n"
    "v_mfma_f32_16x16x32_bf16 v[130:133], v[42:45], v[58:61], v[130:133]\n"
    "ds_read_b128 v[38:41], v212 offset:18432\n"
    "v_mul_f32 v109, v109, v85\n"
    "v_mfma_f32_16x16x32_bf16 v[134:137], v[42:45], v[62:65], v[134:137]\n"
    "v_mul_f32 v110, v110, v86\n"
    "v_mul_f32 v111, v111, v87\n"
    "v_mfma_f32_16x16x32_bf16 v[138:141], v[46:49], v[58:61], v[138:141]\n"
    "v_mul_f32 v112, v112, v88\n"
    "v_mul_f32 v113, v113, v89\n"
    "v_mfma_f32_16x16x32_bf16 v[142:145], v[46:49], v[62:65], v[142:145]\n"
    "v_mfma_f32_16x16x32_bf16 v[146:149], v[50:53], v[58:61], v[146:149]\n"
    "v_mfma_f32_16x16x32_bf16 v[150:153], v[50:53], v[62:65], v[150:153]\n"
    "s_waitcnt lgkmcnt(0)\n"
    "v_mfma_f32_16x16x32_bf16 v[162:165], v[2:5], v[26:29], v[162:165]\n"
    "ds_read_b128 v[58:61], v218 offset:16384\n"
    "ds_read_b128 v[42:45], v231 offset:1024\n"
    "v_mfma_f32_16x16x32_bf16 v[166:169], v[2:5], v[30:33], v[166:169]\n"
    "ds_read_b128 v[62:65], v209 offset:18432\n"
    "v_mfma_f32_16x16x32_bf16 v[170:173], v[6:9], v[26:29], v[170:173]\n"
    "ds_read_b128 v[46:49], v231 offset:2048\n"
    "v_mfma_f32_16x16x32_bf16 v[174:177], v[6:9], v[30:33], v[174:177]\n"
    "ds_read_b128 v[66:69], v218 offset:24576\n"
    "v_mfma_f32_16x16x32_bf16 v[178:181], v[10:13], v[26:29], v[178:181]\n"
    "ds_read_b128 v[50:53], v231 offset:3072\n"
    "v_mfma_f32_16x16x32_bf16 v[182:185], v[10:13], v[30:33], v[182:185]\n"
    "ds_read_b128 v[70:73], v209 offset:26624\n"
    "v_mfma_f32_16x16x32_bf16 v[154:157], v[14:17], v[34:37], v[154:157]\n"
    "ds_read_b128 v[54:57], v222 offset:4096\n"
    "v_mfma_f32_16x16x32_bf16 v[158:161], v[14:17], v[38:41], v[158:161]\n"
    "ds_read_b128 v[74:77], v212 offset:16384\n"
    "v_mfma_f32_16x16x32_bf16 v[130:133], v[2:5], v[18:21], v[130:133]\n"
    "ds_read_b128 v[78:81], v210 offset:18432\n"
    "v_mfma_f32_16x16x32_bf16 v[134:137], v[2:5], v[22:25], v[134:137]\n"
    "v_mfma_f32_16x16x32_bf16 v[138:141], v[6:9], v[18:21], v[138:141]\n"
    "v_mfma_f32_16x16x32_bf16 v[142:145], v[6:9], v[22:25], v[142:145]\n"
    "v_mfma_f32_16x16x32_bf16 v[146:149], v[10:13], v[18:21], v[146:149]\n"
    "v_mfma_f32_16x16x32_bf16 v[150:153], v[10:13], v[22:25], v[150:153]\n"
    "s_lshl_b32 s50, s46, 8\n"
    "s_lshl_b32 s51, s47, 5\n"
    "s_add_u32 s50, s50, s51\n"
    "s_lshl_b32 s51, s45, 3\n"
    "s_add_u32 s50, s50, s51\n"
    "s_add_u32 s50, s50, 0x4000\n"
    "s_waitcnt vmcnt(0)\n"
    "s_mov_b64 exec, 1\n"
    "buffer_store_dwordx2 v[214:215], off, s[36:39], s50 sc1\n"
    "s_mov_b64 exec, -1\n"
    "s_waitcnt lgkmcnt(0)\n"
    "s_barrier\n"
    "v_mfma_f32_16x16x32_bf16 v[162:165], v[42:45], v[66:69], v[162:165]\n"
    "ds_read_b128 v[18:21], v209 offset:32768\n"
    "ds_read_b128 v[2:5], v231 offset:8192\n"
    "v_mfma_f32_16x16x32_bf16 v[166:169], v[42:45], v[70:73], v[166:169]\n"
    "ds_read_b128 v[22:25], v218 offset:34816\n"
    "v_mfma_f32_16x16x32_bf16 v[170:173], v[46:49], v[66:69], v[170:173]\n"
    "ds_read_b128 v[6:9], v231 offset:9216\n"
    "v_mfma_f32_16x16x32_bf16 v[174:177], v[46:49], v[70:73], v[174:177]\n"
    "ds_read_b128 v[26:29], v209 offset:40960\n"
    "v_mfma_f32_16x16x32_bf16 v[178:181], v[50:53], v[66:69], v[178:181]\n"
    "ds_read_b128 v[10:13], v231 offset:10240\n"
    "v_mfma_f32_16x16x32_bf16 v[182:185], v[50:53], v[70:73], v[182:185]\n"
    "ds_read_b128 v[30:33], v218 offset:43008\n"
    "v_mfma_f32_16x16x32_bf16 v[154:157], v[54:57], v[74:77], v[154:157]\n"
    "ds_read_b128 v[14:17], v222 offset:11264\n"
    "v_mfma_f32_16x16x32_bf16 v[158:161], v[54:57], v[78:81], v[158:161]\n"
    "ds_read_b128 v[34:37], v210 offset:32768\n"
    "v_mfma_f32_16x16x32_bf16 v[130:133], v[42:45], v[58:61], v[130:133]\n"
    "ds_read_b128 v[38:41], v212 offset:34816\n"
    "v_mfma_f32_16x16x32_bf16 v[134:137], v[42:45], v[62:65], v[134:137]\n"
    "v_mfma_f32_16x16x32_bf16 v[138:141], v[46:49], v[58:61], v[138:141]\n"
    "v_mfma_f32_16x16x32_bf16 v[142:145], v[46:49], v[62:65], v[142:145]\n"
    "v_mfma_f32_16x16x32_bf16 v[146:149], v[50:53], v[58:61], v[146:149]\n"
    "v_mfma_f32_16x16x32_bf16 v[150:153], v[50:53], v[62:65], v[150:153]\n"
    "s_waitcnt lgkmcnt(0)\n"
    "v_mfma_f32_16x16x32_bf16 v[162:165], v[2:5], v[26:29], v[162:165]\n"
    "ds_read_b128 v[58:61], v218 offset:32768\n"
    "ds_read_b128 v[42:45], v231 offset:15360\n"
    "v_mfma_f32_16x16x32_bf16 v[166:169], v[2:5], v[30:33], v[166:169]\n"
    "ds_read_b128 v[62:65], v209 offset:34816\n"
    "v_mfma_f32_16x16x32_bf16 v[170:173], v[6:9], v[26:29], v[170:173]\n"
    "ds_read_b128 v[46:49], v231 offset:16384\n"
    "v_mfma_f32_16x16x32_bf16 v[174:177], v[6:9], v[30:33], v[174:177]\n"
    "ds_read_b128 v[66:69], v218 offset:40960\n"
    "v_mfma_f32_16x16x32_bf16 v[178:181], v[10:13], v[26:29], v[178:181]\n"
    "ds_read_b128 v[50:53], v231 offset:17408\n"
    "v_mfma_f32_16x16x32_bf16 v[182:185], v[10:13], v[30:33], v[182:185]\n"
    "ds_read_b128 v[70:73], v209 offset:43008\n"
    "v_mfma_f32_16x16x32_bf16 v[154:157], v[14:17], v[34:37], v[154:157]\n"
    "ds_read_b128 v[54:57], v222 offset:18432\n"
    "v_mfma_f32_16x16x32_bf16 v[158:161], v[14:17], v[38:41], v[158:161]\n"
    "ds_read_b128 v[74:77], v212 offset:32768\n"
    "v_mfma_f32_16x16x32_bf16 v[130:133], v[2:5], v[18:21], v[130:133]\n"
    "ds_read_b128 v[78:81], v210 offset:34816\n"
    "v_mfma_f32_16x16x32_bf16 v[134:137], v[2:5], v[22:25], v[134:137]\n"
    "v_mfma_f32_16x16x32_bf16 v[138:141], v[6:9], v[18:21], v[138:141]\n"
    "v_mfma_f32_16x16x32_bf16 v[142:145], v[6:9], v[22:25], v[142:145]\n"
    "v_mfma_f32_16x16x32_bf16 v[146:149], v[10:13], v[18:21], v[146:149]\n"
    "v_mfma_f32_16x16x32_bf16 v[150:153], v[10:13], v[22:25], v[150:153]\n"
    "s_waitcnt lgkmcnt(0)\n"
    "s_barrier\n"
    "v_mfma_f32_16x16x32_bf16 v[162:165], v[42:45], v[66:69], v[162:165]\n"
    "ds_read_b128 v[18:21], v209 offset:49152\n"
    "ds_read_b128 v[2:5], v230 offset:16384\n"
    "v_mfma_f32_16x16x32_bf16 v[166:169], v[42:45], v[70:73], v[166:169]\n"
    "ds_read_b128 v[22:25], v218 offset:51200\n"
    "v_mfma_f32_16x16x32_bf16 v[170:173], v[46:49], v[66:69], v[170:173]\n"
    "ds_read_b128 v[6:9], v230 offset:17408\n"
    "v_mfma_f32_16x16x32_bf16 v[174:177], v[46:49], v[70:73], v[174:177]\n"
    "ds_read_b128 v[26:29], v209 offset:57344\n"
    "v_mfma_f32_16x16x32_bf16 v[178:181], v[50:53], v[66:69], v[178:181]\n"
    "ds_read_b128 v[10:13], v230 offset:18432\n"
    "v_mfma_f32_16x16x32_bf16 v[182:185], v[50:53], v[70:73], v[182:185]\n"
    "ds_read_b128 v[30:33], v218 offset:59392\n"
    "v_mfma_f32_16x16x32_bf16 v[154:157], v[54:57], v[74:77], v[154:157]\n"
    "ds_read_b128 v[14:17], v221 offset:19456\n"
    "v_mfma_f32_16x16x32_bf16 v[158:161], v[54:57], v[78:81], v[158:161]\n"
    "ds_read_b128 v[34:37], v210 offset:49152\n"
    "v_mfma_f32_16x16x32_bf16 v[130:133], v[42:45], v[58:61], v[130:133]\n"
    "ds_read_b128 v[38:41], v212 offset:51200\n"
    "v_mfma_f32_16x16x32_bf16 v[134:137], v[42:45], v[62:65], v[134:137]\n"
    "v_mfma_f32_16x16x32_bf16 v[138:141], v[46:49], v[58:61], v[138:141]\n"
    "v_mfma_f32_16x16x32_bf16 v[142:145], v[46:49], v[62:65], v[142:145]\n"
    "v_mfma_f32_16x16x32_bf16 v[146:149], v[50:53], v[58:61], v[146:149]\n"
    "v_mfma_f32_16x16x32_bf16 v[150:153], v[50:53], v[62:65], v[150:153]\n"
    "s_waitcnt lgkmcnt(0)\n"
    "v_mfma_f32_16x16x32_bf16 v[162:165], v[2:5], v[26:29], v[162:165]\n"
    "ds_read_b128 v[58:61], v218 offset:49152\n"
    "ds_read_b128 v[42:45], v230 offset:23552\n"
    "v_mfma_f32_16x16x32_bf16 v[166:169], v[2:5], v[30:33], v[166:169]\n"
    "ds_read_b128 v[62:65], v209 offset:51200\n"
    "v_mfma_f32_16x16x32_bf16 v[170:173], v[6:9], v[26:29], v[170:173]\n"
    "ds_read_b128 v[46:49], v230 offset:24576\n"
    "v_mfma_f32_16x16x32_bf16 v[174:177], v[6:9], v[30:33], v[174:177]\n"
    "ds_read_b128 v[66:69], v218 offset:57344\n"
    "v_mfma_f32_16x16x32_bf16 v[178:181], v[10:13], v[26:29], v[178:181]\n"
    "ds_read_b128 v[50:53], v230 offset:25600\n"
    "v_mfma_f32_16x16x32_bf16 v[182:185], v[10:13], v[30:33], v[182:185]\n"
    "ds_read_b128 v[70:73], v209 offset:59392\n"
    "v_mfma_f32_16x16x32_bf16 v[154:157], v[14:17], v[34:37], v[154:157]\n"
    "ds_read_b128 v[54:57], v221 offset:26624\n"
    "v_mfma_f32_16x16x32_bf16 v[158:161], v[14:17], v[38:41], v[158:161]\n"
    "ds_read_b128 v[74:77], v212 offset:49152\n"
    "v_mfma_f32_16x16x32_bf16 v[130:133], v[2:5], v[18:21], v[130:133]\n"
    "ds_read_b128 v[78:81], v210 offset:51200\n"
    "v_mfma_f32_16x16x32_bf16 v[134:137], v[2:5], v[22:25], v[134:137]\n"
    "v_mfma_f32_16x16x32_bf16 v[138:141], v[6:9], v[18:21], v[138:141]\n"
    "v_mfma_f32_16x16x32_bf16 v[142:145], v[6:9], v[22:25], v[142:145]\n"
    "v_mfma_f32_16x16x32_bf16 v[146:149], v[10:13], v[18:21], v[146:149]\n"
    "v_mfma_f32_16x16x32_bf16 v[150:153], v[10:13], v[22:25], v[150:153]\n"
    "s_waitcnt lgkmcnt(0)\n"
    "s_barrier\n"
    "v_mfma_f32_16x16x32_bf16 v[162:165], v[42:45], v[66:69], v[162:165]\n"
    "ds_read_b128 v[18:21], v216 offset:0\n"
    "ds_read_b128 v[2:5], v230 offset:30720\n"
    "v_mfma_f32_16x16x32_bf16 v[166:169], v[42:45], v[70:73], v[166:169]\n"
    "ds_read_b128 v[22:25], v219 offset:2048\n"
    "v_mfma_f32_16x16x32_bf16 v[170:173], v[46:49], v[66:69], v[170:173]\n"
    "ds_read_b128 v[6:9], v230 offset:31744\n"
    "v_mfma_f32_16x16x32_bf16 v[174:177], v[46:49], v[70:73], v[174:177]\n"
    "ds_read_b128 v[26:29], v216 offset:8192\n"
    "v_mfma_f32_16x16x32_bf16 v[178:181], v[50:53], v[66:69], v[178:181]\n"
    "ds_read_b128 v[10:13], v230 offset:32768\n"
    "v_mfma_f32_16x16x32_bf16 v[182:185], v[50:53], v[70:73], v[182:185]\n"
    "ds_read_b128 v[30:33], v219 offset:10240\n"
    "v_mfma_f32_16x16x32_bf16 v[154:157], v[54:57], v[74:77], v[154:157]\n"
    "ds_read_b128 v[14:17], v221 offset:33792\n"
    "v_mfma_f32_16x16x32_bf16 v[158:161], v[54:57], v[78:81], v[158:161]\n"
    "ds_read_b128 v[34:37], v211 offset:0\n"
    "v_mfma_f32_16x16x32_bf16 v[130:133], v[42:45], v[58:61], v[130:133]\n"
    "ds_read_b128 v[38:41], v213 offset:2048\n"
    "v_mfma_f32_16x16x32_bf16 v[134:137], v[42:45], v[62:65], v[134:137]\n"
    "v_mfma_f32_16x16x32_bf16 v[138:141], v[46:49], v[58:61], v[138:141]\n"
    "v_mfma_f32_16x16x32_bf16 v[142:145], v[46:49], v[62:65], v[142:145]\n"
    "v_mfma_f32_16x16x32_bf16 v[146:149], v[50:53], v[58:61], v[146:149]\n"
    "v_mfma_f32_16x16x32_bf16 v[150:153], v[50:53], v[62:65], v[150:153]\n"
    "s_waitcnt lgkmcnt(0)\n"
    "v_mfma_f32_16x16x32_bf16 v[162:165], v[2:5], v[26:29], v[162:165]\n"
    "ds_read_b128 v[58:61], v219 offset:0\n"
    "ds_read_b128 v[42:45], v230 offset:37888\n"
    "v_mfma_f32_16x16x32_bf16 v[166:169], v[2:5], v[30:33], v[166:169]\n"
    "ds_read_b128 v[62:65], v216 offset:2048\n"
    "v_mfma_f32_16x16x32_bf16 v[170:173], v[6:9], v[26:29], v[170:173]\n"
    "ds_read_b128 v[46:49], v230 offset:38912\n"
    "v_mfma_f32_16x16x32_bf16 v[174:177], v[6:9], v[30:33], v[174:177]\n"
    "ds_read_b128 v[66:69], v219 offset:8192\n"
    "v_mfma_f32_16x16x32_bf16 v[178:181], v[10:13], v[26:29], v[178:181]\n"
    "ds_read_b128 v[50:53], v230 offset:39936\n"
    "v_mfma_f32_16x16x32_bf16 v[182:185], v[10:13], v[30:33], v[182:185]\n"
    "ds_read_b128 v[70:73], v216 offset:10240\n"
    "v_mfma_f32_16x16x32_bf16 v[154:157], v[14:17], v[34:37], v[154:157]\n"
    "ds_read_b128 v[54:57], v221 offset:40960\n"
    "v_mfma_f32_16x16x32_bf16 v[158:161], v[14:17], v[38:41], v[158:161]\n"
    "ds_read_b128 v[74:77], v213 offset:0\n"
    "v_mfma_f32_16x16x32_bf16 v[130:133], v[2:5], v[18:21], v[130:133]\n"
    "ds_read_b128 v[78:81], v211 offset:2048\n"
    "v_mfma_f32_16x16x32_bf16 v[134:137], v[2:5], v[22:25], v[134:137]\n"
    "v_mfma_f32_16x16x32_bf16 v[138:141], v[6:9], v[18:21], v[138:141]\n"
    "v_mfma_f32_16x16x32_bf16 v[142:145], v[6:9], v[22:25], v[142:145]\n"
    "v_mfma_f32_16x16x32_bf16 v[146:149], v[10:13], v[18:21], v[146:149]\n"
    "v_mfma_f32_16x16x32_bf16 v[150:153], v[10:13], v[22:25], v[150:153]\n"
    "s_waitcnt lgkmcnt(0)\n"
    "s_barrier\n"
    "v_mfma_f32_16x16x32_bf16 v[162:165], v[42:45], v[66:69], v[162:165]\n"
    "ds_read_b128 v[18:21], v209 offset:0\n"
    "ds_read_b128 v[2:5], v230 offset:45056\n"
    "v_mfma_f32_16x16x32_bf16 v[166:169], v[42:45], v[70:73], v[166:169]\n"
    "ds_read_b128 v[22:25], v218 offset:2048\n"
    "v_mfma_f32_16x16x32_bf16 v[170:173], v[46:49], v[66:69], v[170:173]\n"
    "ds_read_b128 v[6:9], v230 offset:46080\n"
    "v_mfma_f32_16x16x32_bf16 v[174:177], v[46:49], v[70:73], v[174:177]\n"
    "ds_read_b128 v[26:29], v209 offset:8192\n"
    "v_mfma_f32_16x16x32_bf16 v[178:181], v[50:53], v[66:69], v[178:181]\n"
    "ds_read_b128 v[10:13], v230 offset:47104\n"
    "v_mfma_f32_16x16x32_bf16 v[182:185], v[50:53], v[70:73], v[182:185]\n"
    "ds_read_b128 v[30:33], v218 offset:10240\n"
    "v_mfma_f32_16x16x32_bf16 v[154:157], v[54:57], v[74:77], v[154:157]\n"
    "ds_read_b128 v[14:17], v221 offset:48128\n"
    "v_mfma_f32_16x16x32_bf16 v[158:161], v[54:57], v[78:81], v[158:161]\n"
    "ds_read_b128 v[34:37], v210 offset:0\n"
    "v_mfma_f32_16x16x32_bf16 v[130:133], v[42:45], v[58:61], v[130:133]\n"
    "ds_read_b128 v[38:41], v212 offset:2048\n"
    "v_mfma_f32_16x16x32_bf16 v[134:137], v[42:45], v[62:65], v[134:137]\n"
    "v_mfma_f32_16x16x32_bf16 v[138:141], v[46:49], v[58:61], v[138:141]\n"
    "v_mfma_f32_16x16x32_bf16 v[142:145], v[46:49], v[62:65], v[142:145]\n"
    "v_mfma_f32_16x16x32_bf16 v[146:149], v[50:53], v[58:61], v[146:149]\n"
    "v_mfma_f32_16x16x32_bf16 v[150:153], v[50:53], v[62:65], v[150:153]\n"
    "s_waitcnt lgkmcnt(0)\n"
    "v_mfma_f32_16x16x32_bf16 v[162:165], v[2:5], v[26:29], v[162:165]\n"
    "ds_read_b128 v[58:61], v218 offset:0\n"
    "ds_read_b128 v[42:45], v230 offset:52224\n"
    "v_mfma_f32_16x16x32_bf16 v[166:169], v[2:5], v[30:33], v[166:169]\n"
    "ds_read_b128 v[62:65], v209 offset:2048\n"
    "v_mfma_f32_16x16x32_bf16 v[170:173], v[6:9], v[26:29], v[170:173]\n"
    "ds_read_b128 v[46:49], v230 offset:53248\n"
    "v_mfma_f32_16x16x32_bf16 v[174:177], v[6:9], v[30:33], v[174:177]\n"
    "ds_read_b128 v[66:69], v218 offset:8192\n"
    "v_mfma_f32_16x16x32_bf16 v[178:181], v[10:13], v[26:29], v[178:181]\n"
    "ds_read_b128 v[50:53], v230 offset:54272\n"
    "v_mfma_f32_16x16x32_bf16 v[182:185], v[10:13], v[30:33], v[182:185]\n"
    "ds_read_b128 v[70:73], v209 offset:10240\n"
    "v_mfma_f32_16x16x32_bf16 v[154:157], v[14:17], v[34:37], v[154:157]\n"
    "ds_read_b128 v[54:57], v221 offset:55296\n"
    "v_mfma_f32_16x16x32_bf16 v[158:161], v[14:17], v[38:41], v[158:161]\n"
    "ds_read_b128 v[74:77], v212 offset:0\n"
    "v_mfma_f32_16x16x32_bf16 v[130:133], v[2:5], v[18:21], v[130:133]\n"
    "ds_read_b128 v[78:81], v210 offset:2048\n"
    "v_mfma_f32_16x16x32_bf16 v[134:137], v[2:5], v[22:25], v[134:137]\n"
    "v_mfma_f32_16x16x32_bf16 v[138:141], v[6:9], v[18:21], v[138:141]\n"
    "v_mfma_f32_16x16x32_bf16 v[142:145], v[6:9], v[22:25], v[142:145]\n"
    "v_mfma_f32_16x16x32_bf16 v[146:149], v[10:13], v[18:21], v[146:149]\n"
    "v_mfma_f32_16x16x32_bf16 v[150:153], v[10:13], v[22:25], v[150:153]\n"
    "s_waitcnt lgkmcnt(0)\n"
    "s_barrier\n"
    "v_mfma_f32_16x16x32_bf16 v[162:165], v[42:45], v[66:69], v[162:165]\n"
    "ds_read_b128 v[18:21], v209 offset:16384\n"
    "ds_read_b128 v[2:5], v230 offset:59392\n"
    "v_mfma_f32_16x16x32_bf16 v[166:169], v[42:45], v[70:73], v[166:169]\n"
    "ds_read_b128 v[22:25], v218 offset:18432\n"
    "v_mfma_f32_16x16x32_bf16 v[170:173], v[46:49], v[66:69], v[170:173]\n"
    "ds_read_b128 v[6:9], v230 offset:60416\n"
    "v_mfma_f32_16x16x32_bf16 v[174:177], v[46:49], v[70:73], v[174:177]\n"
    "ds_read_b128 v[26:29], v209 offset:24576\n"
    "v_mfma_f32_16x16x32_bf16 v[178:181], v[50:53], v[66:69], v[178:181]\n"
    "ds_read_b128 v[10:13], v230 offset:61440\n"
    "v_mfma_f32_16x16x32_bf16 v[182:185], v[50:53], v[70:73], v[182:185]\n"
    "ds_read_b128 v[30:33], v218 offset:26624\n"
    "v_mfma_f32_16x16x32_bf16 v[154:157], v[54:57], v[74:77], v[154:157]\n"
    "ds_read_b128 v[14:17], v221 offset:62464\n"
    "v_mfma_f32_16x16x32_bf16 v[158:161], v[54:57], v[78:81], v[158:161]\n"
    "ds_read_b128 v[34:37], v210 offset:16384\n"
    "v_mfma_f32_16x16x32_bf16 v[130:133], v[42:45], v[58:61], v[130:133]\n"
    "ds_read_b128 v[38:41], v212 offset:18432\n"
    "v_mfma_f32_16x16x32_bf16 v[134:137], v[42:45], v[62:65], v[134:137]\n"
    "v_mfma_f32_16x16x32_bf16 v[138:141], v[46:49], v[58:61], v[138:141]\n"
    "v_mfma_f32_16x16x32_bf16 v[142:145], v[46:49], v[62:65], v[142:145]\n"
    "v_mfma_f32_16x16x32_bf16 v[146:149], v[50:53], v[58:61], v[146:149]\n"
    "v_mfma_f32_16x16x32_bf16 v[150:153], v[50:53], v[62:65], v[150:153]\n"
    "s_waitcnt lgkmcnt(0)\n"
    "v_mfma_f32_16x16x32_bf16 v[162:165], v[2:5], v[26:29], v[162:165]\n"
    "ds_read_b128 v[58:61], v218 offset:16384\n"
    "ds_read_b128 v[42:45], v231 offset:1024\n"
    "v_mfma_f32_16x16x32_bf16 v[166:169], v[2:5], v[30:33], v[166:169]\n"
    "ds_read_b128 v[62:65], v209 offset:18432\n"
    "v_mfma_f32_16x16x32_bf16 v[170:173], v[6:9], v[26:29], v[170:173]\n"
    "ds_read_b128 v[46:49], v231 offset:2048\n"
    "v_mfma_f32_16x16x32_bf16 v[174:177], v[6:9], v[30:33], v[174:177]\n"
    "ds_read_b128 v[66:69], v218 offset:24576\n"
    "v_mfma_f32_16x16x32_bf16 v[178:181], v[10:13], v[26:29], v[178:181]\n"
    "ds_read_b128 v[50:53], v231 offset:3072\n"
    "v_mfma_f32_16x16x32_bf16 v[182:185], v[10:13], v[30:33], v[182:185]\n"
    "ds_read_b128 v[70:73], v209 offset:26624\n"
    "v_mfma_f32_16x16x32_bf16 v[154:157], v[14:17], v[34:37], v[154:157]\n"
    "ds_read_b128 v[54:57], v222 offset:4096\n"
    "v_mfma_f32_16x16x32_bf16 v[158:161], v[14:17], v[38:41], v[158:161]\n"
    "ds_read_b128 v[74:77], v212 offset:16384\n"
    "v_mfma_f32_16x16x32_bf16 v[130:133], v[2:5], v[18:21], v[130:133]\n"
    "ds_read_b128 v[78:81], v210 offset:18432\n"
    "v_mfma_f32_16x16x32_bf16 v[134:137], v[2:5], v[22:25], v[134:137]\n"
    "v_mfma_f32_16x16x32_bf16 v[138:141], v[6:9], v[18:21], v[138:141]\n"
    "v_mfma_f32_16x16x32_bf16 v[142:145], v[6:9], v[22:25], v[142:145]\n"
    "v_mfma_f32_16x16x32_bf16 v[146:149], v[10:13], v[18:21], v[146:149]\n"
    "v_mfma_f32_16x16x32_bf16 v[150:153], v[10:13], v[22:25], v[150:153]\n"
    "s_waitcnt lgkmcnt(0)\n"
    "s_barrier\n"
    "v_mfma_f32_16x16x32_bf16 v[162:165], v[42:45], v[66:69], v[162:165]\n"
    "ds_read_b128 v[18:21], v209 offset:32768\n"
    "v_mfma_f32_16x16x32_bf16 v[166:169], v[42:45], v[70:73], v[166:169]\n"
    "ds_read_b128 v[22:25], v218 offset:34816\n"
    "v_mfma_f32_16x16x32_bf16 v[170:173], v[46:49], v[66:69], v[170:173]\n"
    "v_mfma_f32_16x16x32_bf16 v[174:177], v[46:49], v[70:73], v[174:177]\n"
    "ds_read_b128 v[2:5], v231 offset:18432\n"
    "v_mfma_f32_16x16x32_bf16 v[178:181], v[50:53], v[66:69], v[178:181]\n"
    "ds_read_b128 v[6:9], v231 offset:19456\n"
    "v_mfma_f32_16x16x32_bf16 v[182:185], v[50:53], v[70:73], v[182:185]\n"
    "v_mfma_f32_16x16x32_bf16 v[154:157], v[54:57], v[74:77], v[154:157]\n"
    "ds_read_b128 v[10:13], v231 offset:20480\n"
    "v_mfma_f32_16x16x32_bf16 v[158:161], v[54:57], v[78:81], v[158:161]\n"
    "ds_read_b128 v[14:17], v222 offset:21504\n"
    "v_mfma_f32_16x16x32_bf16 v[130:133], v[42:45], v[58:61], v[130:133]\n"
    "v_mfma_f32_16x16x32_bf16 v[134:137], v[42:45], v[62:65], v[134:137]\n"
    "v_mfma_f32_16x16x32_bf16 v[138:141], v[46:49], v[58:61], v[138:141]\n"
    "v_mfma_f32_16x16x32_bf16 v[142:145], v[46:49], v[62:65], v[142:145]\n"
    "v_mfma_f32_16x16x32_bf16 v[146:149], v[50:53], v[58:61], v[146:149]\n"
    "v_mfma_f32_16x16x32_bf16 v[150:153], v[50:53], v[62:65], v[150:153]\n"
    "s_xor_b32 s63, s46, 1\n"
    "s_lshl_b32 s63, s63, 3\n"
    "s_add_u32 s63, s63, s47\n"
    "s_lshl_b32 s63, s63, 2\n"
    "s_add_u32 s63, s63, s45\n"
    "s_lshl_b32 s63, s63, 12\n"
    "s_add_u32 s63, s63, 0xb00000\n"
    "s_mul_i32 s62, s55, 0xfbfffc00\n"
    "s_add_u32 s62, s62, 0x4000000\n"
    "s_add_u32 s62, s62, s63\n"
    "v_cvt_pk_bf16_f32 v94, v154, v155\n"
    "v_cvt_pk_bf16_f32 v95, v156, v157\n"
    "v_cvt_pk_bf16_f32 v96, v158, v159\n"
    "v_cvt_pk_bf16_f32 v97, v160, v161\n"
    "s_waitcnt lgkmcnt(0)\n"
    "v_mfma_f32_16x16x32_bf16 v[130:133], v[2:5], v[18:21], v[130:133]\n"
    "ds_read_b128 v[58:61], v218 offset:32768\n"
    "ds_read_b128 v[62:65], v209 offset:34816\n"
    "v_mfma_f32_16x16x32_bf16 v[134:137], v[2:5], v[22:25], v[134:137]\n"
    "ds_read_b128 v[42:45], v231 offset:25600\n"
    "buffer_store_dwordx4 v[94:97], v234, s[36:39], s62 offen offset:0 sc1\n"
    "v_mfma_f32_16x16x32_bf16 v[138:141], v[6:9], v[18:21], v[138:141]\n"
    "ds_read_b128 v[46:49], v231 offset:26624\n"
    "v_cvt_pk_bf16_f32 v82, v162, v163\n"
    "v_mfma_f32_16x16x32_bf16 v[142:145], v[6:9], v[22:25], v[142:145]\n"
    "ds_read_b128 v[50:53], v231 offset:27648\n"
    "v_cvt_pk_bf16_f32 v83, v164, v165\n"
    "v_mfma_f32_16x16x32_bf16 v[146:149], v[10:13], v[18:21], v[146:149]\n"
    "ds_read_b128 v[54:57], v222 offset:28672\n"
    "v_cvt_pk_bf16_f32 v84, v166, v167\n"
    "v_mfma_f32_16x16x32_bf16 v[150:153], v[10:13], v[22:25], v[150:153]\n"
    "v_cvt_pk_bf16_f32 v85, v168, v169\n"
    "buffer_store_dwordx4 v[82:85], v234, s[36:39], s63 offen offset:0 sc1\n"
    "v_mfma_f32_16x16x32_bf16 v[154:157], v[14:17], v[18:21], v[154:157]\n"
    "v_cvt_pk_bf16_f32 v86, v170, v171\n"
    "v_cvt_pk_bf16_f32 v87, v172, v173\n"
    "v_mfma_f32_16x16x32_bf16 v[158:161], v[14:17], v[22:25], v[158:161]\n"
    "v_cvt_pk_bf16_f32 v88, v174, v175\n"
    "v_cvt_pk_bf16_f32 v89, v176, v177\n"
    "s_waitcnt lgkmcnt(0)\n"
    "s_barrier\n"
    "v_mfma_f32_16x16x32_bf16 v[130:133], v[42:45], v[58:61], v[130:133]\n"
    "ds_read_b128 v[18:21], v209 offset:40960\n"
    "ds_read_b128 v[22:25], v218 offset:43008\n"
    "v_mfma_f32_16x16x32_bf16 v[134:137], v[42:45], v[62:65], v[134:137]\n"
    "ds_read_b128 v[2:5], v230 offset:30720\n"
    "buffer_store_dwordx4 v[86:89], v234, s[36:39], s63 offen offset:1024 sc1\n"
    "v_mfma_f32_16x16x32_bf16 v[138:141], v[46:49], v[58:61], v[138:141]\n"
    "ds_read_b128 v[6:9], v230 offset:31744\n"
    "v_cvt_pk_bf16_f32 v90, v178, v179\n"
    "v_mfma_f32_16x16x32_bf16 v[142:145], v[46:49], v[62:65], v[142:145]\n"
    "ds_read_b128 v[10:13], v230 offset:32768\n"
    "v_cvt_pk_bf16_f32 v91, v180, v181\n"
    "v_mfma_f32_16x16x32_bf16 v[146:149], v[50:53], v[58:61], v[146:149]\n"
    "ds_read_b128 v[14:17], v221 offset:33792\n"
    "v_cvt_pk_bf16_f32 v92, v182, v183\n"
    "v_mfma_f32_16x16x32_bf16 v[150:153], v[50:53], v[62:65], v[150:153]\n"
    "v_cvt_pk_bf16_f32 v93, v184, v185\n"
    "buffer_store_dwordx4 v[90:93], v234, s[36:39], s63 offen offset:2048 sc1\n"
    "v_mfma_f32_16x16x32_bf16 v[154:157], v[54:57], v[58:61], v[154:157]\n"
    "v_mul_f32 v82, v114, v114\n"
    "v_mul_f32 v83, v115, v115\n"
    "v_mfma_f32_16x16x32_bf16 v[158:161], v[54:57], v[62:65], v[158:161]\n"
    "v_mul_f32 v84, v116, v116\n"
    "v_mul_f32 v85, v117, v117\n"
    "s_waitcnt lgkmcnt(0)\n"
    "v_mfma_f32_16x16x32_bf16 v[130:133], v[2:5], v[18:21], v[130:133]\n"
    "ds_read_b128 v[58:61], v218 offset:40960\n"
    "ds_read_b128 v[62:65], v209 offset:43008\n"
    "v_mfma_f32_16x16x32_bf16 v[134:137], v[2:5], v[22:25], v[134:137]\n"
    "ds_read_b128 v[42:45], v230 offset:37888\n"
    "v_mul_f32 v86, v118, v118\n"
    "v_mfma_f32_16x16x32_bf16 v[138:141], v[6:9], v[18:21], v[138:141]\n"
    "ds_read_b128 v[46:49], v230 offset:38912\n"
    "v_mul_f32 v87, v119, v119\n"
    "v_mfma_f32_16x16x32_bf16 v[142:145], v[6:9], v[22:25], v[142:145]\n"
    "ds_read_b128 v[50:53], v230 offset:39936\n"
    "v_mul_f32 v88, v120, v120\n"
    "v_mfma_f32_16x16x32_bf16 v[146:149], v[10:13], v[18:21], v[146:149]\n"
    "ds_read_b128 v[54:57], v221 offset:40960\n"
    "v_mul_f32 v89, v121, v121\n"
    "v_mfma_f32_16x16x32_bf16 v[150:153], v[10:13], v[22:25], v[150:153]\n"
    "v_fma_f32 v82, v82, v240, v238\n"
    "v_fma_f32 v83, v83, v240, v238\n"
    "v_mfma_f32_16x16x32_bf16 v[154:157], v[14:17], v[18:21], v[154:157]\n"
    "v_fma_f32 v84, v84, v240, v238\n"
    "v_fma_f32 v85, v85, v240, v238\n"
    "v_mfma_f32_16x16x32_bf16 v[158:161], v[14:17], v[22:25], v[158:161]\n"
    "v_fma_f32 v86, v86, v240, v238\n"
    "v_fma_f32 v87, v87, v240, v238\n"
    "s_waitcnt lgkmcnt(0)\n"
    "s_barrier\n"
    "v_mfma_f32_16x16x32_bf16 v[130:133], v[42:45], v[58:61], v[130:133]\n"
    "ds_read_b128 v[18:21], v209 offset:49152\n"
    "ds_read_b128 v[22:25], v218 offset:51200\n"
    "v_mfma_f32_16x16x32_bf16 v[134:137], v[42:45], v[62:65], v[134:137]\n"
    "ds_read_b128 v[2:5], v230 offset:45056\n"
    "v_fma_f32 v88, v88, v240, v238\n"
    "v_mfma_f32_16x16x32_bf16 v[138:141], v[46:49], v[58:61], v[138:141]\n"
    "ds_read_b128 v[6:9], v230 offset:46080\n"
    "v_fma_f32 v89, v89, v240, v238\n"
    "v_mfma_f32_16x16x32_bf16 v[142:145], v[46:49], v[62:65], v[142:145]\n"
    "ds_read_b128 v[10:13], v230 offset:47104\n"
    "v_mul_f32 v82, v82, v114\n"
    "v_mfma_f32_16x16x32_bf16 v[146:149], v[50:53], v[58:61], v[146:149]\n"
    "ds_read_b128 v[14:17], v221 offset:48128\n"
    "v_mul_f32 v83, v83, v115\n"
    "v_mfma_f32_16x16x32_bf16 v[150:153], v[50:53], v[62:65], v[150:153]\n"
    "v_mul_f32 v84, v84, v116\n"
    "v_mul_f32 v85, v85, v117\n"
    "v_mfma_f32_16x16x32_bf16 v[154:157], v[54:57], v[58:61], v[154:157]\n"
    "v_mul_f32 v86, v86, v118\n"
    "v_mul_f32 v87, v87, v119\n"
    "v_mfma_f32_16x16x32_bf16 v[158:161], v[54:57], v[62:65], v[158:161]\n"
    "v_mul_f32 v88, v88, v120\n"
    "v_mul_f32 v89, v89, v121\n"
    "s_waitcnt lgkmcnt(0)\n"
    "v_mfma_f32_16x16x32_bf16 v[130:133], v[2:5], v[18:21], v[130:133]\n"
    "ds_read_b128 v[58:61], v218 offset:49152\n"
    "ds_read_b128 v[62:65], v209 offset:51200\n"
    "v_mfma_f32_16x16x32_bf16 v[134:137], v[2:5], v[22:25], v[134:137]\n"
    "ds_read_b128 v[42:45], v230 offset:52224\n"
    "v_mfma_f32_16x16x32_bf16 v[138:141], v[6:9], v[18:21], v[138:141]\n"
    "ds_read_b128 v[46:49], v230 offset:53248\n"
    "v_exp_f32 v82, v82\n"
    "v_mfma_f32_16x16x32_bf16 v[142:145], v[6:9], v[22:25], v[142:145]\n"
    "ds_read_b128 v[50:53], v230 offset:54272\n"
    "v_mfma_f32_16x16x32_bf16 v[146:149], v[10:13], v[18:21], v[146:149]\n"
    "ds_read_b128 v[54:57], v221 offset:55296\n"
    "v_exp_f32 v83, v83\n"
    "v_mfma_f32_16x16x32_bf16 v[150:153], v[10:13], v[22:25], v[150:153]\n"
    "v_exp_f32 v84, v84\n"
    "v_mfma_f32_16x16x32_bf16 v[154:157], v[14:17], v[18:21], v[154:157]\n"
    "v_exp_f32 v85, v85\n"
    "v_mfma_f32_16x16x32_bf16 v[158:161], v[14:17], v[22:25], v[158:161]\n"
    "v_exp_f32 v86, v86\n"
    "s_waitcnt lgkmcnt(0)\n"
    "s_barrier\n"
    "v_mfma_f32_16x16x32_bf16 v[130:133], v[42:45], v[58:61], v[130:133]\n"
    "ds_read_b128 v[18:21], v209 offset:0\n"
    "ds_read_b128 v[22:25], v218 offset:2048\n"
    "v_mfma_f32_16x16x32_bf16 v[134:137], v[42:45], v[62:65], v[134:137]\n"
    "ds_read_b128 v[2:5], v231 offset:4096\n"
    "v_mfma_f32_16x16x32_bf16 v[138:141], v[46:49], v[58:61], v[138:141]\n"
    "ds_read_b128 v[6:9], v231 offset:5120\n"
    "v_exp_f32 v87, v87\n"
    "v_mfma_f32_16x16x32_bf16 v[142:145], v[46:49], v[62:65], v[142:145]\n"
    "ds_read_b128 v[10:13], v231 offset:6144\n"
    "v_mfma_f32_16x16x32_bf16 v[146:149], v[50:53], v[58:61], v[146:149]\n"
    "ds_read_b128 v[14:17], v222 offset:7168\n"
    "v_exp_f32 v88, v88\n"
    "v_mfma_f32_16x16x32_bf16 v[150:153], v[50:53], v[62:65], v[150:153]\n"
    "v_exp_f32 v89, v89\n"
    "v_mfma_f32_16x16x32_bf16 v[154:157], v[54:57], v[58:61], v[154:157]\n"
    "v_add_f32 v82, 1.0, v82\n"
    "v_add_f32 v83, 1.0, v83\n"
    "v_mfma_f32_16x16x32_bf16 v[158:161], v[54:57], v[62:65], v[158:161]\n"
    "v_add_f32 v84, 1.0, v84\n"
    "v_add_f32 v85, 1.0, v85\n"
    "s_waitcnt lgkmcnt(0)\n"
    "v_mfma_f32_16x16x32_bf16 v[130:133], v[2:5], v[18:21], v[130:133]\n"
    "ds_read_b128 v[58:61], v218 offset:0\n"
    "ds_read_b128 v[62:65], v209 offset:2048\n"
    "v_mfma_f32_16x16x32_bf16 v[134:137], v[2:5], v[22:25], v[134:137]\n"
    "ds_read_b128 v[42:45], v231 offset:11264\n"
    "v_add_f32 v86, 1.0, v86\n"
    "v_mfma_f32_16x16x32_bf16 v[138:141], v[6:9], v[18:21], v[138:141]\n"
    "ds_read_b128 v[46:49], v231 offset:12288\n"
    "v_add_f32 v87, 1.0, v87\n"
    "v_mfma_f32_16x16x32_bf16 v[142:145], v[6:9], v[22:25], v[142:145]\n"
    "ds_read_b128 v[50:53], v231 offset:13312\n"
    "v_add_f32 v88, 1.0, v88\n"
    "v_mfma_f32_16x16x32_bf16 v[146:149], v[10:13], v[18:21], v[146:149]\n"
    "ds_read_b128 v[54:57], v222 offset:14336\n"
    "v_add_f32 v89, 1.0, v89\n"
    "v_mfma_f32_16x16x32_bf16 v[150:153], v[10:13], v[22:25], v[150:153]\n"
    "v_rcp_f32 v82, v82\n"
    "v_mfma_f32_16x16x32_bf16 v[154:157], v[14:17], v[18:21], v[154:157]\n"
    "v_rcp_f32 v83, v83\n"
    "v_mfma_f32_16x16x32_bf16 v[158:161], v[14:17], v[22:25], v[158:161]\n"
    "v_rcp_f32 v84, v84\n"
    "s_lshl_b32 s50, s46, 8\n"
    "s_lshl_b32 s51, s47, 5\n"
    "s_add_u32 s50, s50, s51\n"
    "s_lshl_b32 s51, s45, 3\n"
    "s_add_u32 s50, s50, s51\n"
    "s_add_u32 s50, s50, 0x8000\n"
    "s_waitcnt vmcnt(0)\n"
    "s_mov_b64 exec, 1\n"
    "buffer_store_dwordx2 v[214:215], off, s[36:39], s50 sc1\n"
    "s_mov_b64 exec, -1\n"
    "s_waitcnt lgkmcnt(0)\n"
    "s_barrier\n"
    "v_mfma_f32_16x16x32_bf16 v[130:133], v[42:45], v[58:61], v[130:133]\n"
    "ds_read_b128 v[18:21], v209 offset:8192\n"
    "ds_read_b128 v[22:25], v218 offset:10240\n"
    "v_mfma_f32_16x16x32_bf16 v[134:137], v[42:45], v[62:65], v[134:137]\n"
    "ds_read_b128 v[2:5], v231 offset:18432\n"
    "v_mfma_f32_16x16x32_bf16 v[138:141], v[46:49], v[58:61], v[138:141]\n"
    "ds_read_b128 v[6:9], v231 offset:19456\n"
    "v_rcp_f32 v85, v85\n"
    "v_mfma_f32_16x16x32_bf16 v[142:145], v[46:49], v[62:65], v[142:145]\n"
    "ds_read_b128 v[10:13], v231 offset:20480\n"
    "v_mfma_f32_16x16x32_bf16 v[146:149], v[50:53], v[58:61], v[146:149]\n"
    "ds_read_b128 v[14:17], v222 offset:21504\n"
    "v_rcp_f32 v86, v86\n"
    "v_mfma_f32_16x16x32_bf16 v[150:153], v[50:53], v[62:65], v[150:153]\n"
    "v_rcp_f32 v87, v87\n"
    "v_mfma_f32_16x16x32_bf16 v[154:157], v[54:57], v[58:61], v[154:157]\n"
    "v_rcp_f32 v88, v88\n"
    "v_mfma_f32_16x16x32_bf16 v[158:161], v[54:57], v[62:65], v[158:161]\n"
    "v_rcp_f32 v89, v89\n"
    "s_waitcnt lgkmcnt(0)\n"
    "v_mfma_f32_16x16x32_bf16 v[130:133], v[2:5], v[18:21], v[130:133]\n"
    "ds_read_b128 v[58:61], v218 offset:8192\n"
    "ds_read_b128 v[62:65], v209 offset:10240\n"
    "v_mfma_f32_16x16x32_bf16 v[134:137], v[2:5], v[22:25], v[134:137]\n"
    "ds_read_b128 v[42:45], v231 offset:25600\n"
    "v_mul_f32 v114, v114, v82\n"
    "v_mfma_f32_16x16x32_bf16 v[138:141], v[6:9], v[18:21], v[138:141]\n"
    "ds_read_b128 v[46:49], v231 offset:26624\n"
    "v_mul_f32 v115, v115, v83\n"
    "v_mfma_f32_16x16x32_bf16 v[142:145], v[6:9], v[22:25], v[142:145]\n"
    "ds_read_b128 v[50:53], v231 offset:27648\n"
    "v_mul_f32 v116, v116, v84\n"
    "v_mfma_f32_16x16x32_bf16 v[146:149], v[10:13], v[18:21], v[146:149]\n"
    "ds_read_b128 v[54:57], v222 offset:28672\n"
    "v_mul_f32 v117, v117, v85\n"
    "v_mfma_f32_16x16x32_bf16 v[150:153], v[10:13], v[22:25], v[150:153]\n"
    "v_mul_f32 v118, v118, v86\n"
    "v_mul_f32 v119, v119, v87\n"
    "v_mfma_f32_16x16x32_bf16 v[154:157], v[14:17], v[18:21], v[154:157]\n"
    "v_mul_f32 v120, v120, v88\n"
    "v_mul_f32 v121, v121, v89\n"
    "v_mfma_f32_16x16x32_bf16 v[158:161], v[14:17], v[22:25], v[158:161]\n"
    "v_mul_f32 v82, v122, v122\n"
    "v_mul_f32 v83, v123, v123\n"
    "s_waitcnt lgkmcnt(0)\n"
    "s_barrier\n"
    "v_mfma_f32_16x16x32_bf16 v[130:133], v[42:45], v[58:61], v[130:133]\n"
    "ds_read_b128 v[18:21], v209 offset:16384\n"
    "ds_read_b128 v[22:25], v218 offset:18432\n"
    "v_mfma_f32_16x16x32_bf16 v[134:137], v[42:45], v[62:65], v[134:137]\n"
    "ds_read_b128 v[2:5], v230 offset:30720\n"
    "v_mul_f32 v84, v124, v124\n"
    "v_mfma_f32_16x16x32_bf16 v[138:141], v[46:49], v[58:61], v[138:141]\n"
    "ds_read_b128 v[6:9], v230 offset:31744\n"
    "v_mul_f32 v85, v125, v125\n"
    "v_mfma_f32_16x16x32_bf16 v[142:145], v[46:49], v[62:65], v[142:145]\n"
    "ds_read_b128 v[10:13], v230 offset:32768\n"
    "v_mul_f32 v86, v126, v126\n"
    "v_mfma_f32_16x16x32_bf16 v[146:149], v[50:53], v[58:61], v[146:149]\n"
    "ds_read_b128 v[14:17], v221 offset:33792\n"
    "v_mul_f32 v87, v127, v127\n"
    "v_mfma_f32_16x16x32_bf16 v[150:153], v[50:53], v[62:65], v[150:153]\n"
    "v_mul_f32 v88, v128, v128\n"
    "v_mul_f32 v89, v129, v129\n"
    "v_mfma_f32_16x16x32_bf16 v[154:157], v[54:57], v[58:61], v[154:157]\n"
    "v_fma_f32 v82, v82, v240, v238\n"
    "v_fma_f32 v83, v83, v240, v238\n"
    "v_mfma_f32_16x16x32_bf16 v[158:161], v[54:57], v[62:65], v[158:161]\n"
    "v_fma_f32 v84, v84, v240, v238\n"
    "v_fma_f32 v85, v85, v240, v238\n"
    "s_waitcnt lgkmcnt(0)\n"
    "v_mfma_f32_16x16x32_bf16 v[130:133], v[2:5], v[18:21], v[130:133]\n"
    "ds_read_b128 v[58:61], v218 offset:16384\n"
    "ds_read_b128 v[62:65], v209 offset:18432\n"
    "v_mfma_f32_16x16x32_bf16 v[134:137], v[2:5], v[22:25], v[134:137]\n"
    "ds_read_b128 v[42:45], v230 offset:37888\n"
    "v_fma_f32 v86, v86, v240, v238\n"
    "v_mfma_f32_16x16x32_bf16 v[138:141], v[6:9], v[18:21], v[138:141]\n"
    "ds_read_b128 v[46:49], v230 offset:38912\n"
    "v_fma_f32 v87, v87, v240, v238\n"
    "v_mfma_f32_16x16x32_bf16 v[142:145], v[6:9], v[22:25], v[142:145]\n"
    "ds_read_b128 v[50:53], v230 offset:39936\n"
    "v_fma_f32 v88, v88, v240, v238\n"
    "v_mfma_f32_16x16x32_bf16 v[146:149], v[10:13], v[18:21], v[146:149]\n"
    "ds_read_b128 v[54:57], v221 offset:40960\n"
    "v_fma_f32 v89, v89, v240, v238\n"
    "v_mfma_f32_16x16x32_bf16 v[150:153], v[10:13], v[22:25], v[150:153]\n"
    "v_mul_f32 v82, v82, v122\n"
    "v_mul_f32 v83, v83, v123\n"
    "v_mfma_f32_16x16x32_bf16 v[154:157], v[14:17], v[18:21], v[154:157]\n"
    "v_mul_f32 v84, v84, v124\n"
    "v_mul_f32 v85, v85, v125\n"
    "v_mfma_f32_16x16x32_bf16 v[158:161], v[14:17], v[22:25], v[158:161]\n"
    "v_mul_f32 v86, v86, v126\n"
    "v_mul_f32 v87, v87, v127\n"
    "s_waitcnt lgkmcnt(0)\n"
    "s_barrier\n"
    "v_mfma_f32_16x16x32_bf16 v[130:133], v[42:45], v[58:61], v[130:133]\n"
    "ds_read_b128 v[18:21], v209 offset:24576\n"
    "ds_read_b128 v[22:25], v218 offset:26624\n"
    "v_mfma_f32_16x16x32_bf16 v[134:137], v[42:45], v[62:65], v[134:137]\n"
    "ds_read_b128 v[2:5], v230 offset:55296\n"
    "v_mul_f32 v88, v88, v128\n"
    "v_mfma_f32_16x16x32_bf16 v[138:141], v[46:49], v[58:61], v[138:141]\n"
    "ds_read_b128 v[6:9], v230 offset:56320\n"
    "v_mul_f32 v89, v89, v129\n"
    "v_mfma_f32_16x16x32_bf16 v[142:145], v[46:49], v[62:65], v[142:145]\n"
    "ds_read_b128 v[10:13], v230 offset:57344\n"
    "v_mfma_f32_16x16x32_bf16 v[146:149], v[50:53], v[58:61], v[146:149]\n"
    "ds_read_b128 v[14:17], v221 offset:58368\n"
    "v_exp_f32 v82, v82\n"
    "v_mfma_f32_16x16x32_bf16 v[150:153], v[50:53], v[62:65], v[150:153]\n"
    "v_exp_f32 v83, v83\n"
    "v_mfma_f32_16x16x32_bf16 v[154:157], v[54:57], v[58:61], v[154:157]\n"
    "v_exp_f32 v84, v84\n"
    "v_mfma_f32_16x16x32_bf16 v[158:161], v[54:57], v[62:65], v[158:161]\n"
    "v_exp_f32 v85, v85\n"
    "s_waitcnt lgkmcnt(0)\n"
    "v_mfma_f32_16x16x32_bf16 v[130:133], v[2:5], v[18:21], v[130:133]\n"
    "ds_read_b128 v[58:61], v218 offset:24576\n"
    "ds_read_b128 v[62:65], v209 offset:26624\n"
    "v_mfma_f32_16x16x32_bf16 v[134:137], v[2:5], v[22:25], v[134:137]\n"
    "ds_read_b128 v[42:45], v230 offset:62464\n"
    "v_mfma_f32_16x16x32_bf16 v[138:141], v[6:9], v[18:21], v[138:141]\n"
    "ds_read_b128 v[46:49], v230 offset:63488\n"
    "v_exp_f32 v86, v86\n"
    "v_mfma_f32_16x16x32_bf16 v[142:145], v[6:9], v[22:25], v[142:145]\n"
    "ds_read_b128 v[50:53], v230 offset:64512\n"
    "v_mfma_f32_16x16x32_bf16 v[146:149], v[10:13], v[18:21], v[146:149]\n"
    "ds_read_b128 v[54:57], v222 offset:0\n"
    "v_exp_f32 v87, v87\n"
    "v_mfma_f32_16x16x32_bf16 v[150:153], v[10:13], v[22:25], v[150:153]\n"
    "v_exp_f32 v88, v88\n"
    "v_mfma_f32_16x16x32_bf16 v[154:157], v[14:17], v[18:21], v[154:157]\n"
    "v_exp_f32 v89, v89\n"
    "v_mfma_f32_16x16x32_bf16 v[158:161], v[14:17], v[22:25], v[158:161]\n"
    "v_add_f32 v82, 1.0, v82\n"
    "v_add_f32 v83, 1.0, v83\n"
    "s_xor_b32 s50, s46, 1\n"
    "s_lshl_b32 s50, s50, 3\n"
    "s_add_u32 s50, s50, s47\n"
    "s_lshl_b32 s50, s50, 1\n"
    "s_add_u32 s50, s50, s56\n"
    "s_lshl_b32 s50, s50, 4\n"
    "s_add_u32 s50, s50, 0x8000\n"
    "buffer_load_dwordx4 v[178:181], off, s[36:39], s50 sc1\n"
    "s_waitcnt lgkmcnt(0)\n"
    "s_barrier\n"
    "v_mfma_f32_16x16x32_bf16 v[130:133], v[42:45], v[58:61], v[130:133]\n"
    "ds_read_b128 v[18:21], v209 offset:32768\n"
    "ds_read_b128 v[22:25], v218 offset:34816\n"
    "v_mfma_f32_16x16x32_bf16 v[134:137], v[42:45], v[62:65], v[134:137]\n"
    "ds_read_b128 v[2:5], v231 offset:4096\n"
    "v_add_f32 v84, 1.0, v84\n"
    "v_mfma_f32_16x16x32_bf16 v[138:141], v[46:49], v[58:61], v[138:141]\n"
    "ds_read_b128 v[6:9], v231 offset:5120\n"
    "v_add_f32 v85, 1.0, v85\n"
    "v_mfma_f32_16x16x32_bf16 v[142:145], v[46:49], v[62:65], v[142:145]\n"
    "ds_read_b128 v[10:13], v231 offset:6144\n"
    "v_add_f32 v86, 1.0, v86\n"
    "v_mfma_f32_16x16x32_bf16 v[146:149], v[50:53], v[58:61], v[146:149]\n"
    "ds_read_b128 v[14:17], v222 offset:7168\n"
    "v_add_f32 v87, 1.0, v87\n"
    "v_mfma_f32_16x16x32_bf16 v[150:153], v[50:53], v[62:65], v[150:153]\n"
    "v_add_f32 v88, 1.0, v88\n"
    "v_add_f32 v89, 1.0, v89\n"
    "v_mfma_f32_16x16x32_bf16 v[154:157], v[54:57], v[58:61], v[154:157]\n"
    "v_rcp_f32 v82, v82\n"
    "v_mfma_f32_16x16x32_bf16 v[158:161], v[54:57], v[62:65], v[158:161]\n"
    "v_rcp_f32 v83, v83\n"
    "s_waitcnt lgkmcnt(0)\n"
    "v_mfma_f32_16x16x32_bf16 v[130:133], v[2:5], v[18:21], v[130:133]\n"
    "ds_read_b128 v[58:61], v218 offset:32768\n"
    "ds_read_b128 v[62:65], v209 offset:34816\n"
    "v_mfma_f32_16x16x32_bf16 v[134:137], v[2:5], v[22:25], v[134:137]\n"
    "ds_read_b128 v[42:45], v231 offset:11264\n"
    "v_mfma_f32_16x16x32_bf16 v[138:141], v[6:9], v[18:21], v[138:141]\n"
    "ds_read_b128 v[46:49], v231 offset:12288\n"
    "v_rcp_f32 v84, v84\n"
    "v_mfma_f32_16x16x32_bf16 v[142:145], v[6:9], v[22:25], v[142:145]\n"
    "ds_read_b128 v[50:53], v231 offset:13312\n"
    "v_mfma_f32_16x16x32_bf16 v[146:149], v[10:13], v[18:21], v[146:149]\n"
    "ds_read_b128 v[54:57], v222 offset:14336\n"
    "v_rcp_f32 v85, v85\n"
    "v_mfma_f32_16x16x32_bf16 v[150:153], v[10:13], v[22:25], v[150:153]\n"
    "v_rcp_f32 v86, v86\n"
    "v_mfma_f32_16x16x32_bf16 v[154:157], v[14:17], v[18:21], v[154:157]\n"
    "v_rcp_f32 v87, v87\n"
    "v_mfma_f32_16x16x32_bf16 v[158:161], v[14:17], v[22:25], v[158:161]\n"
    "v_rcp_f32 v88, v88\n"
    "s_waitcnt vmcnt(0)\n"
    "v_cmp_eq_u32 vcc, s48, v178\n"
    "v_cmp_eq_u32_e64 s[64:65], s49, v179\n"
    "s_nop 0\n"
    "s_and_b64 vcc, vcc, s[64:65]\n"
    "v_cmp_eq_u32_e64 s[64:65], s48, v180\n"
    "s_nop 0\n"
    "s_and_b64 vcc, vcc, s[64:65]\n"
    "v_cmp_eq_u32_e64 s[64:65], s49, v181\n"
    "s_nop 0\n"
    "s_and_b64 vcc, vcc, s[64:65]\n"
    "s_cmp_eq_u64 vcc, -1\n"
    "s_cbranch_scc1 L_pok4_%=\n"
    "s_mov_b32 s63, 0x20000\n"
    "L_pl4_%=:\n"
    "s_xor_b32 s50, s46, 1\n"
    "s_lshl_b32 s50, s50, 3\n"
    "s_add_u32 s50, s50, s47\n"
    "s_lshl_b32 s50, s50, 1\n"
    "s_add_u32 s50, s50, s56\n"
    "s_lshl_b32 s50, s50, 4\n"
    "s_add_u32 s50, s50, 0x8000\n"
    "buffer_load_dwordx4 v[178:181], off, s[36:39], s50 sc1\n"
    "s_waitcnt vmcnt(0)\n"
    "v_cmp_eq_u32 vcc, s48, v178\n"
    "v_cmp_eq_u32_e64 s[64:65], s49, v179\n"
    "s_nop 0\n"
    "s_and_b64 vcc, vcc, s[64:65]\n"
    "v_cmp_eq_u32_e64 s[64:65], s48, v180\n"
    "s_nop 0\n"
    "s_and_b64 vcc, vcc, s[64:65]\n"
    "v_cmp_eq_u32_e64 s[64:65], s49, v181\n"
    "s_nop 0\n"
    "s_and_b64 vcc, vcc, s[64:65]\n"
    "s_cmp_eq_u64 vcc, -1\n"
    "s_cbranch_scc1 L_pok4_%=\n"
    "s_sleep 1\n"
    "s_sub_u32 s63, s63, 1\n"
    "s_cmp_lg_u32 s63, 0\n"
    "s_cbranch_scc1 L_pl4_%=\n"
    "L_pok4_%=:\n"
    "s_xor_b32 s63, s46, 0\n"
    "s_lshl_b32 s63, s63, 3\n"
    "s_add_u32 s63, s63, s47\n"
    "s_lshl_b32 s63, s63, 2\n"
    "s_add_u32 s63, s63, s45\n"
    "s_lshl_b32 s63, s63, 12\n"
    "s_add_u32 s63, s63, 0xb00000\n"
    "s_lshl_b32 s62, s55, 26\n"
    "s_add_u32 s62, s62, s63\n"
    "buffer_load_dwordx4 v[162:165], v234, s[36:39], s63 offen offset:0 sc1\n"
    "buffer_load_dwordx4 v[166:169], v234, s[36:39], s63 offen offset:1024 sc1\n"
    "buffer_load_dwordx4 v[170:173], v234, s[36:39], s63 offen offset:2048 sc1\n"
    "buffer_load_dwordx4 v[174:177], v234, s[36:39], s62 offen offset:3072 sc1\n"
    "v_mov_b32 v182, 0x3f80\n"
    "v_mov_b32 v183, 0x3f800000\n"
    "s_waitcnt lgkmcnt(0)\n"
    "s_barrier\n"
    "v_rcp_f32 v89, v89\n"
    "v_mul_f32 v122, v122, v82\n"
    "v_mul_f32 v123, v123, v83\n"
    "v_mul_f32 v124, v124, v84\n"
    "v_mul_f32 v125, v125, v85\n"
    "v_mul_f32 v126, v126, v86\n"
    "v_mul_f32 v127, v127, v87\n"
    "v_mul_f32 v128, v128, v88\n"
    "v_mul_f32 v129, v129, v89\n"
    "v_mfma_f32_16x16x32_bf16 v[130:133], v[42:45], v[58:61], v[130:133]\n"
    "ds_read_b64 v[18:19], v223 offset:8192\n"
    "ds_read_b64 v[20:21], v223 offset:10240\n"
    "ds_read_b64 v[22:23], v223 offset:8704\n"
    "v_mfma_f32_16x16x32_bf16 v[134:137], v[42:45], v[62:65], v[134:137]\n"
    "ds_read_b64 v[24:25], v223 offset:10752\n"
    "ds_read_b64_tr_b16 v[2:3], v227 offset:0\n"
    "v_mfma_f32_16x16x32_bf16 v[138:141], v[46:49], v[58:61], v[138:141]\n"
    "ds_read_b64_tr_b16 v[4:5], v227 offset:2048\n"
    "ds_read_b64_tr_b16 v[6:7], v232 offset:0\n"
    "ds_read_b64_tr_b16 v[8:9], v232 offset:2048\n"
    "v_mfma_f32_16x16x32_bf16 v[142:145], v[46:49], v[62:65], v[142:145]\n"
    "ds_read_b64_tr_b16 v[10:11], v237 offset:0\n"
    "ds_read_b64_tr_b16 v[12:13], v237 offset:2048\n"
    "v_mfma_f32_16x16x32_bf16 v[146:149], v[50:53], v[58:61], v[146:149]\n"
    "ds_read_b64_tr_b16 v[14:15], v244 offset:0\n"
    "ds_read_b64_tr_b16 v[16:17], v244 offset:2048\n"
    "v_mfma_f32_16x16x32_bf16 v[150:153], v[50:53], v[62:65], v[150:153]\n"
    "v_mfma_f32_16x16x32_bf16 v[154:157], v[54:57], v[58:61], v[154:157]\n"
    "v_mfma_f32_16x16x32_bf16 v[158:161], v[54:57], v[62:65], v[158:161]\n"
    "s_waitcnt vmcnt(0)\n"
    "s_mul_i32 s50, s46, 114688\n"
    "s_mul_i32 s51, s47, 14336\n"
    "s_add_u32 s50, s50, s51\n"
    "s_lshl_b32 s51, s56, 10\n"
    "s_add_u32 s50, s50, s51\n"
    "s_add_u32 s50, s50, 0x500000\n"
    "s_lshl_b32 s51, s53, 11\n"
    "s_add_u32 s51, s51, s50\n"
    "v_dot2_f32_bf16 v130, v162, v182, v130\n"
    "v_dot2_f32_bf16 v131, v162, v183, v131\n"
    "v_dot2_f32_bf16 v132, v163, v182, v132\n"
    "v_dot2_f32_bf16 v133, v163, v183, v133\n"
    "v_dot2_f32_bf16 v134, v164, v182, v134\n"
    "v_dot2_f32_bf16 v135, v164, v183, v135\n"
    "v_dot2_f32_bf16 v136, v165, v182, v136\n"
    "v_dot2_f32_bf16 v137, v165, v183, v137\n"
    "v_pk_mul_f32 v[130:131], v[130:131], v[98:99]\n"
    "v_pk_mul_f32 v[132:133], v[132:133], v[100:101]\n"
    "v_pk_mul_f32 v[134:135], v[134:135], v[102:103]\n"
    "v_pk_mul_f32 v[136:137], v[136:137], v[104:105]\n"
    "v_cvt_pk_bf16_f32 v66, v130, v131\n"
    "v_cvt_pk_bf16_f32 v67, v132, v133\n"
    "v_cvt_pk_bf16_f32 v68, v134, v135\n"
    "v_cvt_pk_bf16_f32 v69, v136, v137\n"
    "buffer_store_dwordx2 v[66:67], v233, s[36:39], s51 offen offset:0 sc1\n"
    "buffer_store_dwordx2 v[68:69], v233, s[36:39], s51 offen offset:512 sc1\n"
    "s_lshl_b32 s51, s54, 11\n"
    "s_add_u32 s51, s51, s50\n"
    "v_dot2_f32_bf16 v138, v166, v182, v138\n"
    "v_dot2_f32_bf16 v139, v166, v183, v139\n"
    "v_dot2_f32_bf16 v140, v167, v182, v140\n"
    "v_dot2_f32_bf16 v141, v167, v183, v141\n"
    "v_dot2_f32_bf16 v142, v168, v182, v142\n"
    "v_dot2_f32_bf16 v143, v168, v183, v143\n"
    "v_dot2_f32_bf16 v144, v169, v182, v144\n"
    "v_dot2_f32_bf16 v145, v169, v183, v145\n"
    "v_pk_mul_f32 v[138:139], v[138:139], v[106:107]\n"
    "v_pk_mul_f32 v[140:141], v[140:141], v[108:109]\n"
    "v_pk_mul_f32 v[142:143], v[142:143], v[110:111]\n"
    "v_pk_mul_f32 v[144:145], v[144:145], v[112:113]\n"
    "v_cvt_pk_bf16_f32 v70, v138, v139\n"
    "v_cvt_pk_bf16_f32 v71, v140, v141\n"
    "v_cvt_pk_bf16_f32 v72, v142, v143\n"
    "v_cvt_pk_bf16_f32 v73, v144, v145\n"
    "buffer_store_dwordx2 v[70:71], v233, s[36:39], s51 offen offset:0 sc1\n"
    "buffer_store_dwordx2 v[72:73], v233, s[36:39], s51 offen offset:512 sc1\n"
    "s_lshl_b32 s51, s60, 11\n"
    "s_add_u32 s51, s51, s50\n"
    "v_dot2_f32_bf16 v146, v170, v182, v146\n"
    "v_dot2_f32_bf16 v147, v170, v183, v147\n"
    "v_dot2_f32_bf16 v148, v171, v182, v148\n"
    "v_dot2_f32_bf16 v149, v171, v183, v149\n"
    "v_dot2_f32_bf16 v150, v172, v182, v150\n"
    "v_dot2_f32_bf16 v151, v172, v183, v151\n"
    "v_dot2_f32_bf16 v152, v173, v182, v152\n"
    "v_dot2_f32_bf16 v153, v173, v183, v153\n"
    "v_pk_mul_f32 v[146:147], v[146:147], v[114:115]\n"
    "v_pk_mul_f32 v[148:149], v[148:149], v[116:117]\n"
    "v_pk_mul_f32 v[150:151], v[150:151], v[118:119]\n"
    "v_pk_mul_f32 v[152:153], v[152:153], v[120:121]\n"
    "v_cvt_pk_bf16_f32 v74, v146, v147\n"
    "v_cvt_pk_bf16_f32 v75, v148, v149\n"
    "v_cvt_pk_bf16_f32 v76, v150, v151\n"
    "v_cvt_pk_bf16_f32 v77, v152, v153\n"
    "buffer_store_dwordx2 v[74:75], v233, s[36:39], s51 offen offset:0 sc1\n"
    "buffer_store_dwordx2 v[76:77], v233, s[36:39], s51 offen offset:512 sc1\n"
    "s_lshl_b32 s51, s61, 11\n"
    "s_add_u32 s51, s51, s50\n"
    "v_dot2_f32_bf16 v154, v174, v182, v154\n"
    "v_dot2_f32_bf16 v155, v174, v183, v155\n"
    "v_dot2_f32_bf16 v156, v175, v182, v156\n"
    "v_dot2_f32_bf16 v157, v175, v183, v157\n"
    "v_dot2_f32_bf16 v158, v176, v182, v158\n"
    "v_dot2_f32_bf16 v159, v176, v183, v159\n"
    "v_dot2_f32_bf16 v160, v177, v182, v160\n"
    "v_dot2_f32_bf16 v161, v177, v183, v161\n"
    "v_pk_mul_f32 v[154:155], v[154:155], v[122:123]\n"
    "v_pk_mul_f32 v[156:157], v[156:157], v[124:125]\n"
    "v_pk_mul_f32 v[158:159], v[158:159], v[126:127]\n"
    "v_pk_mul_f32 v[160:161], v[160:161], v[128:129]\n"
    "v_cvt_pk_bf16_f32 v78, v154, v155\n"
    "v_cvt_pk_bf16_f32 v79, v156, v157\n"
    "v_cvt_pk_bf16_f32 v80, v158, v159\n"
    "v_cvt_pk_bf16_f32 v81, v160, v161\n"
    "buffer_store_dwordx2 v[78:79], v233, s[36:39], s51 offen offset:0 sc1\n"
    "buffer_store_dwordx2 v[80:81], v233, s[36:39], s51 offen offset:512 sc1\n"
    "s_lshl_b32 s50, s46, 8\n"
    "s_lshl_b32 s51, s47, 5\n"
    "s_add_u32 s50, s50, s51\n"
    "s_lshl_b32 s51, s45, 3\n"
    "s_add_u32 s50, s50, s51\n"
    "s_add_u32 s50, s50, 0x6000\n"
    "s_waitcnt vmcnt(0)\n"
    "s_mov_b64 exec, 1\n"
    "buffer_store_dwordx2 v[214:215], off, s[36:39], s50 sc1\n"
    "s_mov_b64 exec, -1\n"
    "s_waitcnt lgkmcnt(0)\n"
    "s_barrier\n"
    "v_mfma_f32_16x16x32_bf16 v[162:165], v[2:5], v[18:21], 0\n"
    "ds_read_b64 v[42:43], v223 offset:49152\n"
    "ds_read_b64 v[44:45], v223 offset:51200\n"
    "ds_read_b64 v[46:47], v223 offset:49664\n"
    "v_mfma_f32_16x16x32_bf16 v[178:181], v[2:5], v[22:25], 0\n"
    "ds_read_b64 v[48:49], v223 offset:51712\n"
    "ds_read_b64_tr_b16 v[26:27], v227 offset:40960\n"
    "v_mfma_f32_16x16x32_bf16 v[166:169], v[6:9], v[18:21], 0\n"
    "ds_read_b64_tr_b16 v[28:29], v227 offset:43008\n"
    "ds_read_b64_tr_b16 v[30:31], v232 offset:40960\n"
    "ds_read_b64_tr_b16 v[32:33], v232 offset:43008\n"
    "v_mfma_f32_16x16x32_bf16 v[182:185], v[6:9], v[22:25], 0\n"
    "ds_read_b64_tr_b16 v[34:35], v237 offset:40960\n"
    "ds_read_b64_tr_b16 v[36:37], v237 offset:43008\n"
    "v_mfma_f32_16x16x32_bf16 v[170:173], v[10:13], v[18:21], 0\n"
    "ds_read_b64_tr_b16 v[38:39], v244 offset:40960\n"
    "ds_read_b64_tr_b16 v[40:41], v244 offset:43008\n"
    "v_mfma_f32_16x16x32_bf16 v[186:189], v[10:13], v[22:25], 0\n"
    "v_mfma_f32_16x16x32_bf16 v[174:177], v[14:17], v[18:21], 0\n"
    "v_mfma_f32_16x16x32_bf16 v[190:193], v[14:17], v[22:25], 0\n"
    "s_waitcnt lgkmcnt(0)\n"
    "v_mfma_f32_16x16x32_bf16 v[162:165], v[26:29], v[42:45], v[162:165]\n"
    "ds_read_b64 v[18:19], v223 offset:57344\n"
    "ds_read_b64 v[20:21], v223 offset:59392\n"
    "ds_read_b64 v[22:23], v223 offset:57856\n"
    "v_mfma_f32_16x16x32_bf16 v[178:181], v[26:29], v[46:49], v[178:181]\n"
    "ds_read_b64 v[24:25], v223 offset:59904\n"
    "ds_read_b64_tr_b16 v[2:3], v227 offset:16384\n"
    "v_mfma_f32_16x16x32_bf16 v[166:169], v[30:33], v[42:45], v[166:169]\n"
    "ds_read_b64_tr_b16 v[4:5], v227 offset:18432\n"
    "ds_read_b64_tr_b16 v[6:7], v232 offset:16384\n"
    "ds_read_b64_tr_b16 v[8:9], v232 offset:18432\n"
    "v_mfma_f32_16x16x32_bf16 v[182:185], v[30:33], v[46:49], v[182:185]\n"
    "ds_read_b64_tr_b16 v[10:11], v237 offset:16384\n"
    "ds_read_b64_tr_b16 v[12:13], v237 offset:18432\n"
    "v_mfma_f32_16x16x32_bf16 v[170:173], v[34:37], v[42:45], v[170:173]\n"
    "ds_read_b64_tr_b16 v[14:15], v244 offset:16384\n"
    "ds_read_b64_tr_b16 v[16:17], v244 offset:18432\n"
    "v_mfma_f32_16x16x32_bf16 v[186:189], v[34:37], v[46:49], v[186:189]\n"
    "v_mfma_f32_16x16x32_bf16 v[174:177], v[38:41], v[42:45], v[174:177]\n"
    "v_mfma_f32_16x16x32_bf16 v[190:193], v[38:41], v[46:49], v[190:193]\n"
    "s_waitcnt lgkmcnt(0)\n"
    "s_barrier\n"
    "v_mfma_f32_16x16x32_bf16 v[162:165], v[2:5], v[18:21], v[162:165]\n"
    "ds_read_b64 v[42:43], v223 offset:24576\n"
    "ds_read_b64 v[44:45], v223 offset:26624\n"
    "ds_read_b64 v[46:47], v223 offset:25088\n"
    "v_mfma_f32_16x16x32_bf16 v[178:181], v[2:5], v[22:25], v[178:181]\n"
    "ds_read_b64 v[48:49], v223 offset:27136\n"
    "ds_read_b64_tr_b16 v[26:27], v228 offset:0\n"
    "v_mfma_f32_16x16x32_bf16 v[166:169], v[6:9], v[18:21], v[166:169]\n"
    "ds_read_b64_tr_b16 v[28:29], v228 offset:2048\n"
    "ds_read_b64_tr_b16 v[30:31], v235 offset:0\n"
    "ds_read_b64_tr_b16 v[32:33], v235 offset:2048\n"
    "v_mfma_f32_16x16x32_bf16 v[182:185], v[6:9], v[22:25], v[182:185]\n"
    "ds_read_b64_tr_b16 v[34:35], v242 offset:0\n"
    "ds_read_b64_tr_b16 v[36:37], v242 offset:2048\n"
    "v_mfma_f32_16x16x32_bf16 v[170:173], v[10:13], v[18:21], v[170:173]\n"
    "ds_read_b64_tr_b16 v[38:39], v245 offset:0\n"
    "ds_read_b64_tr_b16 v[40:41], v245 offset:2048\n"
    "v_mfma_f32_16x16x32_bf16 v[186:189], v[10:13], v[22:25], v[186:189]\n"
    "v_mfma_f32_16x16x32_bf16 v[174:177], v[14:17], v[18:21], v[174:177]\n"
    "v_mfma_f32_16x16x32_bf16 v[190:193], v[14:17], v[22:25], v[190:193]\n"
    "s_waitcnt lgkmcnt(0)\n"
    "v_mfma_f32_16x16x32_bf16 v[162:165], v[26:29], v[42:45], v[162:165]\n"
    "ds_read_b64 v[18:19], v224 offset:16384\n"
    "ds_read_b64 v[20:21], v224 offset:18432\n"
    "ds_read_b64 v[22:23], v224 offset:16896\n"
    "v_mfma_f32_16x16x32_bf16 v[178:181], v[26:29], v[46:49], v[178:181]\n"
    "ds_read_b64 v[24:25], v224 offset:18944\n"
    "ds_read_b64_tr_b16 v[2:3], v228 offset:8192\n"
    "v_mfma_f32_16x16x32_bf16 v[166:169], v[30:33], v[42:45], v[166:169]\n"
    "ds_read_b64_tr_b16 v[4:5], v228 offset:10240\n"
    "ds_read_b64_tr_b16 v[6:7], v235 offset:8192\n"
    "ds_read_b64_tr_b16 v[8:9], v235 offset:10240\n"
    "v_mfma_f32_16x16x32_bf16 v[182:185], v[30:33], v[46:49], v[182:185]\n"
    "ds_read_b64_tr_b16 v[10:11], v242 offset:8192\n"
    "ds_read_b64_tr_b16 v[12:13], v242 offset:10240\n"
    "v_mfma_f32_16x16x32_bf16 v[170:173], v[34:37], v[42:45], v[170:173]\n"
    "ds_read_b64_tr_b16 v[14:15], v245 offset:8192\n"
    "ds_read_b64_tr_b16 v[16:17], v245 offset:10240\n"
    "v_mfma_f32_16x16x32_bf16 v[186:189], v[34:37], v[46:49], v[186:189]\n"
    "v_mfma_f32_16x16x32_bf16 v[174:177], v[38:41], v[42:45], v[174:177]\n"
    "v_mfma_f32_16x16x32_bf16 v[190:193], v[38:41], v[46:49], v[190:193]\n"
    "s_waitcnt lgkmcnt(0)\n"
    "s_barrier\n"
    "v_mfma_f32_16x16x32_bf16 v[162:165], v[2:5], v[18:21], v[162:165]\n"
    "ds_read_b64 v[42:43], v223 offset:8192\n"
    "ds_read_b64 v[44:45], v223 offset:10240\n"
    "ds_read_b64 v[46:47], v223 offset:8704\n"
    "v_mfma_f32_16x16x32_bf16 v[178:181], v[2:5], v[22:25], v[178:181]\n"
    "ds_read_b64 v[48:49], v223 offset:10752\n"
    "ds_read_b64_tr_b16 v[26:27], v227 offset:0\n"
    "v_mfma_f32_16x16x32_bf16 v[166:169], v[6:9], v[18:21], v[166:169]\n"
    "ds_read_b64_tr_b16 v[28:29], v227 offset:2048\n"
    "ds_read_b64_tr_b16 v[30:31], v232 offset:0\n"
    "ds_read_b64_tr_b16 v[32:33], v232 offset:2048\n"
    "v_mfma_f32_16x16x32_bf16 v[182:185], v[6:9], v[22:25], v[182:185]\n"
    "ds_read_b64_tr_b16 v[34:35], v237 offset:0\n"
    "ds_read_b64_tr_b16 v[36:37], v237 offset:2048\n"
    "v_mfma_f32_16x16x32_bf16 v[170:173], v[10:13], v[18:21], v[170:173]\n"
    "ds_read_b64_tr_b16 v[38:39], v244 offset:0\n"
    "ds_read_b64_tr_b16 v[40:41], v244 offset:2048\n"
    "v_mfma_f32_16x16x32_bf16 v[186:189], v[10:13], v[22:25], v[186:189]\n"
    "v_mfma_f32_16x16x32_bf16 v[174:177], v[14:17], v[18:21], v[174:177]\n"
    "v_mfma_f32_16x16x32_bf16 v[190:193], v[14:17], v[22:25], v[190:193]\n"
    "s_waitcnt lgkmcnt(0)\n"
    "v_mfma_f32_16x16x32_bf16 v[162:165], v[26:29], v[42:45], v[162:165]\n"
    "ds_read_b64 v[18:19], v224 offset:24576\n"
    "ds_read_b64 v[20:21], v224 offset:26624\n"
    "ds_read_b64 v[22:23], v224 offset:25088\n"
    "v_mfma_f32_16x16x32_bf16 v[178:181], v[26:29], v[46:49], v[178:181]\n"
    "ds_read_b64 v[24:25], v224 offset:27136\n"
    "ds_read_b64_tr_b16 v[2:3], v227 offset:32768\n"
    "v_mfma_f32_16x16x32_bf16 v[166:169], v[30:33], v[42:45], v[166:169]\n"
    "ds_read_b64_tr_b16 v[4:5], v227 offset:34816\n"
    "ds_read_b64_tr_b16 v[6:7], v232 offset:32768\n"
    "ds_read_b64_tr_b16 v[8:9], v232 offset:34816\n"
    "v_mfma_f32_16x16x32_bf16 v[182:185], v[30:33], v[46:49], v[182:185]\n"
    "ds_read_b64_tr_b16 v[10:11], v237 offset:32768\n"
    "ds_read_b64_tr_b16 v[12:13], v237 offset:34816\n"
    "v_mfma_f32_16x16x32_bf16 v[170:173], v[34:37], v[42:45], v[170:173]\n"
    "ds_read_b64_tr_b16 v[14:15], v244 offset:32768\n"
    "ds_read_b64_tr_b16 v[16:17], v244 offset:34816\n"
    "v_mfma_f32_16x16x32_bf16 v[186:189], v[34:37], v[46:49], v[186:189]\n"
    "v_mfma_f32_16x16x32_bf16 v[174:177], v[38:41], v[42:45], v[174:177]\n"
    "v_mfma_f32_16x16x32_bf16 v[190:193], v[38:41], v[46:49], v[190:193]\n"
    "s_waitcnt lgkmcnt(0)\n"
    "s_barrier\n"
    "v_mfma_f32_16x16x32_bf16 v[162:165], v[2:5], v[18:21], v[162:165]\n"
    "ds_read_b64 v[42:43], v224 offset:40960\n"
    "ds_read_b64 v[44:45], v224 offset:43008\n"
    "ds_read_b64 v[46:47], v224 offset:41472\n"
    "v_mfma_f32_16x16x32_bf16 v[178:181], v[2:5], v[22:25], v[178:181]\n"
    "ds_read_b64 v[48:49], v224 offset:43520\n"
    "ds_read_b64_tr_b16 v[26:27], v228 offset:32768\n"
    "v_mfma_f32_16x16x32_bf16 v[166:169], v[6:9], v[18:21], v[166:169]\n"
    "ds_read_b64_tr_b16 v[28:29], v228 offset:34816\n"
    "ds_read_b64_tr_b16 v[30:31], v235 offset:32768\n"
    "ds_read_b64_tr_b16 v[32:33], v235 offset:34816\n"
    "v_mfma_f32_16x16x32_bf16 v[182:185], v[6:9], v[22:25], v[182:185]\n"
    "ds_read_b64_tr_b16 v[34:35], v242 offset:32768\n"
    "ds_read_b64_tr_b16 v[36:37], v242 offset:34816\n"
    "v_mfma_f32_16x16x32_bf16 v[170:173], v[10:13], v[18:21], v[170:173]\n"
    "ds_read_b64_tr_b16 v[38:39], v245 offset:32768\n"
    "ds_read_b64_tr_b16 v[40:41], v245 offset:34816\n"
    "v_mfma_f32_16x16x32_bf16 v[186:189], v[10:13], v[22:25], v[186:189]\n"
    "v_mfma_f32_16x16x32_bf16 v[174:177], v[14:17], v[18:21], v[174:177]\n"
    "v_mfma_f32_16x16x32_bf16 v[190:193], v[14:17], v[22:25], v[190:193]\n"
    "s_waitcnt lgkmcnt(0)\n"
    "s_barrier\n"
    "v_mfma_f32_16x16x32_bf16 v[162:165], v[26:29], v[42:45], v[162:165]\n"
    "ds_read_b64 v[18:19], v224 offset:16384\n"
    "ds_read_b64 v[20:21], v224 offset:18432\n"
    "ds_read_b64 v[22:23], v224 offset:16896\n"
    "v_mfma_f32_16x16x32_bf16 v[178:181], v[26:29], v[46:49], v[178:181]\n"
    "ds_read_b64 v[24:25], v224 offset:18944\n"
    "ds_read_b64_tr_b16 v[2:3], v227 offset:16384\n"
    "v_mfma_f32_16x16x32_bf16 v[166:169], v[30:33], v[42:45], v[166:169]\n"
    "ds_read_b64_tr_b16 v[4:5], v227 offset:18432\n"
    "ds_read_b64_tr_b16 v[6:7], v232 offset:16384\n"
    "ds_read_b64_tr_b16 v[8:9], v232 offset:18432\n"
    "v_mfma_f32_16x16x32_bf16 v[182:185], v[30:33], v[46:49], v[182:185]\n"
    "ds_read_b64_tr_b16 v[10:11], v237 offset:16384\n"
    "ds_read_b64_tr_b16 v[12:13], v237 offset:18432\n"
    "v_mfma_f32_16x16x32_bf16 v[170:173], v[34:37], v[42:45], v[170:173]\n"
    "ds_read_b64_tr_b16 v[14:15], v244 offset:16384\n"
    "ds_read_b64_tr_b16 v[16:17], v244 offset:18432\n"
    "v_mfma_f32_16x16x32_bf16 v[186:189], v[34:37], v[46:49], v[186:189]\n"
    "v_mfma_f32_16x16x32_bf16 v[174:177], v[38:41], v[42:45], v[174:177]\n"
    "v_mfma_f32_16x16x32_bf16 v[190:193], v[38:41], v[46:49], v[190:193]\n"
    "s_waitcnt lgkmcnt(0)\n"
    "v_mfma_f32_16x16x32_bf16 v[162:165], v[2:5], v[18:21], v[162:165]\n"
    "ds_read_b64 v[42:43], v226 offset:0\n"
    "ds_read_b64 v[44:45], v226 offset:2048\n"
    "ds_read_b64 v[46:47], v226 offset:512\n"
    "v_mfma_f32_16x16x32_bf16 v[178:181], v[2:5], v[22:25], v[178:181]\n"
    "ds_read_b64 v[48:49], v226 offset:2560\n"
    "ds_read_b64_tr_b16 v[26:27], v227 offset:40960\n"
    "v_mfma_f32_16x16x32_bf16 v[166:169], v[6:9], v[18:21], v[166:169]\n"
    "ds_read_b64_tr_b16 v[28:29], v227 offset:43008\n"
    "ds_read_b64_tr_b16 v[30:31], v232 offset:40960\n"
    "ds_read_b64_tr_b16 v[32:33], v232 offset:43008\n"
    "v_mfma_f32_16x16x32_bf16 v[182:185], v[6:9], v[22:25], v[182:185]\n"
    "ds_read_b64_tr_b16 v[34:35], v237 offset:40960\n"
    "ds_read_b64_tr_b16 v[36:37], v237 offset:43008\n"
    "v_mfma_f32_16x16x32_bf16 v[170:173], v[10:13], v[18:21], v[170:173]\n"
    "ds_read_b64_tr_b16 v[38:39], v244 offset:40960\n"
    "ds_read_b64_tr_b16 v[40:41], v244 offset:43008\n"
    "v_mfma_f32_16x16x32_bf16 v[186:189], v[10:13], v[22:25], v[186:189]\n"
    "v_mfma_f32_16x16x32_bf16 v[174:177], v[14:17], v[18:21], v[174:177]\n"
    "v_mfma_f32_16x16x32_bf16 v[190:193], v[14:17], v[22:25], v[190:193]\n"
    "s_waitcnt lgkmcnt(0)\n"
    "s_barrier\n"
    "v_mfma_f32_16x16x32_bf16 v[162:165], v[26:29], v[42:45], v[162:165]\n"
    "ds_read_b64 v[18:19], v226 offset:8192\n"
    "ds_read_b64 v[20:21], v226 offset:10240\n"
    "ds_read_b64 v[22:23], v226 offset:8704\n"
    "v_mfma_f32_16x16x32_bf16 v[178:181], v[26:29], v[46:49], v[178:181]\n"
    "ds_read_b64 v[24:25], v226 offset:10752\n"
    "ds_read_b64_tr_b16 v[2:3], v227 offset:49152\n"
    "v_mfma_f32_16x16x32_bf16 v[166:169], v[30:33], v[42:45], v[166:169]\n"
    "ds_read_b64_tr_b16 v[4:5], v227 offset:51200\n"
    "ds_read_b64_tr_b16 v[6:7], v232 offset:49152\n"
    "ds_read_b64_tr_b16 v[8:9], v232 offset:51200\n"
    "v_mfma_f32_16x16x32_bf16 v[182:185], v[30:33], v[46:49], v[182:185]\n"
    "ds_read_b64_tr_b16 v[10:11], v237 offset:49152\n"
    "ds_read_b64_tr_b16 v[12:13], v237 offset:51200\n"
    "v_mfma_f32_16x16x32_bf16 v[170:173], v[34:37], v[42:45], v[170:173]\n"
    "ds_read_b64_tr_b16 v[14:15], v244 offset:49152\n"
    "ds_read_b64_tr_b16 v[16:17], v244 offset:51200\n"
    "v_mfma_f32_16x16x32_bf16 v[186:189], v[34:37], v[46:49], v[186:189]\n"
    "v_mfma_f32_16x16x32_bf16 v[174:177], v[38:41], v[42:45], v[174:177]\n"
    "v_mfma_f32_16x16x32_bf16 v[190:193], v[38:41], v[46:49], v[190:193]\n"
    "s_waitcnt lgkmcnt(0)\n"
    "v_mfma_f32_16x16x32_bf16 v[162:165], v[2:5], v[18:21], v[162:165]\n"
    "ds_read_b64 v[42:43], v226 offset:16384\n"
    "ds_read_b64 v[44:45], v226 offset:18432\n"
    "ds_read_b64 v[46:47], v226 offset:16896\n"
    "v_mfma_f32_16x16x32_bf16 v[178:181], v[2:5], v[22:25], v[178:181]\n"
    "ds_read_b64 v[48:49], v226 offset:18944\n"
    "ds_read_b64_tr_b16 v[26:27], v227 offset:57344\n"
    "v_mfma_f32_16x16x32_bf16 v[166:169], v[6:9], v[18:21], v[166:169]\n"
    "ds_read_b64_tr_b16 v[28:29], v227 offset:59392\n"
    "ds_read_b64_tr_b16 v[30:31], v232 offset:57344\n"
    "ds_read_b64_tr_b16 v[32:33], v232 offset:59392\n"
    "v_mfma_f32_16x16x32_bf16 v[182:185], v[6:9], v[22:25], v[182:185]\n"
    "ds_read_b64_tr_b16 v[34:35], v237 offset:57344\n"
    "ds_read_b64_tr_b16 v[36:37], v237 offset:59392\n"
    "v_mfma_f32_16x16x32_bf16 v[170:173], v[10:13], v[18:21], v[170:173]\n"
    "ds_read_b64_tr_b16 v[38:39], v244 offset:57344\n"
    "ds_read_b64_tr_b16 v[40:41], v244 offset:59392\n"
    "v_mfma_f32_16x16x32_bf16 v[186:189], v[10:13], v[22:25], v[186:189]\n"
    "v_mfma_f32_16x16x32_bf16 v[174:177], v[14:17], v[18:21], v[174:177]\n"
    "v_mfma_f32_16x16x32_bf16 v[190:193], v[14:17], v[22:25], v[190:193]\n"
    "s_waitcnt lgkmcnt(0)\n"
    "s_barrier\n"
    "v_mfma_f32_16x16x32_bf16 v[162:165], v[26:29], v[42:45], v[162:165]\n"
    "ds_read_b64 v[18:19], v226 offset:24576\n"
    "ds_read_b64 v[20:21], v226 offset:26624\n"
    "ds_read_b64 v[22:23], v226 offset:25088\n"
    "v_mfma_f32_16x16x32_bf16 v[178:181], v[26:29], v[46:49], v[178:181]\n"
    "ds_read_b64 v[24:25], v226 offset:27136\n"
    "ds_read_b64_tr_b16 v[2:3], v228 offset:49152\n"
    "v_mfma_f32_16x16x32_bf16 v[166:169], v[30:33], v[42:45], v[166:169]\n"
    "ds_read_b64_tr_b16 v[4:5], v228 offset:51200\n"
    "ds_read_b64_tr_b16 v[6:7], v235 offset:49152\n"
    "ds_read_b64_tr_b16 v[8:9], v235 offset:51200\n"
    "v_mfma_f32_16x16x32_bf16 v[182:185], v[30:33], v[46:49], v[182:185]\n"
    "ds_read_b64_tr_b16 v[10:11], v242 offset:49152\n"
    "ds_read_b64_tr_b16 v[12:13], v242 offset:51200\n"
    "v_mfma_f32_16x16x32_bf16 v[170:173], v[34:37], v[42:45], v[170:173]\n"
    "ds_read_b64_tr_b16 v[14:15], v245 offset:49152\n"
    "ds_read_b64_tr_b16 v[16:17], v245 offset:51200\n"
    "v_mfma_f32_16x16x32_bf16 v[186:189], v[34:37], v[46:49], v[186:189]\n"
    "v_mfma_f32_16x16x32_bf16 v[174:177], v[38:41], v[42:45], v[174:177]\n"
    "v_mfma_f32_16x16x32_bf16 v[190:193], v[38:41], v[46:49], v[190:193]\n"
    "s_waitcnt lgkmcnt(0)\n"
    "v_mfma_f32_16x16x32_bf16 v[162:165], v[2:5], v[18:21], v[162:165]\n"
    "ds_read_b64 v[42:43], v223 offset:0\n"
    "ds_read_b64 v[44:45], v223 offset:2048\n"
    "ds_read_b64 v[46:47], v223 offset:512\n"
    "v_mfma_f32_16x16x32_bf16 v[178:181], v[2:5], v[22:25], v[178:181]\n"
    "ds_read_b64 v[48:49], v223 offset:2560\n"
    "ds_read_b64_tr_b16 v[26:27], v228 offset:57344\n"
    "v_mfma_f32_16x16x32_bf16 v[166:169], v[6:9], v[18:21], v[166:169]\n"
    "ds_read_b64_tr_b16 v[28:29], v228 offset:59392\n"
    "ds_read_b64_tr_b16 v[30:31], v235 offset:57344\n"
    "ds_read_b64_tr_b16 v[32:33], v235 offset:59392\n"
    "v_mfma_f32_16x16x32_bf16 v[182:185], v[6:9], v[22:25], v[182:185]\n"
    "ds_read_b64_tr_b16 v[34:35], v242 offset:57344\n"
    "ds_read_b64_tr_b16 v[36:37], v242 offset:59392\n"
    "v_mfma_f32_16x16x32_bf16 v[170:173], v[10:13], v[18:21], v[170:173]\n"
    "ds_read_b64_tr_b16 v[38:39], v245 offset:57344\n"
    "ds_read_b64_tr_b16 v[40:41], v245 offset:59392\n"
    "v_mfma_f32_16x16x32_bf16 v[186:189], v[10:13], v[22:25], v[186:189]\n"
    "v_mfma_f32_16x16x32_bf16 v[174:177], v[14:17], v[18:21], v[174:177]\n"
    "v_mfma_f32_16x16x32_bf16 v[190:193], v[14:17], v[22:25], v[190:193]\n"
    "s_waitcnt lgkmcnt(0)\n"
    "s_barrier\n"
    "v_mfma_f32_16x16x32_bf16 v[162:165], v[26:29], v[42:45], v[162:165]\n"
    "ds_read_b64 v[18:19], v223 offset:8192\n"
    "ds_read_b64 v[20:21], v223 offset:10240\n"
    "ds_read_b64 v[22:23], v223 offset:8704\n"
    "v_mfma_f32_16x16x32_bf16 v[178:181], v[26:29], v[46:49], v[178:181]\n"
    "ds_read_b64 v[24:25], v223 offset:10752\n"
    "ds_read_b64_tr_b16 v[2:3], v227 offset:24576\n"
    "v_mfma_f32_16x16x32_bf16 v[166:169], v[30:33], v[42:45], v[166:169]\n"
    "ds_read_b64_tr_b16 v[4:5], v227 offset:26624\n"
    "ds_read_b64_tr_b16 v[6:7], v232 offset:24576\n"
    "ds_read_b64_tr_b16 v[8:9], v232 offset:26624\n"
    "v_mfma_f32_16x16x32_bf16 v[182:185], v[30:33], v[46:49], v[182:185]\n"
    "ds_read_b64_tr_b16 v[10:11], v237 offset:24576\n"
    "ds_read_b64_tr_b16 v[12:13], v237 offset:26624\n"
    "v_mfma_f32_16x16x32_bf16 v[170:173], v[34:37], v[42:45], v[170:173]\n"
    "ds_read_b64_tr_b16 v[14:15], v244 offset:24576\n"
    "ds_read_b64_tr_b16 v[16:17], v244 offset:26624\n"
    "v_mfma_f32_16x16x32_bf16 v[186:189], v[34:37], v[46:49], v[186:189]\n"
    "v_mfma_f32_16x16x32_bf16 v[174:177], v[38:41], v[42:45], v[174:177]\n"
    "v_mfma_f32_16x16x32_bf16 v[190:193], v[38:41], v[46:49], v[190:193]\n"
    "s_waitcnt lgkmcnt(0)\n"
    "v_mfma_f32_16x16x32_bf16 v[162:165], v[2:5], v[18:21], v[162:165]\n"
    "ds_read_b64 v[42:43], v223 offset:32768\n"
    "ds_read_b64 v[44:45], v223 offset:34816\n"
    "ds_read_b64 v[46:47], v223 offset:33280\n"
    "v_mfma_f32_16x16x32_bf16 v[178:181], v[2:5], v[22:25], v[178:181]\n"
    "ds_read_b64 v[48:49], v223 offset:35328\n"
    "ds_read_b64_tr_b16 v[26:27], v228 offset:0\n"
    "v_mfma_f32_16x16x32_bf16 v[166:169], v[6:9], v[18:21], v[166:169]\n"
    "ds_read_b64_tr_b16 v[28:29], v228 offset:2048\n"
    "ds_read_b64_tr_b16 v[30:31], v235 offset:0\n"
    "ds_read_b64_tr_b16 v[32:33], v235 offset:2048\n"
    "v_mfma_f32_16x16x32_bf16 v[182:185], v[6:9], v[22:25], v[182:185]\n"
    "ds_read_b64_tr_b16 v[34:35], v242 offset:0\n"
    "ds_read_b64_tr_b16 v[36:37], v242 offset:2048\n"
    "v_mfma_f32_16x16x32_bf16 v[170:173], v[10:13], v[18:21], v[170:173]\n"
    "ds_read_b64_tr_b16 v[38:39], v245 offset:0\n"
    "ds_read_b64_tr_b16 v[40:41], v245 offset:2048\n"
    "v_mfma_f32_16x16x32_bf16 v[186:189], v[10:13], v[22:25], v[186:189]\n"
    "v_mfma_f32_16x16x32_bf16 v[174:177], v[14:17], v[18:21], v[174:177]\n"
    "v_mfma_f32_16x16x32_bf16 v[190:193], v[14:17], v[22:25], v[190:193]\n"
    "s_waitcnt lgkmcnt(0)\n"
    "s_barrier\n"
    "v_mfma_f32_16x16x32_bf16 v[162:165], v[26:29], v[42:45], v[162:165]\n"
    "ds_read_b64 v[18:19], v224 offset:24576\n"
    "ds_read_b64 v[20:21], v224 offset:26624\n"
    "ds_read_b64 v[22:23], v224 offset:25088\n"
    "v_mfma_f32_16x16x32_bf16 v[178:181], v[26:29], v[46:49], v[178:181]\n"
    "ds_read_b64 v[24:25], v224 offset:27136\n"
    "ds_read_b64_tr_b16 v[2:3], v228 offset:8192\n"
    "v_mfma_f32_16x16x32_bf16 v[166:169], v[30:33], v[42:45], v[166:169]\n"
    "ds_read_b64_tr_b16 v[4:5], v228 offset:10240\n"
    "ds_read_b64_tr_b16 v[6:7], v235 offset:8192\n"
    "ds_read_b64_tr_b16 v[8:9], v235 offset:10240\n"
    "v_mfma_f32_16x16x32_bf16 v[182:185], v[30:33], v[46:49], v[182:185]\n"
    "ds_read_b64_tr_b16 v[10:11], v242 offset:8192\n"
    "ds_read_b64_tr_b16 v[12:13], v242 offset:10240\n"
    "v_mfma_f32_16x16x32_bf16 v[170:173], v[34:37], v[42:45], v[170:173]\n"
    "ds_read_b64_tr_b16 v[14:15], v245 offset:8192\n"
    "ds_read_b64_tr_b16 v[16:17], v245 offset:10240\n"
    "v_mfma_f32_16x16x32_bf16 v[186:189], v[34:37], v[46:49], v[186:189]\n"
    "v_mfma_f32_16x16x32_bf16 v[174:177], v[38:41], v[42:45], v[174:177]\n"
    "v_mfma_f32_16x16x32_bf16 v[190:193], v[38:41], v[46:49], v[190:193]\n"
    "s_waitcnt lgkmcnt(0)\n"
    "v_mfma_f32_16x16x32_bf16 v[162:165], v[2:5], v[18:21], v[162:165]\n"
    "ds_read_b64 v[42:43], v224 offset:40960\n"
    "ds_read_b64 v[44:45], v224 offset:43008\n"
    "ds_read_b64 v[46:47], v224 offset:41472\n"
    "v_mfma_f32_16x16x32_bf16 v[178:181], v[2:5], v[22:25], v[178:181]\n"
    "ds_read_b64 v[48:49], v224 offset:43520\n"
    "ds_read_b64_tr_b16 v[26:27], v228 offset:32768\n"
    "v_mfma_f32_16x16x32_bf16 v[166:169], v[6:9], v[18:21], v[166:169]\n"
    "ds_read_b64_tr_b16 v[28:29], v228 offset:34816\n"
    "ds_read_b64_tr_b16 v[30:31], v235 offset:32768\n"
    "ds_read_b64_tr_b16 v[32:33], v235 offset:34816\n"
    "v_mfma_f32_16x16x32_bf16 v[182:185], v[6:9], v[22:25], v[182:185]\n"
    "ds_read_b64_tr_b16 v[34:35], v242 offset:32768\n"
    "ds_read_b64_tr_b16 v[36:37], v242 offset:34816\n"
    "v_mfma_f32_16x16x32_bf16 v[170:173], v[10:13], v[18:21], v[170:173]\n"
    "ds_read_b64_tr_b16 v[38:39], v245 offset:32768\n"
    "ds_read_b64_tr_b16 v[40:41], v245 offset:34816\n"
    "v_mfma_f32_16x16x32_bf16 v[186:189], v[10:13], v[22:25], v[186:189]\n"
    "v_mfma_f32_16x16x32_bf16 v[174:177], v[14:17], v[18:21], v[174:177]\n"
    "v_mfma_f32_16x16x32_bf16 v[190:193], v[14:17], v[22:25], v[190:193]\n"
    "s_waitcnt lgkmcnt(0)\n"
    "s_barrier\n"
    "v_mfma_f32_16x16x32_bf16 v[162:165], v[26:29], v[42:45], v[162:165]\n"
    "ds_read_b64 v[18:19], v223 offset:40960\n"
    "ds_read_b64 v[20:21], v223 offset:43008\n"
    "ds_read_b64 v[22:23], v223 offset:41472\n"
    "v_mfma_f32_16x16x32_bf16 v[178:181], v[26:29], v[46:49], v[178:181]\n"
    "ds_read_b64 v[24:25], v223 offset:43520\n"
    "ds_read_b64_tr_b16 v[2:3], v227 offset:16384\n"
    "v_mfma_f32_16x16x32_bf16 v[166:169], v[30:33], v[42:45], v[166:169]\n"
    "ds_read_b64_tr_b16 v[4:5], v227 offset:18432\n"
    "ds_read_b64_tr_b16 v[6:7], v232 offset:16384\n"
    "ds_read_b64_tr_b16 v[8:9], v232 offset:18432\n"
    "v_mfma_f32_16x16x32_bf16 v[182:185], v[30:33], v[46:49], v[182:185]\n"
    "ds_read_b64_tr_b16 v[10:11], v237 offset:16384\n"
    "ds_read_b64_tr_b16 v[12:13], v237 offset:18432\n"
    "v_mfma_f32_16x16x32_bf16 v[170:173], v[34:37], v[42:45], v[170:173]\n"
    "ds_read_b64_tr_b16 v[14:15], v244 offset:16384\n"
    "ds_read_b64_tr_b16 v[16:17], v244 offset:18432\n"
    "v_mfma_f32_16x16x32_bf16 v[186:189], v[34:37], v[46:49], v[186:189]\n"
    "v_mfma_f32_16x16x32_bf16 v[174:177], v[38:41], v[42:45], v[174:177]\n"
    "v_mfma_f32_16x16x32_bf16 v[190:193], v[38:41], v[46:49], v[190:193]\n"
    "s_waitcnt lgkmcnt(0)\n"
    "v_mfma_f32_16x16x32_bf16 v[162:165], v[2:5], v[18:21], v[162:165]\n"
    "ds_read_b64 v[42:43], v226 offset:0\n"
    "ds_read_b64 v[44:45], v226 offset:2048\n"
    "ds_read_b64 v[46:47], v226 offset:512\n"
    "v_mfma_f32_16x16x32_bf16 v[178:181], v[2:5], v[22:25], v[178:181]\n"
    "ds_read_b64 v[48:49], v226 offset:2560\n"
    "ds_read_b64_tr_b16 v[26:27], v228 offset:16384\n"
    "v_mfma_f32_16x16x32_bf16 v[166:169], v[6:9], v[18:21], v[166:169]\n"
    "ds_read_b64_tr_b16 v[28:29], v228 offset:18432\n"
    "ds_read_b64_tr_b16 v[30:31], v235 offset:16384\n"
    "ds_read_b64_tr_b16 v[32:33], v235 offset:18432\n"
    "v_mfma_f32_16x16x32_bf16 v[182:185], v[6:9], v[22:25], v[182:185]\n"
    "ds_read_b64_tr_b16 v[34:35], v242 offset:16384\n"
    "ds_read_b64_tr_b16 v[36:37], v242 offset:18432\n"
    "v_mfma_f32_16x16x32_bf16 v[170:173], v[10:13], v[18:21], v[170:173]\n"
    "ds_read_b64_tr_b16 v[38:39], v245 offset:16384\n"
    "ds_read_b64_tr_b16 v[40:41], v245 offset:18432\n"
    "v_mfma_f32_16x16x32_bf16 v[186:189], v[10:13], v[22:25], v[186:189]\n"
    "v_mfma_f32_16x16x32_bf16 v[174:177], v[14:17], v[18:21], v[174:177]\n"
    "v_mfma_f32_16x16x32_bf16 v[190:193], v[14:17], v[22:25], v[190:193]\n"
    "s_waitcnt lgkmcnt(0)\n"
    "s_barrier\n"
    "v_mfma_f32_16x16x32_bf16 v[162:165], v[26:29], v[42:45], v[162:165]\n"
    "ds_read_b64 v[18:19], v223 offset:57344\n"
    "ds_read_b64 v[20:21], v223 offset:59392\n"
    "ds_read_b64 v[22:23], v223 offset:57856\n"
    "v_mfma_f32_16x16x32_bf16 v[178:181], v[26:29], v[46:49], v[178:181]\n"
    "ds_read_b64 v[24:25], v223 offset:59904\n"
    "ds_read_b64_tr_b16 v[2:3], v227 offset:49152\n"
    "v_mfma_f32_16x16x32_bf16 v[166:169], v[30:33], v[42:45], v[166:169]\n"
    "ds_read_b64_tr_b16 v[4:5], v227 offset:51200\n"
    "ds_read_b64_tr_b16 v[6:7], v232 offset:49152\n"
    "ds_read_b64_tr_b16 v[8:9], v232 offset:51200\n"
    "v_mfma_f32_16x16x32_bf16 v[182:185], v[30:33], v[46:49], v[182:185]\n"
    "ds_read_b64_tr_b16 v[10:11], v237 offset:49152\n"
    "ds_read_b64_tr_b16 v[12:13], v237 offset:51200\n"
    "v_mfma_f32_16x16x32_bf16 v[170:173], v[34:37], v[42:45], v[170:173]\n"
    "ds_read_b64_tr_b16 v[14:15], v244 offset:49152\n"
    "ds_read_b64_tr_b16 v[16:17], v244 offset:51200\n"
    "v_mfma_f32_16x16x32_bf16 v[186:189], v[34:37], v[46:49], v[186:189]\n"
    "v_mfma_f32_16x16x32_bf16 v[174:177], v[38:41], v[42:45], v[174:177]\n"
    "v_mfma_f32_16x16x32_bf16 v[190:193], v[38:41], v[46:49], v[190:193]\n"
    "s_waitcnt lgkmcnt(0)\n"
    "v_mfma_f32_16x16x32_bf16 v[162:165], v[2:5], v[18:21], v[162:165]\n"
    "ds_read_b64 v[42:43], v226 offset:16384\n"
    "ds_read_b64 v[44:45], v226 offset:18432\n"
    "ds_read_b64 v[46:47], v226 offset:16896\n"
    "v_mfma_f32_16x16x32_bf16 v[178:181], v[2:5], v[22:25], v[178:181]\n"
    "ds_read_b64 v[48:49], v226 offset:18944\n"
    "ds_read_b64_tr_b16 v[26:27], v229 offset:8192\n"
    "v_mfma_f32_16x16x32_bf16 v[166:169], v[6:9], v[18:21], v[166:169]\n"
    "ds_read_b64_tr_b16 v[28:29], v229 offset:10240\n"
    "ds_read_b64_tr_b16 v[30:31], v236 offset:8192\n"
    "ds_read_b64_tr_b16 v[32:33], v236 offset:10240\n"
    "v_mfma_f32_16x16x32_bf16 v[182:185], v[6:9], v[22:25], v[182:185]\n"
    "ds_read_b64_tr_b16 v[34:35], v243 offset:8192\n"
    "ds_read_b64_tr_b16 v[36:37], v243 offset:10240\n"
    "v_mfma_f32_16x16x32_bf16 v[170:173], v[10:13], v[18:21], v[170:173]\n"
    "ds_read_b64_tr_b16 v[38:39], v246 offset:8192\n"
    "ds_read_b64_tr_b16 v[40:41], v246 offset:10240\n"
    "v_mfma_f32_16x16x32_bf16 v[186:189], v[10:13], v[22:25], v[186:189]\n"
    "v_mfma_f32_16x16x32_bf16 v[174:177], v[14:17], v[18:21], v[174:177]\n"
    "v_mfma_f32_16x16x32_bf16 v[190:193], v[14:17], v[22:25], v[190:193]\n"
    "s_waitcnt lgkmcnt(0)\n"
    "s_barrier\n"
    "v_mfma_f32_16x16x32_bf16 v[162:165], v[26:29], v[42:45], v[162:165]\n"
    "v_mfma_f32_16x16x32_bf16 v[178:181], v[26:29], v[46:49], v[178:181]\n"
    "v_mfma_f32_16x16x32_bf16 v[166:169], v[30:33], v[42:45], v[166:169]\n"
    "v_mfma_f32_16x16x32_bf16 v[182:185], v[30:33], v[46:49], v[182:185]\n"
    "v_mfma_f32_16x16x32_bf16 v[170:173], v[34:37], v[42:45], v[170:173]\n"
    "v_mfma_f32_16x16x32_bf16 v[186:189], v[34:37], v[46:49], v[186:189]\n"
    "v_mfma_f32_16x16x32_bf16 v[174:177], v[38:41], v[42:45], v[174:177]\n"
    "v_mfma_f32_16x16x32_bf16 v[190:193], v[38:41], v[46:49], v[190:193]\n"
    "s_nop 0\n"
    "ds_write_b128 v198, v[162:165] offset:0\n"
    "ds_write_b128 v198, v[178:181] offset:4096\n"
    "ds_write_b128 v199, v[166:169] offset:0\n"
    "ds_write_b128 v199, v[182:185] offset:4096\n"
    "ds_write_b128 v200, v[170:173] offset:0\n"
    "ds_write_b128 v200, v[186:189] offset:4096\n"
    "ds_write_b128 v201, v[174:177] offset:0\n"
    "ds_write_b128 v201, v[190:193] offset:4096\n"
    "s_waitcnt lgkmcnt(0)\n"
    "s_barrier\n"
    "ds_read_b128 v[50:53], v248 offset:0\n"
    "ds_read_b128 v[54:57], v249 offset:0\n"
    "ds_read_b128 v[58:61], v248 offset:16384\n"
    "ds_read_b128 v[62:65], v249 offset:16384\n"
    "v_lshlrev_b32 v70, 16, v194\n"
    "v_and_b32 v71, 0xffff0000, v194\n"
    "v_lshlrev_b32 v72, 16, v195\n"
    "v_and_b32 v73, 0xffff0000, v195\n"
    "v_lshlrev_b32 v74, 16, v196\n"
    "v_and_b32 v75, 0xffff0000, v196\n"
    "v_lshlrev_b32 v76, 16, v197\n"
    "v_and_b32 v77, 0xffff0000, v197\n"
    "s_waitcnt lgkmcnt(0)\n"
    "v_pk_add_f32 v[50:51], v[50:51], v[58:59]\n"
    "v_pk_add_f32 v[52:53], v[52:53], v[60:61]\n"
    "v_pk_add_f32 v[54:55], v[54:55], v[62:63]\n"
    "v_pk_add_f32 v[56:57], v[56:57], v[64:65]\n"
    "v_pk_add_f32 v[50:51], v[50:51], v[70:71]\n"
    "v_pk_add_f32 v[52:53], v[52:53], v[72:73]\n"
    "v_pk_add_f32 v[54:55], v[54:55], v[74:75]\n"
    "v_pk_add_f32 v[56:57], v[56:57], v[76:77]\n"
    "v_cvt_pk_bf16_f32 v66, v50, v51\n"
    "v_cvt_pk_bf16_f32 v67, v52, v53\n"
    "v_cvt_pk_bf16_f32 v68, v54, v55\n"
    "v_cvt_pk_bf16_f32 v69, v56, v57\n"
    "buffer_store_dwordx4 v[66:69], v247, s[32:35], 0 offen\n"
    "s_endpgm\n"
    "L_H_%=:\n"
    "s_setprio 1\n"
    "v_and_b32 v1, 63, v0\n"
    "v_and_b32 v2, 15, v0\n"
    "v_bfe_u32 v3, v0, 4, 2\n"
    "v_bfe_u32 v4, v0, 2, 2\n"
    "v_and_b32 v5, 3, v0\n"
    "s_lshl_b32 s50, s46, 3\n"
    "s_add_u32 s50, s50, s44\n"
    "s_lshl_b32 s50, s50, 13\n"
    "v_lshlrev_b32 v9, 4, v2\n"
    "v_lshl_or_b32 v9, v3, 10, v9\n"
    "v_add_u32 v9, s50, v9\n"
    "v_bfe_u32 v79, v0, 3, 3\n"
    "v_and_b32 v80, 7, v0\n"
    "v_bfe_u32 v6, v0, 4, 1\n"
    "v_bfe_u32 v7, v0, 5, 1\n"
    "v_lshl_or_b32 v6, v7, 2, v6\n"
    "v_xor_b32 v81, v6, v80\n"
    "v_lshlrev_b32 v81, 4, v81\n"
    "s_and_b32 s50, s45, 1\n"
    "s_lshl_b32 s50, s50, 6\n"
    "v_xor_b32 v8, s50, v81\n"
    "s_lshl_b32 s50, s46, 6\n"
    "s_lshl_b32 s51, s45, 4\n"
    "s_add_u32 s50, s50, s51\n"
    "v_add_u32 v6, s50, v79\n"
    "v_lshl_or_b32 v82, v6, 12, v8\n"
    "s_and_b32 s51, s45, 1\n"
    "s_lshl_b32 s51, s51, 2\n"
    "v_bfe_u32 v7, v0, 1, 3\n"
    "v_xor_b32 v7, v7, v3\n"
    "v_xor_b32 v7, s51, v7\n"
    "v_and_b32 v85, 1, v0\n"
    "v_lshlrev_b32 v85, 4, v85\n"
    "v_lshl_or_b32 v7, v7, 5, v85\n"
    "s_lshl_b32 s51, s45, 3\n"
    "v_add_u32 v85, s51, v3\n"
    "s_movk_i32 s51, 3584\n"
    "v_mad_u32_u24 v84, v85, s51, v7\n"
    "v_add_u32 v85, 0x3400, v84\n"
    "s_mul_i32 s56, s47, 224\n"
    "s_and_b32 s51, s47, 1\n"
    "s_lshl_b32 s51, s51, 5\n"
    "s_sub_u32 s56, s56, s51\n"
    "s_xor_b32 s55, s47, 0\n"
    "s_mul_i32 s55, s55, 224\n"
    "s_sub_u32 s55, s55, s51\n"
    "s_add_u32 s55, s55, 1792\n"
    "s_waitcnt lgkmcnt(0)\n"
    "s_mov_b64 s[4:5], s[80:81]\n"
    "s_mov_b64 s[16:17], s[86:87]\n"
    "s_add_u32 s62, s47, 0\n"
    "s_and_b32 s62, s62, 3\n"
    "s_lshl_b32 s62, s62, 8\n"
    "buffer_load_dwordx4 v[10:13], v9, s[4:7], s62 offen\n"
    "s_add_u32 s62, s47, 1\n"
    "s_and_b32 s62, s62, 3\n"
    "s_lshl_b32 s62, s62, 8\n"
    "buffer_load_dwordx4 v[14:17], v9, s[4:7], s62 offen\n"
    "s_add_u32 s62, s47, 2\n"
    "s_and_b32 s62, s62, 3\n"
    "s_lshl_b32 s62, s62, 8\n"
    "buffer_load_dwordx4 v[18:21], v9, s[4:7], s62 offen\n"
    "s_add_u32 s62, s47, 3\n"
    "s_and_b32 s62, s62, 3\n"
    "s_lshl_b32 s62, s62, 8\n"
    "buffer_load_dwordx4 v[22:25], v9, s[4:7], s62 offen\n"
    "s_lshl_b32 s99, s47, 1\n"
    "s_add_u32 s99, s99, s46\n"
    "s_and_b32 s99, s99, 7\n"
    "s_mul_i32 s97, s99, 7\n"
    "s_lshr_b32 s97, s97, 2\n"
    "s_lshr_b32 s61, s46, 3\n"
    "s_mul_i32 s61, s61, 0\n"
    "s_add_u32 s61, s61, s47\n"
    "s_and_b32 s61, s61, 3\n"
    "s_mul_i32 s61, s61, 8\n"
    "s_and_b32 s50, s46, 7\n"
    "s_mul_i32 s50, s50, 0\n"
    "s_add_u32 s61, s61, s50\n"
    "v_xor_b32 v83, 32, v82\n"
    "v_add_u32 v83, 0x7c00, v83\n"
    "v_xor_b32 v86, 0x40000, v82\n"
    "v_xor_b32 v87, 32, v86\n"
    "v_add_u32 v87, 0x7c00, v87\n"
    "v_and_b32 v89, 1, v0\n"
    "v_lshlrev_b32 v89, 4, v89\n"
    "s_and_b32 s51, s45, 1\n"
    "s_lshl_b32 s51, s51, 1\n"
    "v_bfe_u32 v7, v0, 4, 1\n"
    "v_or_b32 v7, s51, v7\n"
    "v_bfe_u32 v8, v0, 1, 2\n"
    "v_xor_b32 v7, v7, v8\n"
    "v_lshl_or_b32 v7, v7, 5, v89\n"
    "s_lshl_b32 s51, s45, 3\n"
    "v_add_u32 v88, s51, v79\n"
    "s_movk_i32 s51, 3072\n"
    "v_mad_u32_u24 v88, v88, s51, v7\n"
    "v_bfe_u32 v7, v0, 4, 2\n"
    "v_xor_b32 v7, v7, v8\n"
    "v_lshl_or_b32 v7, v7, 5, v89\n"
    "s_lshl_b32 s51, s45, 4\n"
    "v_add_u32 v90, s51, v79\n"
    "v_lshl_or_b32 v90, v90, 10, v7\n"
    "v_add_u32 v91, 0x1c00, v90\n"
    "s_mul_i32 s53, s45, 3072\n"
    "s_mul_i32 s54, s45, 3584\n"
    "v_lshlrev_b32 v93, 4, v1\n"
    "v_add_u32 v92, s54, v93\n"
    "v_add_u32 v93, s52, v93\n"
    "s_lshl_b32 s58, s46, 16\n"
    "s_add_u32 s58, s58, 0x900000\n"
    "s_mul_i32 s59, s46, 114688\n"
    "s_add_u32 s59, s59, 0x500000\n"
    "s_mul_i32 s60, s47, 458752\n"
    "s_add_u32 s60, s60, 0x100000\n"
    "s_mov_b32 s66, -1\n"
    "s_mov_b32 s67, 0xffffff\n"
    "v_lshlrev_b32 v98, 2, v2\n"
    "s_lshl_b32 s50, s45, 6\n"
    "s_add_u32 s50, s50, 159744\n"
    "v_add_u32 v98, s50, v98\n"
    "s_lshl_b32 s50, s45, 8\n"
    "v_lshlrev_b32 v7, 4, v2\n"
    "v_lshl_or_b32 v7, v3, 10, v7\n"
    "v_add_u32 v99, s50, v7\n"
    "v_lshlrev_b32 v116, 4, v1\n"
    "v_and_b32 v117, 31, v0\n"
    "v_lshlrev_b32 v117, 3, v117\n"
    "v_bfe_u32 v122, v0, 3, 6\n"
    "v_and_b32 v7, 7, v0\n"
    "v_lshlrev_b32 v7, 4, v7\n"
    "v_lshl_or_b32 v122, v122, 10, v7\n"
    "s_lshl_b32 s62, s46, 16\n"
    "s_add_u32 s62, s62, s57\n"
    "v_add_u32 v122, s62, v122\n"
    "v_bfe_u32 v123, v0, 3, 6\n"
    "v_and_b32 v152, 7, v0\n"
    "v_lshlrev_b32 v152, 1, v152\n"
    "v_and_b32 v153, 15, v123\n"
    "v_xor_b32 v152, v152, v153\n"
    "v_lshlrev_b32 v152, 4, v152\n"
    "v_lshl_or_b32 v123, v123, 8, v152\n"
    "v_xor_b32 v152, 16, v123\n"
    "s_lshl_b32 s50, s46, 6\n"
    "s_lshl_b32 s51, s45, 2\n"
    "s_add_u32 s50, s50, s51\n"
    "s_sub_u32 s50, s50, 16\n"
    "v_add_u32 v58, s50, v3\n"
    "v_max_i32 v58, 0, v58\n"
    "v_lshlrev_b32 v78, 4, v2\n"
    "v_lshl_or_b32 v78, v58, 10, v78\n"
    "s_lshr_b32 s50, s45, 1\n"
    "s_lshl_b32 s50, s50, 3\n"
    "s_add_u32 s50, s50, 64\n"
    "s_bfe_u32 s51, s50, 0x20003\n"
    "s_lshl_b32 s51, s51, 1\n"
    "v_bfe_u32 v6, v0, 5, 1\n"
    "v_or_b32 v6, s51, v6\n"
    "v_and_b32 v7, 7, v0\n"
    "v_xor_b32 v6, v6, v7\n"
    "v_lshlrev_b32 v6, 4, v6\n"
    "v_lshl_or_b32 v6, v3, 7, v6\n"
    "s_lshl_b32 s51, s50, 7\n"
    "v_add_u32 v6, s51, v6\n"
    "v_bfe_u32 v7, v0, 3, 1\n"
    "s_add_u32 s51, s47, 0\n"
    "s_and_b32 s51, s51, 3\n"
    "s_lshl_b32 s51, s51, 1\n"
    "s_sub_u32 s51, s51, s43\n"
    "v_add_u32 v8, s51, v7\n"
    "v_and_b32 v8, 7, v8\n"
    "s_movk_i32 s51, 10240\n"
    "v_mad_u32_u24 v74, v8, s51, v6\n"
    "s_add_u32 s51, s47, 1\n"
    "s_and_b32 s51, s51, 3\n"
    "s_lshl_b32 s51, s51, 1\n"
    "s_sub_u32 s51, s51, s43\n"
    "v_add_u32 v8, s51, v7\n"
    "v_and_b32 v8, 7, v8\n"
    "s_movk_i32 s51, 10240\n"
    "v_mad_u32_u24 v75, v8, s51, v6\n"
    "s_add_u32 s51, s47, 2\n"
    "s_and_b32 s51, s51, 3\n"
    "s_lshl_b32 s51, s51, 1\n"
    "s_sub_u32 s51, s51, s43\n"
    "v_add_u32 v8, s51, v7\n"
    "v_and_b32 v8, 7, v8\n"
    "s_movk_i32 s51, 10240\n"
    "v_mad_u32_u24 v76, v8, s51, v6\n"
    "s_add_u32 s51, s47, 3\n"
    "s_and_b32 s51, s51, 3\n"
    "s_lshl_b32 s51, s51, 1\n"
    "s_sub_u32 s51, s51, s43\n"
    "v_add_u32 v8, s51, v7\n"
    "v_and_b32 v8, 7, v8\n"
    "s_movk_i32 s51, 10240\n"
    "v_mad_u32_u24 v77, v8, s51, v6\n"
    "s_and_b32 s50, s45, 1\n"
    "s_lshl_b32 s51, s50, 6\n"
    "s_lshl_b32 s50, s50, 9\n"
    "v_xor_b32 v74, s51, v74\n"
    "v_add_u32 v74, s50, v74\n"
    "v_xor_b32 v75, s51, v75\n"
    "v_add_u32 v75, s50, v75\n"
    "v_xor_b32 v76, s51, v76\n"
    "v_add_u32 v76, s50, v76\n"
    "v_xor_b32 v77, s51, v77\n"
    "v_add_u32 v77, s50, v77\n"
    "s_mov_b64 s[8:9], s[82:83]\n"
    "s_mov_b64 s[12:13], s[84:85]\n"
    "s_mov_b64 s[20:21], s[88:89]\n"
    "s_mov_b64 s[24:25], s[90:91]\n"
    "s_mov_b64 s[28:29], s[92:93]\n"
    "s_mov_b64 s[32:33], s[94:95]\n"
    "v_mov_b32 v153, 0x3f803f80\n"
    "s_lshl_b32 s50, s44, 3\n"
    "s_bfe_u32 s51, s50, 0x20003\n"
    "s_lshl_b32 s51, s51, 1\n"
    "v_bfe_u32 v6, v0, 5, 1\n"
    "v_or_b32 v6, s51, v6\n"
    "v_and_b32 v7, 7, v0\n"
    "v_xor_b32 v6, v6, v7\n"
    "v_lshlrev_b32 v6, 4, v6\n"
    "v_lshl_or_b32 v6, v3, 7, v6\n"
    "s_lshl_b32 s51, s50, 7\n"
    "v_add_u32 v6, s51, v6\n"
    "v_bfe_u32 v7, v0, 3, 1\n"
    "s_add_u32 s51, s47, 0\n"
    "s_and_b32 s51, s51, 3\n"
    "s_lshl_b32 s51, s51, 1\n"
    "s_sub_u32 s51, s51, s43\n"
    "v_add_u32 v8, s51, v7\n"
    "v_and_b32 v8, 7, v8\n"
    "s_movk_i32 s51, 10240\n"
    "v_mad_u32_u24 v70, v8, s51, v6\n"
    "s_add_u32 s51, s47, 1\n"
    "s_and_b32 s51, s51, 3\n"
    "s_lshl_b32 s51, s51, 1\n"
    "s_sub_u32 s51, s51, s43\n"
    "v_add_u32 v8, s51, v7\n"
    "v_and_b32 v8, 7, v8\n"
    "s_movk_i32 s51, 10240\n"
    "v_mad_u32_u24 v71, v8, s51, v6\n"
    "s_add_u32 s51, s47, 2\n"
    "s_and_b32 s51, s51, 3\n"
    "s_lshl_b32 s51, s51, 1\n"
    "s_sub_u32 s51, s51, s43\n"
    "v_add_u32 v8, s51, v7\n"
    "v_and_b32 v8, 7, v8\n"
    "s_movk_i32 s51, 10240\n"
    "v_mad_u32_u24 v72, v8, s51, v6\n"
    "s_add_u32 s51, s47, 3\n"
    "s_and_b32 s51, s51, 3\n"
    "s_lshl_b32 s51, s51, 1\n"
    "s_sub_u32 s51, s51, s43\n"
    "v_add_u32 v8, s51, v7\n"
    "v_and_b32 v8, 7, v8\n"
    "s_movk_i32 s51, 10240\n"
    "v_mad_u32_u24 v73, v8, s51, v6\n"
    "s_add_u32 s62, s47, 0\n"
    "s_and_b32 s62, s62, 3\n"
    "s_lshl_b32 s62, s62, 8\n"
    "s_add_u32 s62, s62, 4096\n"
    "buffer_load_dwordx4 v[26:29], v9, s[4:7], s62 offen\n"
    "s_add_u32 s62, s47, 1\n"
    "s_and_b32 s62, s62, 3\n"
    "s_lshl_b32 s62, s62, 8\n"
    "s_add_u32 s62, s62, 4096\n"
    "buffer_load_dwordx4 v[30:33], v9, s[4:7], s62 offen\n"
    "s_add_u32 s62, s47, 2\n"
    "s_and_b32 s62, s62, 3\n"
    "s_lshl_b32 s62, s62, 8\n"
    "s_add_u32 s62, s62, 4096\n"
    "buffer_load_dwordx4 v[34:37], v9, s[4:7], s62 offen\n"
    "s_add_u32 s62, s47, 3\n"
    "s_and_b32 s62, s62, 3\n"
    "s_lshl_b32 s62, s62, 8\n"
    "s_add_u32 s62, s62, 4096\n"
    "buffer_load_dwordx4 v[38:41], v9, s[4:7], s62 offen\n"
    "s_waitcnt vmcnt(4)\n"
    "v_dot2_f32_bf16 v58, v10, v153, 0\n"
    "v_dot2_f32_bf16 v60, v10, v10, 0\n"
    "v_dot2_f32_bf16 v59, v11, v153, 0\n"
    "v_dot2_f32_bf16 v61, v11, v11, 0\n"
    "v_dot2_f32_bf16 v58, v12, v153, v58\n"
    "v_dot2_f32_bf16 v60, v12, v12, v60\n"
    "v_dot2_f32_bf16 v59, v13, v153, v59\n"
    "v_dot2_f32_bf16 v61, v13, v13, v61\n"
    "v_dot2_f32_bf16 v58, v14, v153, v58\n"
    "v_dot2_f32_bf16 v60, v14, v14, v60\n"
    "v_dot2_f32_bf16 v59, v15, v153, v59\n"
    "v_dot2_f32_bf16 v61, v15, v15, v61\n"
    "v_dot2_f32_bf16 v58, v16, v153, v58\n"
    "v_dot2_f32_bf16 v60, v16, v16, v60\n"
    "v_dot2_f32_bf16 v59, v17, v153, v59\n"
    "v_dot2_f32_bf16 v61, v17, v17, v61\n"
    "v_dot2_f32_bf16 v58, v18, v153, v58\n"
    "v_dot2_f32_bf16 v60, v18, v18, v60\n"
    "v_dot2_f32_bf16 v59, v19, v153, v59\n"
    "v_dot2_f32_bf16 v61, v19, v19, v61\n"
    "v_dot2_f32_bf16 v58, v20, v153, v58\n"
    "v_dot2_f32_bf16 v60, v20, v20, v60\n"
    "v_dot2_f32_bf16 v59, v21, v153, v59\n"
    "v_dot2_f32_bf16 v61, v21, v21, v61\n"
    "v_dot2_f32_bf16 v58, v22, v153, v58\n"
    "v_dot2_f32_bf16 v60, v22, v22, v60\n"
    "v_dot2_f32_bf16 v59, v23, v153, v59\n"
    "v_dot2_f32_bf16 v61, v23, v23, v61\n"
    "v_dot2_f32_bf16 v58, v24, v153, v58\n"
    "v_dot2_f32_bf16 v60, v24, v24, v60\n"
    "v_dot2_f32_bf16 v59, v25, v153, v59\n"
    "v_dot2_f32_bf16 v61, v25, v25, v61\n"
    "s_nop 1\n"
    "v_add_f32 v58, v58, v59\n"
    "v_add_f32 v60, v60, v61\n"
    "s_nop 0\n"
    "v_add_f32_dpp v58, v58, v58 row_ror:8 row_mask:0xf bank_mask:0xf\n"
    "s_add_u32 m0, s52, 0x16000\n"
    "s_lshl_b32 s62, s43, 1\n"
    "s_add_u32 s62, s62, 0\n"
    "s_and_b32 s62, s62, 15\n"
    "s_mul_i32 s62, s62, 114688\n"
    "s_add_u32 s62, s62, s55\n"
    "buffer_load_dwordx4 v84, s[16:19], s62 offen offset:0 lds\n"
    "buffer_load_dwordx4 v85, s[16:19], s62 offen offset:1024 lds\n"
    "s_add_u32 m0, s52, 0x18000\n"
    "s_lshl_b32 s62, s43, 1\n"
    "s_add_u32 s62, s62, 1\n"
    "s_and_b32 s62, s62, 15\n"
    "s_mul_i32 s62, s62, 114688\n"
    "s_add_u32 s62, s62, s55\n"
    "buffer_load_dwordx4 v84, s[16:19], s62 offen offset:0 lds\n"
    "buffer_load_dwordx4 v85, s[16:19], s62 offen offset:1024 lds\n"
    "v_add_f32_dpp v60, v60, v60 row_ror:8 row_mask:0xf bank_mask:0xf\n"
    "v_add_f32_dpp v58, v58, v58 row_ror:4 row_mask:0xf bank_mask:0xf\n"
    "s_nop 0\n"
    "v_add_f32_dpp v60, v60, v60 row_ror:4 row_mask:0xf bank_mask:0xf\n"
    "v_add_f32_dpp v58, v58, v58 row_ror:2 row_mask:0xf bank_mask:0xf\n"
    "s_nop 0\n"
    "v_add_f32_dpp v60, v60, v60 row_ror:2 row_mask:0xf bank_mask:0xf\n"
    "v_add_f32_dpp v58, v58, v58 row_ror:1 row_mask:0xf bank_mask:0xf\n"
    "s_nop 0\n"
    "v_add_f32_dpp v60, v60, v60 row_ror:1 row_mask:0xf bank_mask:0xf\n"
    "v_mul_f32 v58, 0xbb000000, v58\n"
    "v_mul_f32 v60, 0x3b000000, v60\n"
    "v_fma_f32 v60, -v58, v58, v60\n"
    "v_max_f32 v60, 0, v60\n"
    "v_add_f32 v60, 0x3727c5ac, v60\n"
    "v_rsq_f32 v60, v60\n"
    "s_nop 0\n"
    "v_mul_f32 v58, v58, v60\n"
    "v_lshlrev_b32 v62, 16, v10\n"
    "v_and_b32 v63, 0xffff0000, v10\n"
    "v_lshlrev_b32 v64, 16, v11\n"
    "v_and_b32 v65, 0xffff0000, v11\n"
    "v_lshlrev_b32 v66, 16, v12\n"
    "v_and_b32 v67, 0xffff0000, v12\n"
    "v_lshlrev_b32 v68, 16, v13\n"
    "v_and_b32 v69, 0xffff0000, v13\n"
    "v_pk_fma_f32 v[62:63], v[62:63], v[60:61], v[58:59] op_sel_hi:[1,0,0]\n"
    "v_pk_fma_f32 v[64:65], v[64:65], v[60:61], v[58:59] op_sel_hi:[1,0,0]\n"
    "v_pk_fma_f32 v[66:67], v[66:67], v[60:61], v[58:59] op_sel_hi:[1,0,0]\n"
    "v_pk_fma_f32 v[68:69], v[68:69], v[60:61], v[58:59] op_sel_hi:[1,0,0]\n"
    "v_cvt_pk_bf16_f32 v10, v62, v63\n"
    "v_cvt_pk_bf16_f32 v11, v64, v65\n"
    "v_cvt_pk_bf16_f32 v12, v66, v67\n"
    "v_cvt_pk_bf16_f32 v13, v68, v69\n"
    "ds_write_b128 v70, v[10:13] offset:0\n"
    "v_lshlrev_b32 v62, 16, v14\n"
    "v_and_b32 v63, 0xffff0000, v14\n"
    "v_lshlrev_b32 v64, 16, v15\n"
    "v_and_b32 v65, 0xffff0000, v15\n"
    "v_lshlrev_b32 v66, 16, v16\n"
    "v_and_b32 v67, 0xffff0000, v16\n"
    "v_lshlrev_b32 v68, 16, v17\n"
    "v_and_b32 v69, 0xffff0000, v17\n"
    "v_pk_fma_f32 v[62:63], v[62:63], v[60:61], v[58:59] op_sel_hi:[1,0,0]\n"
    "v_pk_fma_f32 v[64:65], v[64:65], v[60:61], v[58:59] op_sel_hi:[1,0,0]\n"
    "v_pk_fma_f32 v[66:67], v[66:67], v[60:61], v[58:59] op_sel_hi:[1,0,0]\n"
    "v_pk_fma_f32 v[68:69], v[68:69], v[60:61], v[58:59] op_sel_hi:[1,0,0]\n"
    "v_cvt_pk_bf16_f32 v14, v62, v63\n"
    "v_cvt_pk_bf16_f32 v15, v64, v65\n"
    "v_cvt_pk_bf16_f32 v16, v66, v67\n"
    "v_cvt_pk_bf16_f32 v17, v68, v69\n"
    "ds_write_b128 v71, v[14:17] offset:0\n"
    "v_lshlrev_b32 v62, 16, v18\n"
    "v_and_b32 v63, 0xffff0000, v18\n"
    "v_lshlrev_b32 v64, 16, v19\n"
    "v_and_b32 v65, 0xffff0000, v19\n"
    "v_lshlrev_b32 v66, 16, v20\n"
    "v_and_b32 v67, 0xffff0000, v20\n"
    "v_lshlrev_b32 v68, 16, v21\n"
    "v_and_b32 v69, 0xffff0000, v21\n"
    "v_pk_fma_f32 v[62:63], v[62:63], v[60:61], v[58:59] op_sel_hi:[1,0,0]\n"
    "v_pk_fma_f32 v[64:65], v[64:65], v[60:61], v[58:59] op_sel_hi:[1,0,0]\n"
    "v_pk_fma_f32 v[66:67], v[66:67], v[60:61], v[58:59] op_sel_hi:[1,0,0]\n"
    "v_pk_fma_f32 v[68:69], v[68:69], v[60:61], v[58:59] op_sel_hi:[1,0,0]\n"
    "v_cvt_pk_bf16_f32 v18, v62, v63\n"
    "v_cvt_pk_bf16_f32 v19, v64, v65\n"
    "v_cvt_pk_bf16_f32 v20, v66, v67\n"
    "v_cvt_pk_bf16_f32 v21, v68, v69\n"
    "ds_write_b128 v72, v[18:21] offset:0\n"
    "v_lshlrev_b32 v62, 16, v22\n"
    "v_and_b32 v63, 0xffff0000, v22\n"
    "v_lshlrev_b32 v64, 16, v23\n"
    "v_and_b32 v65, 0xffff0000, v23\n"
    "v_lshlrev_b32 v66, 16, v24\n"
    "v_and_b32 v67, 0xffff0000, v24\n"
    "v_lshlrev_b32 v68, 16, v25\n"
    "v_and_b32 v69, 0xffff0000, v25\n"
    "v_pk_fma_f32 v[62:63], v[62:63], v[60:61], v[58:59] op_sel_hi:[1,0,0]\n"
    "v_pk_fma_f32 v[64:65], v[64:65], v[60:61], v[58:59] op_sel_hi:[1,0,0]\n"
    "v_pk_fma_f32 v[66:67], v[66:67], v[60:61], v[58:59] op_sel_hi:[1,0,0]\n"
    "v_pk_fma_f32 v[68:69], v[68:69], v[60:61], v[58:59] op_sel_hi:[1,0,0]\n"
    "v_cvt_pk_bf16_f32 v22, v62, v63\n"
    "v_cvt_pk_bf16_f32 v23, v64, v65\n"
    "v_cvt_pk_bf16_f32 v24, v66, v67\n"
    "v_cvt_pk_bf16_f32 v25, v68, v69\n"
    "ds_write_b128 v73, v[22:25] offset:0\n"
    "s_waitcnt vmcnt(4)\n"
    "s_add_u32 m0, s52, 0x1a000\n"
    "s_lshl_b32 s62, s43, 1\n"
    "s_add_u32 s62, s62, 2\n"
    "s_and_b32 s62, s62, 15\n"
    "s_mul_i32 s62, s62, 114688\n"
    "s_add_u32 s62, s62, s55\n"
    "buffer_load_dwordx4 v84, s[16:19], s62 offen offset:0 lds\n"
    "buffer_load_dwordx4 v85, s[16:19], s62 offen offset:1024 lds\n"
    "s_add_u32 m0, s52, 0x1c000\n"
    "s_lshl_b32 s62, s43, 1\n"
    "s_add_u32 s62, s62, 3\n"
    "s_and_b32 s62, s62, 15\n"
    "s_mul_i32 s62, s62, 114688\n"
    "s_add_u32 s62, s62, s55\n"
    "buffer_load_dwordx4 v84, s[16:19], s62 offen offset:0 lds\n"
    "buffer_load_dwordx4 v85, s[16:19], s62 offen offset:1024 lds\n"
    "s_add_u32 m0, s52, 0x1e000\n"
    "s_lshl_b32 s62, s43, 1\n"
    "s_add_u32 s62, s62, 4\n"
    "s_and_b32 s62, s62, 15\n"
    "s_mul_i32 s62, s62, 114688\n"
    "s_add_u32 s62, s62, s55\n"
    "buffer_load_dwordx4 v84, s[16:19], s62 offen offset:0 lds\n"
    "buffer_load_dwordx4 v85, s[16:19], s62 offen offset:1024 lds\n"
    "v_dot2_f32_bf16 v58, v26, v153, 0\n"
    "v_dot2_f32_bf16 v60, v26, v26, 0\n"
    "v_dot2_f32_bf16 v59, v27, v153, 0\n"
    "v_dot2_f32_bf16 v61, v27, v27, 0\n"
    "v_dot2_f32_bf16 v58, v28, v153, v58\n"
    "v_dot2_f32_bf16 v60, v28, v28, v60\n"
    "v_dot2_f32_bf16 v59, v29, v153, v59\n"
    "v_dot2_f32_bf16 v61, v29, v29, v61\n"
    "v_dot2_f32_bf16 v58, v30, v153, v58\n"
    "v_dot2_f32_bf16 v60, v30, v30, v60\n"
    "v_dot2_f32_bf16 v59, v31, v153, v59\n"
    "v_dot2_f32_bf16 v61, v31, v31, v61\n"
    "v_dot2_f32_bf16 v58, v32, v153, v58\n"
    "v_dot2_f32_bf16 v60, v32, v32, v60\n"
    "v_dot2_f32_bf16 v59, v33, v153, v59\n"
    "v_dot2_f32_bf16 v61, v33, v33, v61\n"
    "v_dot2_f32_bf16 v58, v34, v153, v58\n"
    "v_dot2_f32_bf16 v60, v34, v34, v60\n"
    "v_dot2_f32_bf16 v59, v35, v153, v59\n"
    "v_dot2_f32_bf16 v61, v35, v35, v61\n"
    "v_dot2_f32_bf16 v58, v36, v153, v58\n"
    "v_dot2_f32_bf16 v60, v36, v36, v60\n"
    "v_dot2_f32_bf16 v59, v37, v153, v59\n"
    "v_dot2_f32_bf16 v61, v37, v37, v61\n"
    "v_dot2_f32_bf16 v58, v38, v153, v58\n"
    "v_dot2_f32_bf16 v60, v38, v38, v60\n"
    "v_dot2_f32_bf16 v59, v39, v153, v59\n"
    "v_dot2_f32_bf16 v61, v39, v39, v61\n"
    "v_dot2_f32_bf16 v58, v40, v153, v58\n"
    "v_dot2_f32_bf16 v60, v40, v40, v60\n"
    "v_dot2_f32_bf16 v59, v41, v153, v59\n"
    "v_dot2_f32_bf16 v61, v41, v41, v61\n"
    "s_nop 1\n"
    "v_add_f32 v58, v58, v59\n"
    "v_add_f32 v60, v60, v61\n"
    "s_nop 0\n"
    "v_add_f32_dpp v58, v58, v58 row_ror:8 row_mask:0xf bank_mask:0xf\n"
    "v_add_f32_dpp v60, v60, v60 row_ror:8 row_mask:0xf bank_mask:0xf\n"
    "s_nop 0\n"
    "v_add_f32_dpp v58, v58, v58 row_ror:4 row_mask:0xf bank_mask:0xf\n"
    "v_add_f32_dpp v60, v60, v60 row_ror:4 row_mask:0xf bank_mask:0xf\n"
    "s_nop 0\n"
    "v_add_f32_dpp v58, v58, v58 row_ror:2 row_mask:0xf bank_mask:0xf\n"
    "v_add_f32_dpp v60, v60, v60 row_ror:2 row_mask:0xf bank_mask:0xf\n"
    "s_nop 0\n"
    "v_add_f32_dpp v58, v58, v58 row_ror:1 row_mask:0xf bank_mask:0xf\n"
    "v_add_f32_dpp v60, v60, v60 row_ror:1 row_mask:0xf bank_mask:0xf\n"
    "v_mul_f32 v58, 0xbb000000, v58\n"
    "v_mul_f32 v60, 0x3b000000, v60\n"
    "v_fma_f32 v60, -v58, v58, v60\n"
    "v_max_f32 v60, 0, v60\n"
    "v_add_f32 v60, 0x3727c5ac, v60\n"
    "v_rsq_f32 v60, v60\n"
    "s_nop 0\n"
    "v_mul_f32 v58, v58, v60\n"
    "v_lshlrev_b32 v62, 16, v26\n"
    "v_and_b32 v63, 0xffff0000, v26\n"
    "v_lshlrev_b32 v64, 16, v27\n"
    "v_and_b32 v65, 0xffff0000, v27\n"
    "v_lshlrev_b32 v66, 16, v28\n"
    "v_and_b32 v67, 0xffff0000, v28\n"
    "v_lshlrev_b32 v68, 16, v29\n"
    "v_and_b32 v69, 0xffff0000, v29\n"
    "v_pk_fma_f32 v[62:63], v[62:63], v[60:61], v[58:59] op_sel_hi:[1,0,0]\n"
    "v_pk_fma_f32 v[64:65], v[64:65], v[60:61], v[58:59] op_sel_hi:[1,0,0]\n"
    "v_pk_fma_f32 v[66:67], v[66:67], v[60:61], v[58:59] op_sel_hi:[1,0,0]\n"
    "v_pk_fma_f32 v[68:69], v[68:69], v[60:61], v[58:59] op_sel_hi:[1,0,0]\n"
    "v_cvt_pk_bf16_f32 v26, v62, v63\n"
    "v_cvt_pk_bf16_f32 v27, v64, v65\n"
    "v_cvt_pk_bf16_f32 v28, v66, v67\n"
    "v_cvt_pk_bf16_f32 v29, v68, v69\n"
    "v_xor_b32 v70, 64, v70\n"
    "ds_write_b128 v70, v[26:29] offset:512\n"
    "v_lshlrev_b32 v62, 16, v30\n"
    "v_and_b32 v63, 0xffff0000, v30\n"
    "v_lshlrev_b32 v64, 16, v31\n"
    "v_and_b32 v65, 0xffff0000, v31\n"
    "v_lshlrev_b32 v66, 16, v32\n"
    "v_and_b32 v67, 0xffff0000, v32\n"
    "v_lshlrev_b32 v68, 16, v33\n"
    "v_and_b32 v69, 0xffff0000, v33\n"
    "v_pk_fma_f32 v[62:63], v[62:63], v[60:61], v[58:59] op_sel_hi:[1,0,0]\n"
    "v_pk_fma_f32 v[64:65], v[64:65], v[60:61], v[58:59] op_sel_hi:[1,0,0]\n"
    "v_pk_fma_f32 v[66:67], v[66:67], v[60:61], v[58:59] op_sel_hi:[1,0,0]\n"
    "v_pk_fma_f32 v[68:69], v[68:69], v[60:61], v[58:59] op_sel_hi:[1,0,0]\n"
    "v_cvt_pk_bf16_f32 v30, v62, v63\n"
    "v_cvt_pk_bf16_f32 v31, v64, v65\n"
    "v_cvt_pk_bf16_f32 v32, v66, v67\n"
    "v_cvt_pk_bf16_f32 v33, v68, v69\n"
    "v_xor_b32 v71, 64, v71\n"
    "ds_write_b128 v71, v[30:33] offset:512\n"
    "v_lshlrev_b32 v62, 16, v34\n"
    "v_and_b32 v63, 0xffff0000, v34\n"
    "v_lshlrev_b32 v64, 16, v35\n"
    "v_and_b32 v65, 0xffff0000, v35\n"
    "v_lshlrev_b32 v66, 16, v36\n"
    "v_and_b32 v67, 0xffff0000, v36\n"
    "v_lshlrev_b32 v68, 16, v37\n"
    "v_and_b32 v69, 0xffff0000, v37\n"
    "v_pk_fma_f32 v[62:63], v[62:63], v[60:61], v[58:59] op_sel_hi:[1,0,0]\n"
    "v_pk_fma_f32 v[64:65], v[64:65], v[60:61], v[58:59] op_sel_hi:[1,0,0]\n"
    "v_pk_fma_f32 v[66:67], v[66:67], v[60:61], v[58:59] op_sel_hi:[1,0,0]\n"
    "v_pk_fma_f32 v[68:69], v[68:69], v[60:61], v[58:59] op_sel_hi:[1,0,0]\n"
    "v_cvt_pk_bf16_f32 v34, v62, v63\n"
    "v_cvt_pk_bf16_f32 v35, v64, v65\n"
    "v_cvt_pk_bf16_f32 v36, v66, v67\n"
    "v_cvt_pk_bf16_f32 v37, v68, v69\n"
    "v_xor_b32 v72, 64, v72\n"
    "ds_write_b128 v72, v[34:37] offset:512\n"
    "v_lshlrev_b32 v62, 16, v38\n"
    "v_and_b32 v63, 0xffff0000, v38\n"
    "v_lshlrev_b32 v64, 16, v39\n"
    "v_and_b32 v65, 0xffff0000, v39\n"
    "v_lshlrev_b32 v66, 16, v40\n"
    "v_and_b32 v67, 0xffff0000, v40\n"
    "v_lshlrev_b32 v68, 16, v41\n"
    "v_and_b32 v69, 0xffff0000, v41\n"
    "v_pk_fma_f32 v[62:63], v[62:63], v[60:61], v[58:59] op_sel_hi:[1,0,0]\n"
    "v_pk_fma_f32 v[64:65], v[64:65], v[60:61], v[58:59] op_sel_hi:[1,0,0]\n"
    "v_pk_fma_f32 v[66:67], v[66:67], v[60:61], v[58:59] op_sel_hi:[1,0,0]\n"
    "v_pk_fma_f32 v[68:69], v[68:69], v[60:61], v[58:59] op_sel_hi:[1,0,0]\n"
    "v_cvt_pk_bf16_f32 v38, v62, v63\n"
    "v_cvt_pk_bf16_f32 v39, v64, v65\n"
    "v_cvt_pk_bf16_f32 v40, v66, v67\n"
    "v_cvt_pk_bf16_f32 v41, v68, v69\n"
    "v_xor_b32 v73, 64, v73\n"
    "ds_write_b128 v73, v[38:41] offset:512\n"
    "s_add_u32 m0, s52, 0x20000\n"
    "s_lshl_b32 s62, s43, 1\n"
    "s_add_u32 s62, s62, 5\n"
    "s_and_b32 s62, s62, 15\n"
    "s_mul_i32 s62, s62, 114688\n"
    "s_add_u32 s62, s62, s55\n"
    "buffer_load_dwordx4 v84, s[16:19], s62 offen offset:0 lds\n"
    "buffer_load_dwordx4 v85, s[16:19], s62 offen offset:1024 lds\n"
    "s_add_u32 m0, s52, 0x22000\n"
    "s_lshl_b32 s62, s43, 1\n"
    "s_add_u32 s62, s62, 6\n"
    "s_and_b32 s62, s62, 15\n"
    "s_mul_i32 s62, s62, 114688\n"
    "s_add_u32 s62, s62, s55\n"
    "buffer_load_dwordx4 v84, s[16:19], s62 offen offset:0 lds\n"
    "buffer_load_dwordx4 v85, s[16:19], s62 offen offset:1024 lds\n"
    "s_setprio 0\n"
    "s_waitcnt vmcnt(10) lgkmcnt(0)\n"
    "s_barrier\n"
    "s_add_u32 m0, s52, 0x24000\n"
    "s_lshl_b32 s62, s43, 1\n"
    "s_add_u32 s62, s62, 7\n"
    "s_and_b32 s62, s62, 15\n"
    "s_mul_i32 s62, s62, 114688\n"
    "s_add_u32 s62, s62, s55\n"
    "buffer_load_dwordx4 v84, s[16:19], s62 offen offset:0 lds\n"
    "buffer_load_dwordx4 v85, s[16:19], s62 offen offset:1024 lds\n"
    "s_add_u32 m0, s52, 0x14000\n"
    "s_lshl_b32 s62, s43, 1\n"
    "s_add_u32 s62, s62, 8\n"
    "s_and_b32 s62, s62, 15\n"
    "s_mul_i32 s62, s62, 114688\n"
    "s_add_u32 s62, s62, s55\n"
    "buffer_load_dwordx4 v84, s[16:19], s62 offen offset:0 lds\n"
    "buffer_load_dwordx4 v85, s[16:19], s62 offen offset:1024 lds\n"
    "buffer_load_dwordx2 v[154:155], off, s[36:39], 0 sc1\n"
    "s_waitcnt vmcnt(11)\n"
    "s_barrier\n"
    "s_add_u32 m0, s52, 0x16000\n"
    "s_lshl_b32 s62, s43, 1\n"
    "s_add_u32 s62, s62, 9\n"
    "s_and_b32 s62, s62, 15\n"
    "s_mul_i32 s62, s62, 114688\n"
    "s_add_u32 s62, s62, s55\n"
    "buffer_load_dwordx4 v84, s[16:19], s62 offen offset:0 lds\n"
    "buffer_load_dwordx4 v85, s[16:19], s62 offen offset:1024 lds\n"
    "s_add_u32 m0, s52, 0x18000\n"
    "s_lshl_b32 s62, s43, 1\n"
    "s_add_u32 s62, s62, 10\n"
    "s_and_b32 s62, s62, 15\n"
    "s_mul_i32 s62, s62, 114688\n"
    "s_add_u32 s62, s62, s55\n"
    "buffer_load_dwordx4 v84, s[16:19], s62 offen offset:0 lds\n"
    "buffer_load_dwordx4 v85, s[16:19], s62 offen offset:1024 lds\n"
    "s_waitcnt vmcnt(11)\n"
    "s_barrier\n"
    "s_add_u32 m0, s52, 0x1a000\n"
    "s_lshl_b32 s62, s43, 1\n"
    "s_add_u32 s62, s62, 11\n"
    "s_and_b32 s62, s62, 15\n"
    "s_mul_i32 s62, s62, 114688\n"
    "s_add_u32 s62, s62, s55\n"
    "buffer_load_dwordx4 v84, s[16:19], s62 offen offset:0 lds\n"
    "buffer_load_dwordx4 v85, s[16:19], s62 offen offset:1024 lds\n"
    "s_add_u32 m0, s52, 0x1c000\n"
    "s_lshl_b32 s62, s43, 1\n"
    "s_add_u32 s62, s62, 12\n"
    "s_and_b32 s62, s62, 15\n"
    "s_mul_i32 s62, s62, 114688\n"
    "s_add_u32 s62, s62, s55\n"
    "buffer_load_dwordx4 v84, s[16:19], s62 offen offset:0 lds\n"
    "buffer_load_dwordx4 v85, s[16:19], s62 offen offset:1024 lds\n"
    "s_waitcnt vmcnt(11)\n"
    "s_barrier\n"
    "s_add_u32 m0, s52, 0x1e000\n"
    "s_lshl_b32 s62, s43, 1\n"
    "s_add_u32 s62, s62, 13\n"
    "s_and_b32 s62, s62, 15\n"
    "s_mul_i32 s62, s62, 114688\n"
    "s_add_u32 s62, s62, s55\n"
    "buffer_load_dwordx4 v84, s[16:19], s62 offen offset:0 lds\n"
    "buffer_load_dwordx4 v85, s[16:19], s62 offen offset:1024 lds\n"
    "s_add_u32 m0, s52, 0x20000\n"
    "s_lshl_b32 s62, s43, 1\n"
    "s_add_u32 s62, s62, 14\n"
    "s_and_b32 s62, s62, 15\n"
    "s_mul_i32 s62, s62, 114688\n"
    "s_add_u32 s62, s62, s55\n"
    "buffer_load_dwordx4 v84, s[16:19], s62 offen offset:0 lds\n"
    "buffer_load_dwordx4 v85, s[16:19], s62 offen offset:1024 lds\n"
    "s_waitcnt vmcnt(10)\n"
    "s_barrier\n"
    "s_add_u32 m0, s52, 0x22000\n"
    "s_lshl_b32 s62, s43, 1\n"
    "s_add_u32 s62, s62, 15\n"
    "s_and_b32 s62, s62, 15\n"
    "s_mul_i32 s62, s62, 114688\n"
    "s_add_u32 s62, s62, s55\n"
    "buffer_load_dwordx4 v84, s[16:19], s62 offen offset:0 lds\n"
    "buffer_load_dwordx4 v85, s[16:19], s62 offen offset:1024 lds\n"
    "s_add_u32 m0, s52, 0x24000\n"
    "s_lshl_b32 s62, s43, 1\n"
    "s_add_u32 s62, s62, 0\n"
    "s_and_b32 s62, s62, 15\n"
    "s_mul_i32 s62, s62, 114688\n"
    "s_add_u32 s62, s62, s56\n"
    "buffer_load_dwordx4 v84, s[16:19], s62 offen offset:0 lds\n"
    "buffer_load_dwordx4 v85, s[16:19], s62 offen offset:1024 lds\n"
    "s_waitcnt vmcnt(10)\n"
    "s_barrier\n"
    "s_add_u32 m0, s52, 0x14000\n"
    "s_lshl_b32 s62, s43, 1\n"
    "s_add_u32 s62, s62, 1\n"
    "s_and_b32 s62, s62, 15\n"
    "s_mul_i32 s62, s62, 114688\n"
    "s_add_u32 s62, s62, s56\n"
    "buffer_load_dwordx4 v84, s[16:19], s62 offen offset:0 lds\n"
    "buffer_load_dwordx4 v85, s[16:19], s62 offen offset:1024 lds\n"
    "s_add_u32 s62, s47, 0\n"
    "s_and_b32 s62, s62, 3\n"
    "s_lshl_b32 s62, s62, 8\n"
    "buffer_load_dwordx4 v[10:13], v78, s[4:7], s62 offen\n"
    "s_add_u32 s62, s47, 1\n"
    "s_and_b32 s62, s62, 3\n"
    "s_lshl_b32 s62, s62, 8\n"
    "buffer_load_dwordx4 v[14:17], v78, s[4:7], s62 offen\n"
    "s_add_u32 s62, s47, 2\n"
    "s_and_b32 s62, s62, 3\n"
    "s_lshl_b32 s62, s62, 8\n"
    "buffer_load_dwordx4 v[18:21], v78, s[4:7], s62 offen\n"
    "s_add_u32 s62, s47, 3\n"
    "s_and_b32 s62, s62, 3\n"
    "s_lshl_b32 s62, s62, 8\n"
    "buffer_load_dwordx4 v[22:25], v78, s[4:7], s62 offen\n"
    "s_waitcnt vmcnt(12)\n"
    "s_barrier\n"
    "s_waitcnt vmcnt(8)\n"
    "s_barrier\n"
    "s_waitcnt vmcnt(4)\n"
    "s_barrier\n"
    "v_readfirstlane_b32 s48, v154\n"
    "v_readfirstlane_b32 s49, v155\n"
    "s_add_u32 s48, s48, 0x7f4a7c15\n"
    "s_addc_u32 s49, s49, 0x9e3779b9\n"
    "s_barrier\n"
    "s_add_u32 m0, s52, 0x16000\n"
    "s_lshl_b32 s62, s43, 1\n"
    "s_add_u32 s62, s62, 2\n"
    "s_and_b32 s62, s62, 15\n"
    "s_mul_i32 s62, s62, 114688\n"
    "s_add_u32 s62, s62, s56\n"
    "buffer_load_dwordx4 v84, s[16:19], s62 offen offset:0 lds\n"
    "buffer_load_dwordx4 v85, s[16:19], s62 offen offset:1024 lds\n"
    "s_add_u32 m0, s52, 0x18000\n"
    "s_lshl_b32 s62, s43, 1\n"
    "s_add_u32 s62, s62, 3\n"
    "s_and_b32 s62, s62, 15\n"
    "s_mul_i32 s62, s62, 114688\n"
    "s_add_u32 s62, s62, s56\n"
    "buffer_load_dwordx4 v84, s[16:19], s62 offen offset:0 lds\n"
    "buffer_load_dwordx4 v85, s[16:19], s62 offen offset:1024 lds\n"
    "s_add_u32 m0, s52, 0x1a000\n"
    "s_lshl_b32 s62, s43, 1\n"
    "s_add_u32 s62, s62, 4\n"
    "s_and_b32 s62, s62, 15\n"
    "s_mul_i32 s62, s62, 114688\n"
    "s_add_u32 s62, s62, s56\n"
    "buffer_load_dwordx4 v84, s[16:19], s62 offen offset:0 lds\n"
    "buffer_load_dwordx4 v85, s[16:19], s62 offen offset:1024 lds\n"
    "s_add_u32 m0, s52, 0x1c000\n"
    "s_lshl_b32 s62, s43, 1\n"
    "s_add_u32 s62, s62, 5\n"
    "s_and_b32 s62, s62, 15\n"
    "s_mul_i32 s62, s62, 114688\n"
    "s_add_u32 s62, s62, s56\n"
    "buffer_load_dwordx4 v84, s[16:19], s62 offen offset:0 lds\n"
    "buffer_load_dwordx4 v85, s[16:19], s62 offen offset:1024 lds\n"
    "s_add_u32 m0, s52, 0x1e000\n"
    "s_lshl_b32 s62, s43, 1\n"
    "s_add_u32 s62, s62, 6\n"
    "s_and_b32 s62, s62, 15\n"
    "s_mul_i32 s62, s62, 114688\n"
    "s_add_u32 s62, s62, s56\n"
    "buffer_load_dwordx4 v84, s[16:19], s62 offen offset:0 lds\n"
    "buffer_load_dwordx4 v85, s[16:19], s62 offen offset:1024 lds\n"
    "s_add_u32 m0, s52, 0x20000\n"
    "s_lshl_b32 s62, s43, 1\n"
    "s_add_u32 s62, s62, 7\n"
    "s_and_b32 s62, s62, 15\n"
    "s_mul_i32 s62, s62, 114688\n"
    "s_add_u32 s62, s62, s56\n"
    "buffer_load_dwordx4 v84, s[16:19], s62 offen offset:0 lds\n"
    "buffer_load_dwordx4 v85, s[16:19], s62 offen offset:1024 lds\n"
    "s_add_u32 m0, s52, 0x22000\n"
    "s_lshl_b32 s62, s43, 1\n"
    "s_add_u32 s62, s62, 8\n"
    "s_and_b32 s62, s62, 15\n"
    "s_mul_i32 s62, s62, 114688\n"
    "s_add_u32 s62, s62, s56\n"
    "buffer_load_dwordx4 v84, s[16:19], s62 offen offset:0 lds\n"
    "buffer_load_dwordx4 v85, s[16:19], s62 offen offset:1024 lds\n"
    "s_add_u32 m0, s52, 0x24000\n"
    "s_lshl_b32 s62, s43, 1\n"
    "s_add_u32 s62, s62, 9\n"
    "s_and_b32 s62, s62, 15\n"
    "s_mul_i32 s62, s62, 114688\n"
    "s_add_u32 s62, s62, s56\n"
    "buffer_load_dwordx4 v84, s[16:19], s62 offen offset:0 lds\n"
    "buffer_load_dwordx4 v85, s[16:19], s62 offen offset:1024 lds\n"
    "s_add_u32 m0, s52, 0x14000\n"
    "s_lshl_b32 s62, s43, 1\n"
    "s_add_u32 s62, s62, 10\n"
    "s_and_b32 s62, s62, 15\n"
    "s_mul_i32 s62, s62, 114688\n"
    "s_add_u32 s62, s62, s56\n"
    "buffer_load_dwordx4 v84, s[16:19], s62 offen offset:0 lds\n"
    "buffer_load_dwordx4 v85, s[16:19], s62 offen offset:1024 lds\n"
    "s_waitcnt vmcnt(18)\n"
    "v_dot2_f32_bf16 v58, v10, v153, 0\n"
    "v_dot2_f32_bf16 v60, v10, v10, 0\n"
    "v_dot2_f32_bf16 v59, v11, v153, 0\n"
    "v_dot2_f32_bf16 v61, v11, v11, 0\n"
    "v_dot2_f32_bf16 v58, v12, v153, v58\n"
    "v_dot2_f32_bf16 v60, v12, v12, v60\n"
    "v_dot2_f32_bf16 v59, v13, v153, v59\n"
    "v_dot2_f32_bf16 v61, v13, v13, v61\n"
    "v_dot2_f32_bf16 v58, v14, v153, v58\n"
    "v_dot2_f32_bf16 v60, v14, v14, v60\n"
    "v_dot2_f32_bf16 v59, v15, v153, v59\n"
    "v_dot2_f32_bf16 v61, v15, v15, v61\n"
    "v_dot2_f32_bf16 v58, v16, v153, v58\n"
    "v_dot2_f32_bf16 v60, v16, v16, v60\n"
    "v_dot2_f32_bf16 v59, v17, v153, v59\n"
    "v_dot2_f32_bf16 v61, v17, v17, v61\n"
    "v_dot2_f32_bf16 v58, v18, v153, v58\n"
    "v_dot2_f32_bf16 v60, v18, v18, v60\n"
    "v_dot2_f32_bf16 v59, v19, v153, v59\n"
    "v_dot2_f32_bf16 v61, v19, v19, v61\n"
    "v_dot2_f32_bf16 v58, v20, v153, v58\n"
    "v_dot2_f32_bf16 v60, v20, v20, v60\n"
    "v_dot2_f32_bf16 v59, v21, v153, v59\n"
    "v_dot2_f32_bf16 v61, v21, v21, v61\n"
    "v_dot2_f32_bf16 v58, v22, v153, v58\n"
    "v_dot2_f32_bf16 v60, v22, v22, v60\n"
    "v_dot2_f32_bf16 v59, v23, v153, v59\n"
    "v_dot2_f32_bf16 v61, v23, v23, v61\n"
    "v_dot2_f32_bf16 v58, v24, v153, v58\n"
    "v_dot2_f32_bf16 v60, v24, v24, v60\n"
    "v_dot2_f32_bf16 v59, v25, v153, v59\n"
    "v_dot2_f32_bf16 v61, v25, v25, v61\n"
    "s_nop 1\n"
    "v_add_f32 v58, v58, v59\n"
    "v_add_f32 v60, v60, v61\n"
    "s_nop 0\n"
    "v_add_f32_dpp v58, v58, v58 row_ror:8 row_mask:0xf bank_mask:0xf\n"
    "v_add_f32_dpp v60, v60, v60 row_ror:8 row_mask:0xf bank_mask:0xf\n"
    "s_nop 0\n"
    "v_add_f32_dpp v58, v58, v58 row_ror:4 row_mask:0xf bank_mask:0xf\n"
    "v_add_f32_dpp v60, v60, v60 row_ror:4 row_mask:0xf bank_mask:0xf\n"
    "s_nop 0\n"
    "v_add_f32_dpp v58, v58, v58 row_ror:2 row_mask:0xf bank_mask:0xf\n"
    "v_add_f32_dpp v60, v60, v60 row_ror:2 row_mask:0xf bank_mask:0xf\n"
    "s_nop 0\n"
    "v_add_f32_dpp v58, v58, v58 row_ror:1 row_mask:0xf bank_mask:0xf\n"
    "v_add_f32_dpp v60, v60, v60 row_ror:1 row_mask:0xf bank_mask:0xf\n"
    "v_mul_f32 v58, 0xbb000000, v58\n"
    "v_mul_f32 v60, 0x3b000000, v60\n"
    "v_fma_f32 v60, -v58, v58, v60\n"
    "v_max_f32 v60, 0, v60\n"
    "v_add_f32 v60, 0x3727c5ac, v60\n"
    "v_rsq_f32 v60, v60\n"
    "s_nop 0\n"
    "v_mul_f32 v58, v58, v60\n"
    "v_lshlrev_b32 v62, 16, v10\n"
    "v_and_b32 v63, 0xffff0000, v10\n"
    "v_lshlrev_b32 v64, 16, v11\n"
    "v_and_b32 v65, 0xffff0000, v11\n"
    "v_lshlrev_b32 v66, 16, v12\n"
    "v_and_b32 v67, 0xffff0000, v12\n"
    "v_lshlrev_b32 v68, 16, v13\n"
    "v_and_b32 v69, 0xffff0000, v13\n"
    "v_pk_fma_f32 v[62:63], v[62:63], v[60:61], v[58:59] op_sel_hi:[1,0,0]\n"
    "v_pk_fma_f32 v[64:65], v[64:65], v[60:61], v[58:59] op_sel_hi:[1,0,0]\n"
    "v_pk_fma_f32 v[66:67], v[66:67], v[60:61], v[58:59] op_sel_hi:[1,0,0]\n"
    "v_pk_fma_f32 v[68:69], v[68:69], v[60:61], v[58:59] op_sel_hi:[1,0,0]\n"
    "v_cvt_pk_bf16_f32 v10, v62, v63\n"
    "v_cvt_pk_bf16_f32 v11, v64, v65\n"
    "v_cvt_pk_bf16_f32 v12, v66, v67\n"
    "v_cvt_pk_bf16_f32 v13, v68, v69\n"
    "ds_write_b128 v74, v[10:13] offset:0\n"
    "v_lshlrev_b32 v62, 16, v14\n"
    "v_and_b32 v63, 0xffff0000, v14\n"
    "v_lshlrev_b32 v64, 16, v15\n"
    "v_and_b32 v65, 0xffff0000, v15\n"
    "v_lshlrev_b32 v66, 16, v16\n"
    "v_and_b32 v67, 0xffff0000, v16\n"
    "v_lshlrev_b32 v68, 16, v17\n"
    "v_and_b32 v69, 0xffff0000, v17\n"
    "v_pk_fma_f32 v[62:63], v[62:63], v[60:61], v[58:59] op_sel_hi:[1,0,0]\n"
    "v_pk_fma_f32 v[64:65], v[64:65], v[60:61], v[58:59] op_sel_hi:[1,0,0]\n"
    "v_pk_fma_f32 v[66:67], v[66:67], v[60:61], v[58:59] op_sel_hi:[1,0,0]\n"
    "v_pk_fma_f32 v[68:69], v[68:69], v[60:61], v[58:59] op_sel_hi:[1,0,0]\n"
    "v_cvt_pk_bf16_f32 v14, v62, v63\n"
    "v_cvt_pk_bf16_f32 v15, v64, v65\n"
    "v_cvt_pk_bf16_f32 v16, v66, v67\n"
    "v_cvt_pk_bf16_f32 v17, v68, v69\n"
    "ds_write_b128 v75, v[14:17] offset:0\n"
    "v_lshlrev_b32 v62, 16, v18\n"
    "v_and_b32 v63, 0xffff0000, v18\n"
    "v_lshlrev_b32 v64, 16, v19\n"
    "v_and_b32 v65, 0xffff0000, v19\n"
    "v_lshlrev_b32 v66, 16, v20\n"
    "v_and_b32 v67, 0xffff0000, v20\n"
    "v_lshlrev_b32 v68, 16, v21\n"
    "v_and_b32 v69, 0xffff0000, v21\n"
    "v_pk_fma_f32 v[62:63], v[62:63], v[60:61], v[58:59] op_sel_hi:[1,0,0]\n"
    "v_pk_fma_f32 v[64:65], v[64:65], v[60:61], v[58:59] op_sel_hi:[1,0,0]\n"
    "v_pk_fma_f32 v[66:67], v[66:67], v[60:61], v[58:59] op_sel_hi:[1,0,0]\n"
    "v_pk_fma_f32 v[68:69], v[68:69], v[60:61], v[58:59] op_sel_hi:[1,0,0]\n"
    "v_cvt_pk_bf16_f32 v18, v62, v63\n"
    "v_cvt_pk_bf16_f32 v19, v64, v65\n"
    "v_cvt_pk_bf16_f32 v20, v66, v67\n"
    "v_cvt_pk_bf16_f32 v21, v68, v69\n"
    "ds_write_b128 v76, v[18:21] offset:0\n"
    "v_lshlrev_b32 v62, 16, v22\n"
    "v_and_b32 v63, 0xffff0000, v22\n"
    "v_lshlrev_b32 v64, 16, v23\n"
    "v_and_b32 v65, 0xffff0000, v23\n"
    "v_lshlrev_b32 v66, 16, v24\n"
    "v_and_b32 v67, 0xffff0000, v24\n"
    "v_lshlrev_b32 v68, 16, v25\n"
    "v_and_b32 v69, 0xffff0000, v25\n"
    "v_pk_fma_f32 v[62:63], v[62:63], v[60:61], v[58:59] op_sel_hi:[1,0,0]\n"
    "v_pk_fma_f32 v[64:65], v[64:65], v[60:61], v[58:59] op_sel_hi:[1,0,0]\n"
    "v_pk_fma_f32 v[66:67], v[66:67], v[60:61], v[58:59] op_sel_hi:[1,0,0]\n"
    "v_pk_fma_f32 v[68:69], v[68:69], v[60:61], v[58:59] op_sel_hi:[1,0,0]\n"
    "v_cvt_pk_bf16_f32 v22, v62, v63\n"
    "v_cvt_pk_bf16_f32 v23, v64, v65\n"
    "v_cvt_pk_bf16_f32 v24, v66, v67\n"
    "v_cvt_pk_bf16_f32 v25, v68, v69\n"
    "ds_write_b128 v77, v[22:25] offset:0\n"
    "s_and_b32 s41, s46, 7\n"
    "s_lshl_b32 s41, s41, 2\n"
    "s_add_u32 s41, s41, s45\n"
    "s_mul_i32 s42, s41, 28672\n"
    "s_lshl_b32 s41, s41, 14\n"
    "s_add_u32 s41, s41, s57\n"
    "s_add_u32 s42, s42, s57\n"
    "v_bfe_u32 v7, v0, 1, 5\n"
    "v_min_u32 v7, 27, v7\n"
    "v_lshlrev_b32 v7, 10, v7\n"
    "buffer_load_dword v157, v7, s[28:31], s42 offen\n"
    "v_and_b32 v7, 0x3fff, v7\n"
    "buffer_load_dword v157, v7, s[12:15], s41 offen\n"
    "s_waitcnt vmcnt(16) lgkmcnt(0)\n"
    "s_barrier\n"
    "s_waitcnt vmcnt(12)\n"
    "s_barrier\n"
    "s_add_u32 m0, s52, 0x16000\n"
    "s_lshl_b32 s62, s43, 1\n"
    "s_add_u32 s62, s62, 11\n"
    "s_and_b32 s62, s62, 15\n"
    "s_mul_i32 s62, s62, 114688\n"
    "s_add_u32 s62, s62, s56\n"
    "buffer_load_dwordx4 v84, s[16:19], s62 offen offset:0 lds\n"
    "buffer_load_dwordx4 v85, s[16:19], s62 offen offset:1024 lds\n"
    "s_add_u32 m0, s52, 0x18000\n"
    "s_lshl_b32 s62, s43, 1\n"
    "s_add_u32 s62, s62, 12\n"
    "s_and_b32 s62, s62, 15\n"
    "s_mul_i32 s62, s62, 114688\n"
    "s_add_u32 s62, s62, s56\n"
    "buffer_load_dwordx4 v84, s[16:19], s62 offen offset:0 lds\n"
    "buffer_load_dwordx4 v85, s[16:19], s62 offen offset:1024 lds\n"
    "s_waitcnt vmcnt(12)\n"
    "s_barrier\n"
    "s_add_u32 m0, s52, 0x1a000\n"
    "s_lshl_b32 s62, s43, 1\n"
    "s_add_u32 s62, s62, 13\n"
    "s_and_b32 s62, s62, 15\n"
    "s_mul_i32 s62, s62, 114688\n"
    "s_add_u32 s62, s62, s56\n"
    "buffer_load_dwordx4 v84, s[16:19], s62 offen offset:0 lds\n"
    "buffer_load_dwordx4 v85, s[16:19], s62 offen offset:1024 lds\n"
    "s_add_u32 m0, s52, 0x1c000\n"
    "s_lshl_b32 s62, s43, 1\n"
    "s_add_u32 s62, s62, 14\n"
    "s_and_b32 s62, s62, 15\n"
    "s_mul_i32 s62, s62, 114688\n"
    "s_add_u32 s62, s62, s56\n"
    "buffer_load_dwordx4 v84, s[16:19], s62 offen offset:0 lds\n"
    "buffer_load_dwordx4 v85, s[16:19], s62 offen offset:1024 lds\n"
    "s_waitcnt vmcnt(12)\n"
    "s_barrier\n"
    "s_add_u32 m0, s52, 0x1e000\n"
    "s_lshl_b32 s62, s43, 1\n"
    "s_add_u32 s62, s62, 15\n"
    "s_and_b32 s62, s62, 15\n"
    "s_mul_i32 s62, s62, 114688\n"
    "s_add_u32 s62, s62, s56\n"
    "buffer_load_dwordx4 v84, s[16:19], s62 offen offset:0 lds\n"
    "buffer_load_dwordx4 v85, s[16:19], s62 offen offset:1024 lds\n"
    "s_waitcnt vmcnt(8)\n"
    "s_barrier\n"
    "s_lshl_b32 s50, s46, 14\n"
    "s_add_u32 s50, s50, 0x10000\n"
    "buffer_load_dwordx4 v[100:103], v99, s[36:39], s50 offen sc1\n"
    "s_add_u32 s50, s50, 4096\n"
    "buffer_load_dwordx4 v[104:107], v99, s[36:39], s50 offen sc1\n"
    "s_add_u32 s50, s50, 4096\n"
    "buffer_load_dwordx4 v[108:111], v99, s[36:39], s50 offen sc1\n"
    "s_add_u32 s50, s50, 4096\n"
    "buffer_load_dwordx4 v[112:115], v99, s[36:39], s50 offen sc1\n"
    "s_add_u32 m0, s53, 0x20000\n"
    "s_lshl_b32 s62, s43, 1\n"
    "s_add_u32 s62, s62, 0\n"
    "s_and_b32 s62, s62, 15\n"
    "s_mul_i32 s62, s62, 98304\n"
    "s_add_u32 s62, s62, s57\n"
    "buffer_load_dwordx4 v88, s[8:11], s62 offen offset:0 lds\n"
    "buffer_load_dwordx4 v88, s[8:11], s62 offen offset:1024 lds\n"
    "buffer_load_dwordx4 v88, s[8:11], s62 offen offset:2048 lds\n"
    "s_add_u32 m0, s53, 0x23000\n"
    "s_lshl_b32 s62, s43, 1\n"
    "s_add_u32 s62, s62, 1\n"
    "s_and_b32 s62, s62, 15\n"
    "s_mul_i32 s62, s62, 98304\n"
    "s_add_u32 s62, s62, s57\n"
    "buffer_load_dwordx4 v88, s[8:11], s62 offen offset:0 lds\n"
    "buffer_load_dwordx4 v88, s[8:11], s62 offen offset:1024 lds\n"
    "buffer_load_dwordx4 v88, s[8:11], s62 offen offset:2048 lds\n"
    "s_waitcnt vmcnt(14)\n"
    "s_barrier\n"
    "s_add_u32 m0, s53, 0x14000\n"
    "s_lshl_b32 s62, s43, 1\n"
    "s_add_u32 s62, s62, 2\n"
    "s_and_b32 s62, s62, 15\n"
    "s_mul_i32 s62, s62, 98304\n"
    "s_add_u32 s62, s62, s57\n"
    "buffer_load_dwordx4 v88, s[8:11], s62 offen offset:0 lds\n"
    "buffer_load_dwordx4 v88, s[8:11], s62 offen offset:1024 lds\n"
    "buffer_load_dwordx4 v88, s[8:11], s62 offen offset:2048 lds\n"
    "s_waitcnt vmcnt(13)\n"
    "s_barrier\n"
    "s_add_u32 m0, s53, 0x17000\n"
    "s_lshl_b32 s62, s43, 1\n"
    "s_add_u32 s62, s62, 3\n"
    "s_and_b32 s62, s62, 15\n"
    "s_mul_i32 s62, s62, 98304\n"
    "s_add_u32 s62, s62, s57\n"
    "buffer_load_dwordx4 v88, s[8:11], s62 offen offset:0 lds\n"
    "buffer_load_dwordx4 v88, s[8:11], s62 offen offset:1024 lds\n"
    "buffer_load_dwordx4 v88, s[8:11], s62 offen offset:2048 lds\n"
    "s_waitcnt vmcnt(12)\n"
    "v_cmpx_eq_u32 vcc, s48, v101\n"
    "v_cmpx_eq_u32 vcc, s49, v103\n"
    "v_cmpx_eq_u32 vcc, s48, v105\n"
    "v_cmpx_eq_u32 vcc, s49, v107\n"
    "v_cmpx_eq_u32 vcc, s48, v109\n"
    "v_cmpx_eq_u32 vcc, s49, v111\n"
    "v_cmpx_eq_u32 vcc, s48, v113\n"
    "v_cmpx_eq_u32 vcc, s49, v115\n"
    "s_nop 1\n"
    "s_cmp_eq_u64 vcc, -1\n"
    "s_mov_b64 exec, -1\n"
    "s_cbranch_scc1 L_pok0_%=\n"
    "s_mov_b32 s63, 0x20000\n"
    "L_pl0_%=:\n"
    "s_lshl_b32 s50, s46, 14\n"
    "s_add_u32 s50, s50, 0x10000\n"
    "buffer_load_dwordx4 v[100:103], v99, s[36:39], s50 offen sc1\n"
    "s_add_u32 s50, s50, 4096\n"
    "buffer_load_dwordx4 v[104:107], v99, s[36:39], s50 offen sc1\n"
    "s_add_u32 s50, s50, 4096\n"
    "buffer_load_dwordx4 v[108:111], v99, s[36:39], s50 offen sc1\n"
    "s_add_u32 s50, s50, 4096\n"
    "buffer_load_dwordx4 v[112:115], v99, s[36:39], s50 offen sc1\n"
    "s_waitcnt vmcnt(0)\n"
    "v_cmpx_eq_u32 vcc, s48, v101\n"
    "v_cmpx_eq_u32 vcc, s49, v103\n"
    "v_cmpx_eq_u32 vcc, s48, v105\n"
    "v_cmpx_eq_u32 vcc, s49, v107\n"
    "v_cmpx_eq_u32 vcc, s48, v109\n"
    "v_cmpx_eq_u32 vcc, s49, v111\n"
    "v_cmpx_eq_u32 vcc, s48, v113\n"
    "v_cmpx_eq_u32 vcc, s49, v115\n"
    "s_nop 1\n"
    "s_cmp_eq_u64 vcc, -1\n"
    "s_mov_b64 exec, -1\n"
    "s_cbranch_scc1 L_pok0_%=\n"
    "s_sleep 1\n"
    "s_sub_u32 s63, s63, 1\n"
    "s_cmp_lg_u32 s63, 0\n"
    "s_cbranch_scc1 L_pl0_%=\n"
    "L_pok0_%=:\n"
    "v_add_f32 v100, v100, v104\n"
    "v_add_f32 v102, v102, v106\n"
    "v_add_f32 v108, v108, v112\n"
    "v_add_f32 v110, v110, v114\n"
    "v_add_f32 v100, v100, v108\n"
    "v_add_f32 v102, v102, v110\n"
    "v_mov_b32 v94, v100\n"
    "v_mov_b32 v95, v102\n"
    "s_nop 0\n"
    "v_permlane16_swap_b32 v100, v94\n"
    "v_permlane16_swap_b32 v102, v95\n"
    "v_add_f32 v100, v100, v94\n"
    "v_add_f32 v102, v102, v95\n"
    "v_mov_b32 v94, v100\n"
    "v_mov_b32 v95, v102\n"
    "s_nop 0\n"
    "v_permlane32_swap_b32 v100, v94\n"
    "v_permlane32_swap_b32 v102, v95\n"
    "v_add_f32 v100, v100, v94\n"
    "v_add_f32 v102, v102, v95\n"
    "v_mul_f32 v100, 0xba924925, v100\n"
    "v_mul_f32 v102, 0x3a924925, v102\n"
    "v_fma_f32 v102, -v100, v100, v102\n"
    "v_max_f32 v102, 0, v102\n"
    "v_add_f32 v102, 0x3727c5ac, v102\n"
    "v_rsq_f32 v94, v102\n"
    "s_nop 0\n"
    "v_mul_f32 v95, v100, v94\n"
    "ds_write_b32 v98, v94 offset:2048\n"
    "ds_write_b32 v98, v95 offset:2304\n"
    "s_waitcnt vmcnt(9) lgkmcnt(0)\n"
    "s_barrier\n"
    "s_add_u32 m0, s53, 0x1a000\n"
    "s_lshl_b32 s62, s43, 1\n"
    "s_add_u32 s62, s62, 4\n"
    "s_and_b32 s62, s62, 15\n"
    "s_mul_i32 s62, s62, 98304\n"
    "s_add_u32 s62, s62, s57\n"
    "buffer_load_dwordx4 v88, s[8:11], s62 offen offset:0 lds\n"
    "buffer_load_dwordx4 v88, s[8:11], s62 offen offset:1024 lds\n"
    "buffer_load_dwordx4 v88, s[8:11], s62 offen offset:2048 lds\n"
    "s_add_u32 m0, s53, 0x1d000\n"
    "s_lshl_b32 s62, s43, 1\n"
    "s_add_u32 s62, s62, 5\n"
    "s_and_b32 s62, s62, 15\n"
    "s_mul_i32 s62, s62, 98304\n"
    "s_add_u32 s62, s62, s57\n"
    "buffer_load_dwordx4 v88, s[8:11], s62 offen offset:0 lds\n"
    "buffer_load_dwordx4 v88, s[8:11], s62 offen offset:1024 lds\n"
    "buffer_load_dwordx4 v88, s[8:11], s62 offen offset:2048 lds\n"
    "s_barrier\n"
    "s_add_u32 m0, s53, 0x20000\n"
    "s_lshl_b32 s62, s43, 1\n"
    "s_add_u32 s62, s62, 6\n"
    "s_and_b32 s62, s62, 15\n"
    "s_mul_i32 s62, s62, 98304\n"
    "s_add_u32 s62, s62, s57\n"
    "buffer_load_dwordx4 v88, s[8:11], s62 offen offset:0 lds\n"
    "buffer_load_dwordx4 v88, s[8:11], s62 offen offset:1024 lds\n"
    "buffer_load_dwordx4 v88, s[8:11], s62 offen offset:2048 lds\n"
    "s_waitcnt vmcnt(15)\n"
    "s_barrier\n"
    "s_and_b32 s41, s47, 3\n"
    "s_mul_i32 s40, s41, 12\n"
    "s_add_u32 s40, s40, 2\n"
    "s_lshr_b32 s40, s40, 2\n"
    "s_mul_i32 s61, s41, 8\n"
    "s_lshr_b32 s61, s61, 2\n"
    "s_lshr_b32 s42, s46, 1\n"
    "s_and_b32 s41, s46, 1\n"
    "s_mul_i32 s41, s41, 12\n"
    "s_waitcnt vmcnt(9)\n"
    "s_barrier\n"
    "s_add_u32 m0, s53, 0x23000\n"
    "s_lshl_b32 s62, s43, 1\n"
    "s_add_u32 s62, s62, 7\n"
    "s_and_b32 s62, s62, 15\n"
    "s_mul_i32 s62, s62, 98304\n"
    "s_add_u32 s62, s62, s57\n"
    "buffer_load_dwordx4 v88, s[8:11], s62 offen offset:0 lds\n"
    "buffer_load_dwordx4 v88, s[8:11], s62 offen offset:1024 lds\n"
    "buffer_load_dwordx4 v88, s[8:11], s62 offen offset:2048 lds\n"
    "s_waitcnt vmcnt(6)\n"
    "s_barrier\n"
    "s_add_u32 m0, s53, 0x14000\n"
    "s_lshl_b32 s62, s43, 1\n"
    "s_add_u32 s62, s62, 8\n"
    "s_and_b32 s62, s62, 15\n"
    "s_mul_i32 s62, s62, 98304\n"
    "s_add_u32 s62, s62, s57\n"
    "buffer_load_dwordx4 v88, s[8:11], s62 offen offset:0 lds\n"
    "buffer_load_dwordx4 v88, s[8:11], s62 offen offset:1024 lds\n"
    "buffer_load_dwordx4 v88, s[8:11], s62 offen offset:2048 lds\n"
    "s_add_u32 m0, s53, 0x17000\n"
    "s_lshl_b32 s62, s43, 1\n"
    "s_add_u32 s62, s62, 9\n"
    "s_and_b32 s62, s62, 15\n"
    "s_mul_i32 s62, s62, 98304\n"
    "s_add_u32 s62, s62, s57\n"
    "buffer_load_dwordx4 v88, s[8:11], s62 offen offset:0 lds\n"
    "buffer_load_dwordx4 v88, s[8:11], s62 offen offset:1024 lds\n"
    "buffer_load_dwordx4 v88, s[8:11], s62 offen offset:2048 lds\n"
    "s_waitcnt vmcnt(6)\n"
    "s_barrier\n"
    "s_add_u32 m0, s53, 0x1a000\n"
    "s_lshl_b32 s62, s43, 1\n"
    "s_add_u32 s62, s62, 10\n"
    "s_and_b32 s62, s62, 15\n"
    "s_mul_i32 s62, s62, 98304\n"
    "s_add_u32 s62, s62, s57\n"
    "buffer_load_dwordx4 v88, s[8:11], s62 offen offset:0 lds\n"
    "buffer_load_dwordx4 v88, s[8:11], s62 offen offset:1024 lds\n"
    "buffer_load_dwordx4 v88, s[8:11], s62 offen offset:2048 lds\n"
    "s_add_u32 m0, s53, 0x1d000\n"
    "s_lshl_b32 s62, s43, 1\n"
    "s_add_u32 s62, s62, 11\n"
    "s_and_b32 s62, s62, 15\n"
    "s_mul_i32 s62, s62, 98304\n"
    "s_add_u32 s62, s62, s57\n"
    "buffer_load_dwordx4 v88, s[8:11], s62 offen offset:0 lds\n"
    "buffer_load_dwordx4 v88, s[8:11], s62 offen offset:1024 lds\n"
    "buffer_load_dwordx4 v88, s[8:11], s62 offen offset:2048 lds\n"
    "s_waitcnt vmcnt(6)\n"
    "s_barrier\n"
    "s_add_u32 m0, s53, 0x20000\n"
    "s_lshl_b32 s62, s43, 1\n"
    "s_add_u32 s62, s62, 12\n"
    "s_and_b32 s62, s62, 15\n"
    "s_mul_i32 s62, s62, 98304\n"
    "s_add_u32 s62, s62, s57\n"
    "buffer_load_dwordx4 v88, s[8:11], s62 offen offset:0 lds\n"
    "buffer_load_dwordx4 v88, s[8:11], s62 offen offset:1024 lds\n"
    "buffer_load_dwordx4 v88, s[8:11], s62 offen offset:2048 lds\n"
    "s_add_u32 m0, s53, 0x23000\n"
    "s_lshl_b32 s62, s43, 1\n"
    "s_add_u32 s62, s62, 13\n"
    "s_and_b32 s62, s62, 15\n"
    "s_mul_i32 s62, s62, 98304\n"
    "s_add_u32 s62, s62, s57\n"
    "buffer_load_dwordx4 v88, s[8:11], s62 offen offset:0 lds\n"
    "buffer_load_dwordx4 v88, s[8:11], s62 offen offset:1024 lds\n"
    "buffer_load_dwordx4 v88, s[8:11], s62 offen offset:2048 lds\n"
    "s_lshl_b32 s50, s47, 10\n"
    "s_add_u32 s50, s50, 0x1000\n"
    "buffer_load_dwordx4 v[100:103], v116, s[36:39], s50 offen sc1\n"
    "s_waitcnt vmcnt(7)\n"
    "s_barrier\n"
    "s_add_u32 m0, s53, 0x14000\n"
    "s_lshl_b32 s62, s43, 1\n"
    "s_add_u32 s62, s62, 14\n"
    "s_and_b32 s62, s62, 15\n"
    "s_mul_i32 s62, s62, 98304\n"
    "s_add_u32 s62, s62, s57\n"
    "buffer_load_dwordx4 v88, s[8:11], s62 offen offset:0 lds\n"
    "buffer_load_dwordx4 v88, s[8:11], s62 offen offset:1024 lds\n"
    "buffer_load_dwordx4 v88, s[8:11], s62 offen offset:2048 lds\n"
    "s_add_u32 m0, s53, 0x17000\n"
    "s_lshl_b32 s62, s43, 1\n"
    "s_add_u32 s62, s62, 15\n"
    "s_and_b32 s62, s62, 15\n"
    "s_mul_i32 s62, s62, 98304\n"
    "s_add_u32 s62, s62, s57\n"
    "buffer_load_dwordx4 v88, s[8:11], s62 offen offset:0 lds\n"
    "buffer_load_dwordx4 v88, s[8:11], s62 offen offset:1024 lds\n"
    "buffer_load_dwordx4 v88, s[8:11], s62 offen offset:2048 lds\n"
    "s_add_u32 s50, s40, 0\n"
    "s_sub_u32 s51, s50, 12\n"
    "s_cselect_b32 s50, s50, s51\n"
    "s_add_u32 s50, s50, s41\n"
    "s_add_u32 m0, s52, 0x0\n"
    "s_mul_i32 s62, s50, 128\n"
    "buffer_load_dwordx4 v82, s[20:23], s62 offen offset:0 lds\n"
    "buffer_load_dwordx4 v83, s[20:23], s62 offen offset:1024 lds\n"
    "s_add_u32 m0, s52, 0x2000\n"
    "s_nop 0\n"
    "buffer_load_dwordx4 v86, s[20:23], s62 offen offset:0 lds\n"
    "buffer_load_dwordx4 v87, s[20:23], s62 offen offset:1024 lds\n"
    "s_waitcnt vmcnt(11)\n"
    "s_barrier\n"
    "s_waitcnt vmcnt(10)\n"
    "v_cmp_eq_u32 vcc, s48, v100\n"
    "v_cmp_eq_u32_e64 s[64:65], s49, v101\n"
    "s_nop 0\n"
    "s_and_b64 vcc, vcc, s[64:65]\n"
    "v_cmp_eq_u32_e64 s[64:65], s48, v102\n"
    "s_nop 0\n"
    "s_and_b64 vcc, vcc, s[64:65]\n"
    "v_cmp_eq_u32_e64 s[64:65], s49, v103\n"
    "s_nop 0\n"
    "s_and_b64 vcc, vcc, s[64:65]\n"
    "s_cmp_eq_u64 vcc, -1\n"
    "s_cbranch_scc1 L_pok1_%=\n"
    "s_mov_b32 s63, 0x20000\n"
    "L_pl1_%=:\n"
    "s_lshl_b32 s50, s47, 10\n"
    "s_add_u32 s50, s50, 0x1000\n"
    "buffer_load_dwordx4 v[100:103], v116, s[36:39], s50 offen sc1\n"
    "s_waitcnt vmcnt(0)\n"
    "v_cmp_eq_u32 vcc, s48, v100\n"
    "v_cmp_eq_u32_e64 s[64:65], s49, v101\n"
    "s_nop 0\n"
    "s_and_b64 vcc, vcc, s[64:65]\n"
    "v_cmp_eq_u32_e64 s[64:65], s48, v102\n"
    "s_nop 0\n"
    "s_and_b64 vcc, vcc, s[64:65]\n"
    "v_cmp_eq_u32_e64 s[64:65], s49, v103\n"
    "s_nop 0\n"
    "s_and_b64 vcc, vcc, s[64:65]\n"
    "s_cmp_eq_u64 vcc, -1\n"
    "s_cbranch_scc1 L_pok1_%=\n"
    "s_sleep 1\n"
    "s_sub_u32 s63, s63, 1\n"
    "s_cmp_lg_u32 s63, 0\n"
    "s_cbranch_scc1 L_pl1_%=\n"
    "L_pok1_%=:\n"
    "s_or_b32 s50, s2, s45\n"
    "s_cmp_lg_u32 s50, 0\n"
    "s_cbranch_scc1 L_noe_%=\n"
    "v_mov_b32 v100, s48\n"
    "v_mov_b32 v101, s49\n"
    "s_mov_b64 exec, 1\n"
    "buffer_store_dwordx2 v[100:101], off, s[36:39], 0 sc1\n"
    "s_mov_b64 exec, -1\n"
    "s_waitcnt vmcnt(0)\n"
    "L_noe_%=:\n"
    "s_add_u32 s50, s40, 0\n"
    "s_sub_u32 s51, s50, 12\n"
    "s_cselect_b32 s50, s50, s51\n"
    "s_add_u32 s50, s50, s41\n"
    "s_add_u32 m0, s54, 0x1b000\n"
    "s_mul_i32 s62, s50, 14336\n"
    "s_add_u32 s62, s62, s60\n"
    "s_mov_b64 exec, s[66:67]\n"
    "buffer_load_dwordx4 v92, s[36:39], s62 offen offset:0 sc1 lds\n"
    "buffer_load_dwordx4 v92, s[36:39], s62 offen offset:896 sc1 lds\n"
    "buffer_load_dwordx4 v92, s[36:39], s62 offen offset:1792 sc1 lds\n"
    "buffer_load_dwordx4 v92, s[36:39], s62 offen offset:2688 sc1 lds\n"
    "s_mov_b64 exec, -1\n"
    "s_add_u32 s50, s40, 1\n"
    "s_sub_u32 s51, s50, 12\n"
    "s_cselect_b32 s50, s50, s51\n"
    "s_add_u32 s50, s50, s41\n"
    "s_add_u32 m0, s52, 0x4000\n"
    "s_mul_i32 s62, s50, 128\n"
    "buffer_load_dwordx4 v82, s[20:23], s62 offen offset:0 lds\n"
    "buffer_load_dwordx4 v83, s[20:23], s62 offen offset:1024 lds\n"
    "s_add_u32 m0, s52, 0x6000\n"
    "s_nop 0\n"
    "buffer_load_dwordx4 v86, s[20:23], s62 offen offset:0 lds\n"
    "buffer_load_dwordx4 v87, s[20:23], s62 offen offset:1024 lds\n"
    "s_waitcnt vmcnt(15)\n"
    "s_barrier\n"
    "s_add_u32 s50, s40, 1\n"
    "s_sub_u32 s51, s50, 12\n"
    "s_cselect_b32 s50, s50, s51\n"
    "s_add_u32 s50, s50, s41\n"
    "s_add_u32 m0, s54, 0x1e800\n"
    "s_mul_i32 s62, s50, 14336\n"
    "s_add_u32 s62, s62, s60\n"
    "s_mov_b64 exec, s[66:67]\n"
    "buffer_load_dwordx4 v92, s[36:39], s62 offen offset:0 sc1 lds\n"
    "buffer_load_dwordx4 v92, s[36:39], s62 offen offset:896 sc1 lds\n"
    "buffer_load_dwordx4 v92, s[36:39], s62 offen offset:1792 sc1 lds\n"
    "buffer_load_dwordx4 v92, s[36:39], s62 offen offset:2688 sc1 lds\n"
    "s_mov_b64 exec, -1\n"
    "s_waitcnt vmcnt(16)\n"
    "s_barrier\n"
    "s_add_u32 s50, s40, 2\n"
    "s_sub_u32 s51, s50, 12\n"
    "s_cselect_b32 s50, s50, s51\n"
    "s_add_u32 s50, s50, s41\n"
    "s_add_u32 m0, s52, 0x8000\n"
    "s_mul_i32 s62, s50, 128\n"
    "buffer_load_dwordx4 v82, s[20:23], s62 offen offset:0 lds\n"
    "buffer_load_dwordx4 v83, s[20:23], s62 offen offset:1024 lds\n"
    "s_add_u32 m0, s52, 0xa000\n"
    "s_nop 0\n"
    "buffer_load_dwordx4 v86, s[20:23], s62 offen offset:0 lds\n"
    "buffer_load_dwordx4 v87, s[20:23], s62 offen offset:1024 lds\n"
    "s_barrier\n"
    "s_add_u32 s50, s40, 2\n"
    "s_sub_u32 s51, s50, 12\n"
    "s_cselect_b32 s50, s50, s51\n"
    "s_add_u32 s50, s50, s41\n"
    "s_add_u32 m0, s54, 0x22000\n"
    "s_mul_i32 s62, s50, 14336\n"
    "s_add_u32 s62, s62, s60\n"
    "s_mov_b64 exec, s[66:67]\n"
    "buffer_load_dwordx4 v92, s[36:39], s62 offen offset:0 sc1 lds\n"
    "buffer_load_dwordx4 v92, s[36:39], s62 offen offset:896 sc1 lds\n"
    "buffer_load_dwordx4 v92, s[36:39], s62 offen offset:1792 sc1 lds\n"
    "buffer_load_dwordx4 v92, s[36:39], s62 offen offset:2688 sc1 lds\n"
    "s_mov_b64 exec, -1\n"
    "s_add_u32 s50, s40, 3\n"
    "s_sub_u32 s51, s50, 12\n"
    "s_cselect_b32 s50, s50, s51\n"
    "s_add_u32 s50, s50, s41\n"
    "s_add_u32 m0, s52, 0xc000\n"
    "s_mul_i32 s62, s50, 128\n"
    "buffer_load_dwordx4 v82, s[20:23], s62 offen offset:0 lds\n"
    "buffer_load_dwordx4 v83, s[20:23], s62 offen offset:1024 lds\n"
    "s_add_u32 m0, s52, 0xe000\n"
    "s_nop 0\n"
    "buffer_load_dwordx4 v86, s[20:23], s62 offen offset:0 lds\n"
    "buffer_load_dwordx4 v87, s[20:23], s62 offen offset:1024 lds\n"
    "s_add_u32 s50, s40, 3\n"
    "s_sub_u32 s51, s50, 12\n"
    "s_cselect_b32 s50, s50, s51\n"
    "s_add_u32 s50, s50, s41\n"
    "s_add_u32 m0, s54, 0x14000\n"
    "s_mul_i32 s62, s50, 14336\n"
    "s_add_u32 s62, s62, s60\n"
    "s_waitcnt vmcnt(20)\n"
    "s_barrier\n"
    "s_mov_b64 exec, s[66:67]\n"
    "buffer_load_dwordx4 v92, s[36:39], s62 offen offset:0 sc1 lds\n"
    "buffer_load_dwordx4 v92, s[36:39], s62 offen offset:896 sc1 lds\n"
    "buffer_load_dwordx4 v92, s[36:39], s62 offen offset:1792 sc1 lds\n"
    "buffer_load_dwordx4 v92, s[36:39], s62 offen offset:2688 sc1 lds\n"
    "s_mov_b64 exec, -1\n"
    "s_add_u32 s50, s40, 4\n"
    "s_sub_u32 s51, s50, 12\n"
    "s_cselect_b32 s50, s50, s51\n"
    "s_add_u32 s50, s50, s41\n"
    "s_add_u32 m0, s52, 0x10000\n"
    "s_mul_i32 s62, s50, 128\n"
    "buffer_load_dwordx4 v82, s[20:23], s62 offen offset:0 lds\n"
    "buffer_load_dwordx4 v83, s[20:23], s62 offen offset:1024 lds\n"
    "s_add_u32 m0, s52, 0x12000\n"
    "s_nop 0\n"
    "buffer_load_dwordx4 v86, s[20:23], s62 offen offset:0 lds\n"
    "buffer_load_dwordx4 v87, s[20:23], s62 offen offset:1024 lds\n"
    "s_add_u32 s50, s40, 4\n"
    "s_sub_u32 s51, s50, 12\n"
    "s_cselect_b32 s50, s50, s51\n"
    "s_add_u32 s50, s50, s41\n"
    "s_add_u32 m0, s54, 0x17800\n"
    "s_mul_i32 s62, s50, 14336\n"
    "s_add_u32 s62, s62, s60\n"
    "s_waitcnt vmcnt(20)\n"
    "s_barrier\n"
    "s_mov_b64 exec, s[66:67]\n"
    "buffer_load_dwordx4 v92, s[36:39], s62 offen offset:0 sc1 lds\n"
    "buffer_load_dwordx4 v92, s[36:39], s62 offen offset:896 sc1 lds\n"
    "buffer_load_dwordx4 v92, s[36:39], s62 offen offset:1792 sc1 lds\n"
    "buffer_load_dwordx4 v92, s[36:39], s62 offen offset:2688 sc1 lds\n"
    "s_mov_b64 exec, -1\n"
    "s_add_u32 s50, s40, 5\n"
    "s_sub_u32 s51, s50, 12\n"
    "s_cselect_b32 s50, s50, s51\n"
    "s_add_u32 s50, s50, s41\n"
    "s_add_u32 m0, s52, 0x0\n"
    "s_mul_i32 s62, s50, 128\n"
    "buffer_load_dwordx4 v82, s[20:23], s62 offen offset:0 lds\n"
    "buffer_load_dwordx4 v83, s[20:23], s62 offen offset:1024 lds\n"
    "s_add_u32 m0, s52, 0x2000\n"
    "s_nop 0\n"
    "buffer_load_dwordx4 v86, s[20:23], s62 offen offset:0 lds\n"
    "buffer_load_dwordx4 v87, s[20:23], s62 offen offset:1024 lds\n"
    "s_add_u32 s50, s40, 5\n"
    "s_sub_u32 s51, s50, 12\n"
    "s_cselect_b32 s50, s50, s51\n"
    "s_add_u32 s50, s50, s41\n"
    "s_add_u32 m0, s54, 0x1b000\n"
    "s_mul_i32 s62, s50, 14336\n"
    "s_add_u32 s62, s62, s60\n"
    "s_waitcnt vmcnt(20)\n"
    "s_barrier\n"
    "s_mov_b64 exec, s[66:67]\n"
    "buffer_load_dwordx4 v92, s[36:39], s62 offen offset:0 sc1 lds\n"
    "buffer_load_dwordx4 v92, s[36:39], s62 offen offset:896 sc1 lds\n"
    "buffer_load_dwordx4 v92, s[36:39], s62 offen offset:1792 sc1 lds\n"
    "buffer_load_dwordx4 v92, s[36:39], s62 offen offset:2688 sc1 lds\n"
    "s_mov_b64 exec, -1\n"
    "s_add_u32 s50, s40, 6\n"
    "s_sub_u32 s51, s50, 12\n"
    "s_cselect_b32 s50, s50, s51\n"
    "s_add_u32 s50, s50, s41\n"
    "s_add_u32 m0, s52, 0x4000\n"
    "s_mul_i32 s62, s50, 128\n"
    "buffer_load_dwordx4 v82, s[20:23], s62 offen offset:0 lds\n"
    "buffer_load_dwordx4 v83, s[20:23], s62 offen offset:1024 lds\n"
    "s_add_u32 m0, s52, 0x6000\n"
    "s_nop 0\n"
    "buffer_load_dwordx4 v86, s[20:23], s62 offen offset:0 lds\n"
    "buffer_load_dwordx4 v87, s[20:23], s62 offen offset:1024 lds\n"
    "s_add_u32 s50, s40, 6\n"
    "s_sub_u32 s51, s50, 12\n"
    "s_cselect_b32 s50, s50, s51\n"
    "s_add_u32 s50, s50, s41\n"
    "s_add_u32 m0, s54, 0x1e800\n"
    "s_mul_i32 s62, s50, 14336\n"
    "s_add_u32 s62, s62, s60\n"
    "s_waitcnt vmcnt(20)\n"
    "s_barrier\n"
    "s_mov_b64 exec, s[66:67]\n"
    "buffer_load_dwordx4 v92, s[36:39], s62 offen offset:0 sc1 lds\n"
    "buffer_load_dwordx4 v92, s[36:39], s62 offen offset:896 sc1 lds\n"
    "buffer_load_dwordx4 v92, s[36:39], s62 offen offset:1792 sc1 lds\n"
    "buffer_load_dwordx4 v92, s[36:39], s62 offen offset:2688 sc1 lds\n"
    "s_mov_b64 exec, -1\n"
    "s_add_u32 s50, s40, 7\n"
    "s_sub_u32 s51, s50, 12\n"
    "s_cselect_b32 s50, s50, s51\n"
    "s_add_u32 s50, s50, s41\n"
    "s_add_u32 m0, s52, 0x8000\n"
    "s_mul_i32 s62, s50, 128\n"
    "buffer_load_dwordx4 v82, s[20:23], s62 offen offset:0 lds\n"
    "buffer_load_dwordx4 v83, s[20:23], s62 offen offset:1024 lds\n"
    "s_add_u32 m0, s52, 0xa000\n"
    "s_nop 0\n"
    "buffer_load_dwordx4 v86, s[20:23], s62 offen offset:0 lds\n"
    "buffer_load_dwordx4 v87, s[20:23], s62 offen offset:1024 lds\n"
    "s_add_u32 s50, s40, 7\n"
    "s_sub_u32 s51, s50, 12\n"
    "s_cselect_b32 s50, s50, s51\n"
    "s_add_u32 s50, s50, s41\n"
    "s_add_u32 m0, s54, 0x22000\n"
    "s_mul_i32 s62, s50, 14336\n"
    "s_add_u32 s62, s62, s60\n"
    "s_waitcnt vmcnt(20)\n"
    "s_barrier\n"
    "s_mov_b64 exec, s[66:67]\n"
    "buffer_load_dwordx4 v92, s[36:39], s62 offen offset:0 sc1 lds\n"
    "buffer_load_dwordx4 v92, s[36:39], s62 offen offset:896 sc1 lds\n"
    "buffer_load_dwordx4 v92, s[36:39], s62 offen offset:1792 sc1 lds\n"
    "buffer_load_dwordx4 v92, s[36:39], s62 offen offset:2688 sc1 lds\n"
    "s_mov_b64 exec, -1\n"
    "s_add_u32 s50, s40, 8\n"
    "s_sub_u32 s51, s50, 12\n"
    "s_cselect_b32 s50, s50, s51\n"
    "s_add_u32 s50, s50, s41\n"
    "s_add_u32 m0, s52, 0xc000\n"
    "s_mul_i32 s62, s50, 128\n"
    "buffer_load_dwordx4 v82, s[20:23], s62 offen offset:0 lds\n"
    "buffer_load_dwordx4 v83, s[20:23], s62 offen offset:1024 lds\n"
    "s_add_u32 m0, s52, 0xe000\n"
    "s_nop 0\n"
    "buffer_load_dwordx4 v86, s[20:23], s62 offen offset:0 lds\n"
    "buffer_load_dwordx4 v87, s[20:23], s62 offen offset:1024 lds\n"
    "s_add_u32 s50, s40, 8\n"
    "s_sub_u32 s51, s50, 12\n"
    "s_cselect_b32 s50, s50, s51\n"
    "s_add_u32 s50, s50, s41\n"
    "s_add_u32 m0, s54, 0x14000\n"
    "s_mul_i32 s62, s50, 14336\n"
    "s_add_u32 s62, s62, s60\n"
    "s_waitcnt vmcnt(20)\n"
    "s_barrier\n"
    "s_mov_b64 exec, s[66:67]\n"
    "buffer_load_dwordx4 v92, s[36:39], s62 offen offset:0 sc1 lds\n"
    "buffer_load_dwordx4 v92, s[36:39], s62 offen offset:896 sc1 lds\n"
    "buffer_load_dwordx4 v92, s[36:39], s62 offen offset:1792 sc1 lds\n"
    "buffer_load_dwordx4 v92, s[36:39], s62 offen offset:2688 sc1 lds\n"
    "s_mov_b64 exec, -1\n"
    "s_add_u32 s50, s40, 9\n"
    "s_sub_u32 s51, s50, 12\n"
    "s_cselect_b32 s50, s50, s51\n"
    "s_add_u32 s50, s50, s41\n"
    "s_add_u32 m0, s52, 0x10000\n"
    "s_mul_i32 s62, s50, 128\n"
    "buffer_load_dwordx4 v82, s[20:23], s62 offen offset:0 lds\n"
    "buffer_load_dwordx4 v83, s[20:23], s62 offen offset:1024 lds\n"
    "s_add_u32 m0, s52, 0x12000\n"
    "s_nop 0\n"
    "buffer_load_dwordx4 v86, s[20:23], s62 offen offset:0 lds\n"
    "buffer_load_dwordx4 v87, s[20:23], s62 offen offset:1024 lds\n"
    "s_add_u32 s50, s40, 9\n"
    "s_sub_u32 s51, s50, 12\n"
    "s_cselect_b32 s50, s50, s51\n"
    "s_add_u32 s50, s50, s41\n"
    "s_add_u32 m0, s54, 0x17800\n"
    "s_mul_i32 s62, s50, 14336\n"
    "s_add_u32 s62, s62, s60\n"
    "s_waitcnt vmcnt(20)\n"
    "s_barrier\n"
    "s_mov_b64 exec, s[66:67]\n"
    "buffer_load_dwordx4 v92, s[36:39], s62 offen offset:0 sc1 lds\n"
    "buffer_load_dwordx4 v92, s[36:39], s62 offen offset:896 sc1 lds\n"
    "buffer_load_dwordx4 v92, s[36:39], s62 offen offset:1792 sc1 lds\n"
    "buffer_load_dwordx4 v92, s[36:39], s62 offen offset:2688 sc1 lds\n"
    "s_mov_b64 exec, -1\n"
    "s_add_u32 s50, s40, 10\n"
    "s_sub_u32 s51, s50, 12\n"
    "s_cselect_b32 s50, s50, s51\n"
    "s_add_u32 s50, s50, s41\n"
    "s_add_u32 m0, s52, 0x0\n"
    "s_mul_i32 s62, s50, 128\n"
    "buffer_load_dwordx4 v82, s[20:23], s62 offen offset:0 lds\n"
    "buffer_load_dwordx4 v83, s[20:23], s62 offen offset:1024 lds\n"
    "s_add_u32 m0, s52, 0x2000\n"
    "s_nop 0\n"
    "buffer_load_dwordx4 v86, s[20:23], s62 offen offset:0 lds\n"
    "buffer_load_dwordx4 v87, s[20:23], s62 offen offset:1024 lds\n"
    "s_add_u32 s50, s40, 10\n"
    "s_sub_u32 s51, s50, 12\n"
    "s_cselect_b32 s50, s50, s51\n"
    "s_add_u32 s50, s50, s41\n"
    "s_add_u32 m0, s54, 0x1b000\n"
    "s_mul_i32 s62, s50, 14336\n"
    "s_add_u32 s62, s62, s60\n"
    "s_waitcnt vmcnt(20)\n"
    "s_barrier\n"
    "s_mov_b64 exec, s[66:67]\n"
    "buffer_load_dwordx4 v92, s[36:39], s62 offen offset:0 sc1 lds\n"
    "buffer_load_dwordx4 v92, s[36:39], s62 offen offset:896 sc1 lds\n"
    "buffer_load_dwordx4 v92, s[36:39], s62 offen offset:1792 sc1 lds\n"
    "buffer_load_dwordx4 v92, s[36:39], s62 offen offset:2688 sc1 lds\n"
    "s_mov_b64 exec, -1\n"
    "s_add_u32 s50, s40, 11\n"
    "s_sub_u32 s51, s50, 12\n"
    "s_cselect_b32 s50, s50, s51\n"
    "s_add_u32 s50, s50, s41\n"
    "s_add_u32 m0, s52, 0x4000\n"
    "s_mul_i32 s62, s50, 128\n"
    "buffer_load_dwordx4 v82, s[20:23], s62 offen offset:0 lds\n"
    "buffer_load_dwordx4 v83, s[20:23], s62 offen offset:1024 lds\n"
    "s_add_u32 m0, s52, 0x6000\n"
    "s_nop 0\n"
    "buffer_load_dwordx4 v86, s[20:23], s62 offen offset:0 lds\n"
    "buffer_load_dwordx4 v87, s[20:23], s62 offen offset:1024 lds\n"
    "s_add_u32 s50, s40, 11\n"
    "s_sub_u32 s51, s50, 12\n"
    "s_cselect_b32 s50, s50, s51\n"
    "s_add_u32 s50, s50, s41\n"
    "s_add_u32 m0, s54, 0x1e800\n"
    "s_mul_i32 s62, s50, 14336\n"
    "s_add_u32 s62, s62, s60\n"
    "s_waitcnt vmcnt(20)\n"
    "s_barrier\n"
    "s_mov_b64 exec, s[66:67]\n"
    "buffer_load_dwordx4 v92, s[36:39], s62 offen offset:0 sc1 lds\n"
    "buffer_load_dwordx4 v92, s[36:39], s62 offen offset:896 sc1 lds\n"
    "buffer_load_dwordx4 v92, s[36:39], s62 offen offset:1792 sc1 lds\n"
    "buffer_load_dwordx4 v92, s[36:39], s62 offen offset:2688 sc1 lds\n"
    "s_mov_b64 exec, -1\n"
    "s_add_u32 s50, s61, 0\n"
    "s_sub_u32 s51, s50, 8\n"
    "s_cselect_b32 s50, s50, s51\n"
    "s_add_u32 s50, s50, 24\n"
    "s_add_u32 m0, s52, 0x8000\n"
    "s_mul_i32 s62, s50, 128\n"
    "buffer_load_dwordx4 v82, s[20:23], s62 offen offset:0 lds\n"
    "buffer_load_dwordx4 v83, s[20:23], s62 offen offset:1024 lds\n"
    "s_add_u32 s50, s61, 0\n"
    "s_sub_u32 s51, s50, 8\n"
    "s_cselect_b32 s50, s50, s51\n"
    "s_add_u32 s50, s50, 24\n"
    "s_add_u32 m0, s54, 0x24800\n"
    "s_mul_i32 s62, s50, 14336\n"
    "s_add_u32 s62, s62, s60\n"
    "s_waitcnt vmcnt(18)\n"
    "s_barrier\n"
    "s_mov_b64 exec, s[66:67]\n"
    "buffer_load_dwordx4 v92, s[36:39], s62 offen offset:0 sc1 lds\n"
    "buffer_load_dwordx4 v92, s[36:39], s62 offen offset:896 sc1 lds\n"
    "buffer_load_dwordx4 v92, s[36:39], s62 offen offset:1792 sc1 lds\n"
    "buffer_load_dwordx4 v92, s[36:39], s62 offen offset:2688 sc1 lds\n"
    "s_mov_b64 exec, -1\n"
    "s_add_u32 s50, s61, 1\n"
    "s_sub_u32 s51, s50, 8\n"
    "s_cselect_b32 s50, s50, s51\n"
    "s_add_u32 s50, s50, 24\n"
    "s_add_u32 m0, s52, 0xa000\n"
    "s_mul_i32 s62, s50, 128\n"
    "buffer_load_dwordx4 v82, s[20:23], s62 offen offset:0 lds\n"
    "buffer_load_dwordx4 v83, s[20:23], s62 offen offset:1024 lds\n"
    "s_add_u32 s50, s61, 1\n"
    "s_sub_u32 s51, s50, 8\n"
    "s_cselect_b32 s50, s50, s51\n"
    "s_add_u32 s50, s50, 24\n"
    "s_add_u32 m0, s54, 0x17800\n"
    "s_mul_i32 s62, s50, 14336\n"
    "s_add_u32 s62, s62, s60\n"
    "s_waitcnt vmcnt(16)\n"
    "s_barrier\n"
    "s_mov_b64 exec, s[66:67]\n"
    "buffer_load_dwordx4 v92, s[36:39], s62 offen offset:0 sc1 lds\n"
    "buffer_load_dwordx4 v92, s[36:39], s62 offen offset:896 sc1 lds\n"
    "buffer_load_dwordx4 v92, s[36:39], s62 offen offset:1792 sc1 lds\n"
    "buffer_load_dwordx4 v92, s[36:39], s62 offen offset:2688 sc1 lds\n"
    "s_mov_b64 exec, -1\n"
    "s_add_u32 s50, s61, 2\n"
    "s_sub_u32 s51, s50, 8\n"
    "s_cselect_b32 s50, s50, s51\n"
    "s_add_u32 s50, s50, 24\n"
    "s_add_u32 m0, s52, 0xc000\n"
    "s_mul_i32 s62, s50, 128\n"
    "buffer_load_dwordx4 v82, s[20:23], s62 offen offset:0 lds\n"
    "buffer_load_dwordx4 v83, s[20:23], s62 offen offset:1024 lds\n"
    "s_add_u32 s50, s61, 2\n"
    "s_sub_u32 s51, s50, 8\n"
    "s_cselect_b32 s50, s50, s51\n"
    "s_add_u32 s50, s50, 24\n"
    "s_add_u32 m0, s54, 0x1b000\n"
    "s_mul_i32 s62, s50, 14336\n"
    "s_add_u32 s62, s62, s60\n"
    "s_waitcnt vmcnt(14)\n"
    "s_barrier\n"
    "s_mov_b64 exec, s[66:67]\n"
    "buffer_load_dwordx4 v92, s[36:39], s62 offen offset:0 sc1 lds\n"
    "buffer_load_dwordx4 v92, s[36:39], s62 offen offset:896 sc1 lds\n"
    "buffer_load_dwordx4 v92, s[36:39], s62 offen offset:1792 sc1 lds\n"
    "buffer_load_dwordx4 v92, s[36:39], s62 offen offset:2688 sc1 lds\n"
    "s_mov_b64 exec, -1\n"
    "s_add_u32 s50, s61, 3\n"
    "s_sub_u32 s51, s50, 8\n"
    "s_cselect_b32 s50, s50, s51\n"
    "s_add_u32 s50, s50, 24\n"
    "s_add_u32 m0, s52, 0x0\n"
    "s_mul_i32 s62, s50, 128\n"
    "buffer_load_dwordx4 v82, s[20:23], s62 offen offset:0 lds\n"
    "buffer_load_dwordx4 v83, s[20:23], s62 offen offset:1024 lds\n"
    "s_lshl_b32 s50, s46, 8\n"
    "s_add_u32 s50, s50, 0x4000\n"
    "buffer_load_dwordx2 v[100:101], v117, s[36:39], s50 offen sc1\n"
    "s_add_u32 s50, s61, 3\n"
    "s_sub_u32 s51, s50, 8\n"
    "s_cselect_b32 s50, s50, s51\n"
    "s_add_u32 s50, s50, 24\n"
    "s_add_u32 m0, s54, 0x21000\n"
    "s_mul_i32 s62, s50, 14336\n"
    "s_add_u32 s62, s62, s60\n"
    "s_waitcnt vmcnt(15)\n"
    "s_barrier\n"
    "s_mov_b64 exec, s[66:67]\n"
    "buffer_load_dwordx4 v92, s[36:39], s62 offen offset:0 sc1 lds\n"
    "buffer_load_dwordx4 v92, s[36:39], s62 offen offset:896 sc1 lds\n"
    "buffer_load_dwordx4 v92, s[36:39], s62 offen offset:1792 sc1 lds\n"
    "buffer_load_dwordx4 v92, s[36:39], s62 offen offset:2688 sc1 lds\n"
    "s_mov_b64 exec, -1\n"
    "s_add_u32 s50, s61, 4\n"
    "s_sub_u32 s51, s50, 8\n"
    "s_cselect_b32 s50, s50, s51\n"
    "s_add_u32 s50, s50, 24\n"
    "s_add_u32 m0, s52, 0x2000\n"
    "s_mul_i32 s62, s50, 128\n"
    "buffer_load_dwordx4 v82, s[20:23], s62 offen offset:0 lds\n"
    "buffer_load_dwordx4 v83, s[20:23], s62 offen offset:1024 lds\n"
    "buffer_load_dwordx4 v[118:121], v122, s[4:7], 0 offen\n"
    "s_add_u32 s50, s61, 4\n"
    "s_sub_u32 s51, s50, 8\n"
    "s_cselect_b32 s50, s50, s51\n"
    "s_add_u32 s50, s50, 24\n"
    "s_add_u32 m0, s54, 0x24800\n"
    "s_mul_i32 s62, s50, 14336\n"
    "s_add_u32 s62, s62, s60\n"
    "s_waitcnt vmcnt(16)\n"
    "s_barrier\n"
    "s_mov_b64 exec, s[66:67]\n"
    "buffer_load_dwordx4 v92, s[36:39], s62 offen offset:0 sc1 lds\n"
    "buffer_load_dwordx4 v92, s[36:39], s62 offen offset:896 sc1 lds\n"
    "buffer_load_dwordx4 v92, s[36:39], s62 offen offset:1792 sc1 lds\n"
    "buffer_load_dwordx4 v92, s[36:39], s62 offen offset:2688 sc1 lds\n"
    "s_mov_b64 exec, -1\n"
    "s_add_u32 s50, s61, 5\n"
    "s_sub_u32 s51, s50, 8\n"
    "s_cselect_b32 s50, s50, s51\n"
    "s_add_u32 s50, s50, 24\n"
    "s_add_u32 m0, s52, 0x4000\n"
    "s_mul_i32 s62, s50, 128\n"
    "buffer_load_dwordx4 v82, s[20:23], s62 offen offset:0 lds\n"
    "buffer_load_dwordx4 v83, s[20:23], s62 offen offset:1024 lds\n"
    "s_waitcnt vmcnt(13)\n"
    "v_cmp_eq_u32 vcc, s48, v100\n"
    "v_cmp_eq_u32_e64 s[64:65], s49, v101\n"
    "s_nop 0\n"
    "s_and_b64 vcc, vcc, s[64:65]\n"
    "s_cmp_eq_u64 vcc, -1\n"
    "s_cbranch_scc1 L_pok2_%=\n"
    "s_mov_b32 s63, 0x20000\n"
    "L_pl2_%=:\n"
    "s_lshl_b32 s50, s46, 8\n"
    "s_add_u32 s50, s50, 0x4000\n"
    "buffer_load_dwordx2 v[100:101], v117, s[36:39], s50 offen sc1\n"
    "s_waitcnt vmcnt(0)\n"
    "v_cmp_eq_u32 vcc, s48, v100\n"
    "v_cmp_eq_u32_e64 s[64:65], s49, v101\n"
    "s_nop 0\n"
    "s_and_b64 vcc, vcc, s[64:65]\n"
    "s_cmp_eq_u64 vcc, -1\n"
    "s_cbranch_scc1 L_pok2_%=\n"
    "s_sleep 1\n"
    "s_sub_u32 s63, s63, 1\n"
    "s_cmp_lg_u32 s63, 0\n"
    "s_cbranch_scc1 L_pl2_%=\n"
    "L_pok2_%=:\n"
    "s_add_u32 s50, s61, 5\n"
    "s_sub_u32 s51, s50, 8\n"
    "s_cselect_b32 s50, s50, s51\n"
    "s_add_u32 s50, s50, 24\n"
    "s_add_u32 m0, s54, 0x17800\n"
    "s_mul_i32 s62, s50, 14336\n"
    "s_add_u32 s62, s62, s60\n"
    "s_barrier\n"
    "s_mov_b64 exec, s[66:67]\n"
    "buffer_load_dwordx4 v92, s[36:39], s62 offen offset:0 sc1 lds\n"
    "buffer_load_dwordx4 v92, s[36:39], s62 offen offset:896 sc1 lds\n"
    "buffer_load_dwordx4 v92, s[36:39], s62 offen offset:1792 sc1 lds\n"
    "buffer_load_dwordx4 v92, s[36:39], s62 offen offset:2688 sc1 lds\n"
    "s_mov_b64 exec, -1\n"
    "s_add_u32 s50, s61, 6\n"
    "s_sub_u32 s51, s50, 8\n"
    "s_cselect_b32 s50, s50, s51\n"
    "s_add_u32 s50, s50, 24\n"
    "s_add_u32 m0, s52, 0x6000\n"
    "s_mul_i32 s62, s50, 128\n"
    "buffer_load_dwordx4 v82, s[20:23], s62 offen offset:0 lds\n"
    "buffer_load_dwordx4 v83, s[20:23], s62 offen offset:1024 lds\n"
    "s_add_u32 s50, s61, 6\n"
    "s_sub_u32 s51, s50, 8\n"
    "s_cselect_b32 s50, s50, s51\n"
    "s_add_u32 s50, s50, 24\n"
    "s_add_u32 m0, s54, 0x1d800\n"
    "s_mul_i32 s62, s50, 14336\n"
    "s_add_u32 s62, s62, s60\n"
    "s_waitcnt vmcnt(15)\n"
    "s_barrier\n"
    "s_mov_b64 exec, s[66:67]\n"
    "buffer_load_dwordx4 v92, s[36:39], s62 offen offset:0 sc1 lds\n"
    "buffer_load_dwordx4 v92, s[36:39], s62 offen offset:896 sc1 lds\n"
    "buffer_load_dwordx4 v92, s[36:39], s62 offen offset:1792 sc1 lds\n"
    "buffer_load_dwordx4 v92, s[36:39], s62 offen offset:2688 sc1 lds\n"
    "s_mov_b64 exec, -1\n"
    "s_add_u32 s50, s61, 7\n"
    "s_sub_u32 s51, s50, 8\n"
    "s_cselect_b32 s50, s50, s51\n"
    "s_add_u32 s50, s50, 24\n"
    "s_add_u32 m0, s52, 0x8000\n"
    "s_mul_i32 s62, s50, 128\n"
    "buffer_load_dwordx4 v82, s[20:23], s62 offen offset:0 lds\n"
    "buffer_load_dwordx4 v83, s[20:23], s62 offen offset:1024 lds\n"
    "s_add_u32 s50, s61, 7\n"
    "s_sub_u32 s51, s50, 8\n"
    "s_cselect_b32 s50, s50, s51\n"
    "s_add_u32 s50, s50, 24\n"
    "s_add_u32 m0, s54, 0x21000\n"
    "s_mul_i32 s62, s50, 14336\n"
    "s_add_u32 s62, s62, s60\n"
    "s_waitcnt vmcnt(14)\n"
    "s_barrier\n"
    "s_mov_b64 exec, s[66:67]\n"
    "buffer_load_dwordx4 v92, s[36:39], s62 offen offset:0 sc1 lds\n"
    "buffer_load_dwordx4 v92, s[36:39], s62 offen offset:896 sc1 lds\n"
    "buffer_load_dwordx4 v92, s[36:39], s62 offen offset:1792 sc1 lds\n"
    "buffer_load_dwordx4 v92, s[36:39], s62 offen offset:2688 sc1 lds\n"
    "s_mov_b64 exec, -1\n"
    "s_add_u32 m0, s52, 0x0\n"
    "s_add_u32 s62, s99, 0\n"
    "s_and_b32 s62, s62, 7\n"
    "s_mul_i32 s62, s62, 65536\n"
    "s_add_u32 s62, s62, s57\n"
    "buffer_load_dwordx4 v90, s[12:15], s62 offen offset:0 lds\n"
    "buffer_load_dwordx4 v91, s[12:15], s62 offen offset:1024 lds\n"
    "s_add_u32 m0, s52, 0x2000\n"
    "s_add_u32 s62, s99, 0\n"
    "s_and_b32 s62, s62, 7\n"
    "s_mul_i32 s62, s62, 8192\n"
    "s_add_u32 s62, s62, s58\n"
    "s_waitcnt vmcnt(14)\n"
    "s_barrier\n"
    "buffer_load_dwordx4 v93, s[36:39], s62 offen offset:0 sc1 lds\n"
    "buffer_load_dwordx4 v93, s[36:39], s62 offen offset:1024 sc1 lds\n"
    "s_add_u32 m0, s52, 0xa000\n"
    "s_add_u32 s62, s99, 1\n"
    "s_and_b32 s62, s62, 7\n"
    "s_mul_i32 s62, s62, 65536\n"
    "s_add_u32 s62, s62, s57\n"
    "buffer_load_dwordx4 v90, s[12:15], s62 offen offset:0 lds\n"
    "buffer_load_dwordx4 v91, s[12:15], s62 offen offset:1024 lds\n"
    "s_add_u32 m0, s52, 0xc000\n"
    "s_add_u32 s62, s99, 1\n"
    "s_and_b32 s62, s62, 7\n"
    "s_mul_i32 s62, s62, 8192\n"
    "s_add_u32 s62, s62, s58\n"
    "buffer_load_dwordx4 v93, s[36:39], s62 offen offset:0 sc1 lds\n"
    "buffer_load_dwordx4 v93, s[36:39], s62 offen offset:1024 sc1 lds\n"
    "s_add_u32 m0, s52, 0x4000\n"
    "s_add_u32 s62, s99, 2\n"
    "s_and_b32 s62, s62, 7\n"
    "s_mul_i32 s62, s62, 65536\n"
    "s_add_u32 s62, s62, s57\n"
    "s_waitcnt vmcnt(14)\n"
    "s_barrier\n"
    "buffer_load_dwordx4 v90, s[12:15], s62 offen offset:0 lds\n"
    "buffer_load_dwordx4 v91, s[12:15], s62 offen offset:1024 lds\n"
    "s_add_u32 m0, s52, 0xe000\n"
    "s_add_u32 s62, s99, 2\n"
    "s_and_b32 s62, s62, 7\n"
    "s_mul_i32 s62, s62, 8192\n"
    "s_add_u32 s62, s62, s58\n"
    "buffer_load_dwordx4 v93, s[36:39], s62 offen offset:0 sc1 lds\n"
    "buffer_load_dwordx4 v93, s[36:39], s62 offen offset:1024 sc1 lds\n"
    "s_add_u32 m0, s52, 0x10000\n"
    "s_add_u32 s62, s99, 3\n"
    "s_and_b32 s62, s62, 7\n"
    "s_mul_i32 s62, s62, 65536\n"
    "s_add_u32 s62, s62, s57\n"
    "buffer_load_dwordx4 v90, s[12:15], s62 offen offset:0 lds\n"
    "buffer_load_dwordx4 v91, s[12:15], s62 offen offset:1024 lds\n"
    "s_add_u32 m0, s52, 0x6000\n"
    "s_add_u32 s62, s99, 3\n"
    "s_and_b32 s62, s62, 7\n"
    "s_mul_i32 s62, s62, 8192\n"
    "s_add_u32 s62, s62, s58\n"
    "s_waitcnt vmcnt(14)\n"
    "s_barrier\n"
    "buffer_load_dwordx4 v93, s[36:39], s62 offen offset:0 sc1 lds\n"
    "buffer_load_dwordx4 v93, s[36:39], s62 offen offset:1024 sc1 lds\n"
    "s_add_u32 m0, s52, 0x12000\n"
    "s_add_u32 s62, s99, 4\n"
    "s_and_b32 s62, s62, 7\n"
    "s_mul_i32 s62, s62, 65536\n"
    "s_add_u32 s62, s62, s57\n"
    "buffer_load_dwordx4 v90, s[12:15], s62 offen offset:0 lds\n"
    "buffer_load_dwordx4 v91, s[12:15], s62 offen offset:1024 lds\n"
    "s_add_u32 m0, s52, 0x14000\n"
    "s_add_u32 s62, s99, 4\n"
    "s_and_b32 s62, s62, 7\n"
    "s_mul_i32 s62, s62, 8192\n"
    "s_add_u32 s62, s62, s58\n"
    "buffer_load_dwordx4 v93, s[36:39], s62 offen offset:0 sc1 lds\n"
    "buffer_load_dwordx4 v93, s[36:39], s62 offen offset:1024 sc1 lds\n"
    "s_waitcnt vmcnt(16)\n"
    "s_barrier\n"
    "s_add_u32 m0, s52, 0x0\n"
    "s_add_u32 s62, s99, 5\n"
    "s_and_b32 s62, s62, 7\n"
    "s_mul_i32 s62, s62, 65536\n"
    "s_add_u32 s62, s62, s57\n"
    "s_waitcnt vmcnt(8)\n"
    "s_barrier\n"
    "buffer_load_dwordx4 v90, s[12:15], s62 offen offset:0 lds\n"
    "buffer_load_dwordx4 v91, s[12:15], s62 offen offset:1024 lds\n"
    "s_add_u32 m0, s52, 0x2000\n"
    "s_add_u32 s62, s99, 5\n"
    "s_and_b32 s62, s62, 7\n"
    "s_mul_i32 s62, s62, 8192\n"
    "s_add_u32 s62, s62, s58\n"
    "buffer_load_dwordx4 v93, s[36:39], s62 offen offset:0 sc1 lds\n"
    "buffer_load_dwordx4 v93, s[36:39], s62 offen offset:1024 sc1 lds\n"
    "s_add_u32 m0, s52, 0x8000\n"
    "s_add_u32 s62, s99, 6\n"
    "s_and_b32 s62, s62, 7\n"
    "s_mul_i32 s62, s62, 65536\n"
    "s_add_u32 s62, s62, s57\n"
    "buffer_load_dwordx4 v90, s[12:15], s62 offen offset:0 lds\n"
    "buffer_load_dwordx4 v91, s[12:15], s62 offen offset:1024 lds\n"
    "s_add_u32 m0, s52, 0x16000\n"
    "s_add_u32 s62, s99, 6\n"
    "s_and_b32 s62, s62, 7\n"
    "s_mul_i32 s62, s62, 8192\n"
    "s_add_u32 s62, s62, s58\n"
    "buffer_load_dwordx4 v93, s[36:39], s62 offen offset:0 sc1 lds\n"
    "buffer_load_dwordx4 v93, s[36:39], s62 offen offset:1024 sc1 lds\n"
    "s_add_u32 m0, s52, 0x18000\n"
    "s_add_u32 s62, s99, 7\n"
    "s_and_b32 s62, s62, 7\n"
    "s_mul_i32 s62, s62, 65536\n"
    "s_add_u32 s62, s62, s57\n"
    "buffer_load_dwordx4 v90, s[12:15], s62 offen offset:0 lds\n"
    "buffer_load_dwordx4 v91, s[12:15], s62 offen offset:1024 lds\n"
    "s_add_u32 m0, s52, 0x1a000\n"
    "s_add_u32 s62, s99, 7\n"
    "s_and_b32 s62, s62, 7\n"
    "s_mul_i32 s62, s62, 8192\n"
    "s_add_u32 s62, s62, s58\n"
    "buffer_load_dwordx4 v93, s[36:39], s62 offen offset:0 sc1 lds\n"
    "buffer_load_dwordx4 v93, s[36:39], s62 offen offset:1024 sc1 lds\n"
    "s_add_u32 m0, s52, 0x4000\n"
    "s_add_u32 s62, s97, 0\n"
    "s_sub_u32 s50, s62, 14\n"
    "s_cselect_b32 s62, s62, s50\n"
    "s_mul_i32 s62, s62, 65536\n"
    "s_add_u32 s62, s62, s57\n"
    "s_waitcnt vmcnt(12)\n"
    "s_barrier\n"
    "buffer_load_dwordx4 v90, s[28:31], s62 offen offset:0 lds\n"
    "buffer_load_dwordx4 v91, s[28:31], s62 offen offset:1024 lds\n"
    "s_add_u32 m0, s52, 0xa000\n"
    "s_add_u32 s62, s97, 1\n"
    "s_sub_u32 s50, s62, 14\n"
    "s_cselect_b32 s62, s62, s50\n"
    "s_mul_i32 s62, s62, 65536\n"
    "s_add_u32 s62, s62, s57\n"
    "buffer_load_dwordx4 v90, s[28:31], s62 offen offset:0 lds\n"
    "buffer_load_dwordx4 v91, s[28:31], s62 offen offset:1024 lds\n"
    "s_add_u32 m0, s52, 0xc000\n"
    "s_add_u32 s62, s97, 2\n"
    "s_sub_u32 s50, s62, 14\n"
    "s_cselect_b32 s62, s62, s50\n"
    "s_mul_i32 s62, s62, 65536\n"
    "s_add_u32 s62, s62, s57\n"
    "buffer_load_dwordx4 v90, s[28:31], s62 offen offset:0 lds\n"
    "buffer_load_dwordx4 v91, s[28:31], s62 offen offset:1024 lds\n"
    "s_add_u32 m0, s52, 0xe000\n"
    "s_add_u32 s62, s97, 3\n"
    "s_sub_u32 s50, s62, 14\n"
    "s_cselect_b32 s62, s62, s50\n"
    "s_mul_i32 s62, s62, 65536\n"
    "s_add_u32 s62, s62, s57\n"
    "buffer_load_dwordx4 v90, s[28:31], s62 offen offset:0 lds\n"
    "buffer_load_dwordx4 v91, s[28:31], s62 offen offset:1024 lds\n"
    "s_add_u32 m0, s52, 0x1c000\n"
    "s_add_u32 s62, s97, 4\n"
    "s_sub_u32 s50, s62, 14\n"
    "s_cselect_b32 s62, s62, s50\n"
    "s_mul_i32 s62, s62, 65536\n"
    "s_add_u32 s62, s62, s57\n"
    "buffer_load_dwordx4 v90, s[28:31], s62 offen offset:0 lds\n"
    "buffer_load_dwordx4 v91, s[28:31], s62 offen offset:1024 lds\n"
    "s_add_u32 m0, s52, 0x1e000\n"
    "s_add_u32 s62, s97, 5\n"
    "s_sub_u32 s50, s62, 14\n"
    "s_cselect_b32 s62, s62, s50\n"
    "s_mul_i32 s62, s62, 65536\n"
    "s_add_u32 s62, s62, s57\n"
    "buffer_load_dwordx4 v90, s[28:31], s62 offen offset:0 lds\n"
    "buffer_load_dwordx4 v91, s[28:31], s62 offen offset:1024 lds\n"
    "s_add_u32 m0, s52, 0x6000\n"
    "s_add_u32 s62, s97, 6\n"
    "s_sub_u32 s50, s62, 14\n"
    "s_cselect_b32 s62, s62, s50\n"
    "s_mul_i32 s62, s62, 65536\n"
    "s_add_u32 s62, s62, s57\n"
    "s_waitcnt vmcnt(16)\n"
    "s_barrier\n"
    "buffer_load_dwordx4 v90, s[28:31], s62 offen offset:0 lds\n"
    "buffer_load_dwordx4 v91, s[28:31], s62 offen offset:1024 lds\n"
    "s_add_u32 m0, s52, 0x10000\n"
    "s_add_u32 s62, s97, 7\n"
    "s_sub_u32 s50, s62, 14\n"
    "s_cselect_b32 s62, s62, s50\n"
    "s_mul_i32 s62, s62, 65536\n"
    "s_add_u32 s62, s62, s57\n"
    "buffer_load_dwordx4 v90, s[28:31], s62 offen offset:0 lds\n"
    "buffer_load_dwordx4 v91, s[28:31], s62 offen offset:1024 lds\n"
    "s_add_u32 m0, s52, 0x12000\n"
    "s_add_u32 s62, s97, 8\n"
    "s_sub_u32 s50, s62, 14\n"
    "s_cselect_b32 s62, s62, s50\n"
    "s_mul_i32 s62, s62, 65536\n"
    "s_add_u32 s62, s62, s57\n"
    "buffer_load_dwordx4 v90, s[28:31], s62 offen offset:0 lds\n"
    "buffer_load_dwordx4 v91, s[28:31], s62 offen offset:1024 lds\n"
    "s_lshl_b32 s50, s46, 8\n"
    "s_add_u32 s50, s50, 0x6000\n"
    "buffer_load_dwordx2 v[100:101], v117, s[36:39], s50 offen sc1\n"
    "s_waitcnt vmcnt(0)\n"
    "v_cmp_eq_u32 vcc, s48, v100\n"
    "v_cmp_eq_u32_e64 s[64:65], s49, v101\n"
    "s_nop 0\n"
    "s_and_b64 vcc, vcc, s[64:65]\n"
    "s_cmp_eq_u64 vcc, -1\n"
    "s_cbranch_scc1 L_pok3_%=\n"
    "s_mov_b32 s63, 0x20000\n"
    "L_pl3_%=:\n"
    "s_lshl_b32 s50, s46, 8\n"
    "s_add_u32 s50, s50, 0x6000\n"
    "buffer_load_dwordx2 v[100:101], v117, s[36:39], s50 offen sc1\n"
    "s_waitcnt vmcnt(0)\n"
    "v_cmp_eq_u32 vcc, s48, v100\n"
    "v_cmp_eq_u32_e64 s[64:65], s49, v101\n"
    "s_nop 0\n"
    "s_and_b64 vcc, vcc, s[64:65]\n"
    "s_cmp_eq_u64 vcc, -1\n"
    "s_cbranch_scc1 L_pok3_%=\n"
    "s_sleep 1\n"
    "s_sub_u32 s63, s63, 1\n"
    "s_cmp_lg_u32 s63, 0\n"
    "s_cbranch_scc1 L_pl3_%=\n"
    "L_pok3_%=:\n"
    "s_add_u32 m0, s52, 0x14000\n"
    "s_add_u32 s62, s97, 0\n"
    "s_sub_u32 s50, s62, 14\n"
    "s_cselect_b32 s62, s62, s50\n"
    "s_mul_i32 s62, s62, 8192\n"
    "s_add_u32 s62, s62, s59\n"
    "buffer_load_dwordx4 v93, s[36:39], s62 offen offset:0 sc1 lds\n"
    "buffer_load_dwordx4 v93, s[36:39], s62 offen offset:1024 sc1 lds\n"
    "s_add_u32 m0, s52, 0x20000\n"
    "s_add_u32 s62, s97, 1\n"
    "s_sub_u32 s50, s62, 14\n"
    "s_cselect_b32 s62, s62, s50\n"
    "s_mul_i32 s62, s62, 8192\n"
    "s_add_u32 s62, s62, s59\n"
    "buffer_load_dwordx4 v93, s[36:39], s62 offen offset:0 sc1 lds\n"
    "buffer_load_dwordx4 v93, s[36:39], s62 offen offset:1024 sc1 lds\n"
    "s_add_u32 m0, s52, 0x22000\n"
    "s_add_u32 s62, s97, 2\n"
    "s_sub_u32 s50, s62, 14\n"
    "s_cselect_b32 s62, s62, s50\n"
    "s_mul_i32 s62, s62, 8192\n"
    "s_add_u32 s62, s62, s59\n"
    "buffer_load_dwordx4 v93, s[36:39], s62 offen offset:0 sc1 lds\n"
    "buffer_load_dwordx4 v93, s[36:39], s62 offen offset:1024 sc1 lds\n"
    "s_add_u32 m0, s52, 0x24000\n"
    "s_add_u32 s62, s97, 3\n"
    "s_sub_u32 s50, s62, 14\n"
    "s_cselect_b32 s62, s62, s50\n"
    "s_mul_i32 s62, s62, 8192\n"
    "s_add_u32 s62, s62, s59\n"
    "buffer_load_dwordx4 v93, s[36:39], s62 offen offset:0 sc1 lds\n"
    "buffer_load_dwordx4 v93, s[36:39], s62 offen offset:1024 sc1 lds\n"
    "s_add_u32 m0, s52, 0x26000\n"
    "s_add_u32 s62, s97, 4\n"
    "s_sub_u32 s50, s62, 14\n"
    "s_cselect_b32 s62, s62, s50\n"
    "s_mul_i32 s62, s62, 8192\n"
    "s_add_u32 s62, s62, s59\n"
    "buffer_load_dwordx4 v93, s[36:39], s62 offen offset:0 sc1 lds\n"
    "buffer_load_dwordx4 v93, s[36:39], s62 offen offset:1024 sc1 lds\n"
    "s_add_u32 m0, s52, 0x0\n"
    "s_add_u32 s62, s97, 5\n"
    "s_sub_u32 s50, s62, 14\n"
    "s_cselect_b32 s62, s62, s50\n"
    "s_mul_i32 s62, s62, 8192\n"
    "s_add_u32 s62, s62, s59\n"
    "s_barrier\n"
    "buffer_load_dwordx4 v93, s[36:39], s62 offen offset:0 sc1 lds\n"
    "buffer_load_dwordx4 v93, s[36:39], s62 offen offset:1024 sc1 lds\n"
    "s_add_u32 m0, s52, 0x2000\n"
    "s_add_u32 s62, s97, 6\n"
    "s_sub_u32 s50, s62, 14\n"
    "s_cselect_b32 s62, s62, s50\n"
    "s_mul_i32 s62, s62, 8192\n"
    "s_add_u32 s62, s62, s59\n"
    "buffer_load_dwordx4 v93, s[36:39], s62 offen offset:0 sc1 lds\n"
    "buffer_load_dwordx4 v93, s[36:39], s62 offen offset:1024 sc1 lds\n"
    "s_add_u32 m0, s52, 0x8000\n"
    "s_add_u32 s62, s97, 7\n"
    "s_sub_u32 s50, s62, 14\n"
    "s_cselect_b32 s62, s62, s50\n"
    "s_mul_i32 s62, s62, 8192\n"
    "s_add_u32 s62, s62, s59\n"
    "buffer_load_dwordx4 v93, s[36:39], s62 offen offset:0 sc1 lds\n"
    "buffer_load_dwordx4 v93, s[36:39], s62 offen offset:1024 sc1 lds\n"
    "s_add_u32 m0, s52, 0x16000\n"
    "s_add_u32 s62, s97, 8\n"
    "s_sub_u32 s50, s62, 14\n"
    "s_cselect_b32 s62, s62, s50\n"
    "s_mul_i32 s62, s62, 8192\n"
    "s_add_u32 s62, s62, s59\n"
    "s_waitcnt vmcnt(12)\n"
    "s_barrier\n"
    "buffer_load_dwordx4 v93, s[36:39], s62 offen offset:0 sc1 lds\n"
    "buffer_load_dwordx4 v93, s[36:39], s62 offen offset:1024 sc1 lds\n"
    "s_add_u32 m0, s52, 0x18000\n"
    "s_add_u32 s62, s97, 9\n"
    "s_sub_u32 s50, s62, 14\n"
    "s_cselect_b32 s62, s62, s50\n"
    "s_mul_i32 s62, s62, 65536\n"
    "s_add_u32 s62, s62, s57\n"
    "buffer_load_dwordx4 v90, s[28:31], s62 offen offset:0 lds\n"
    "buffer_load_dwordx4 v91, s[28:31], s62 offen offset:1024 lds\n"
    "s_add_u32 m0, s52, 0x1a000\n"
    "s_add_u32 s62, s97, 9\n"
    "s_sub_u32 s50, s62, 14\n"
    "s_cselect_b32 s62, s62, s50\n"
    "s_mul_i32 s62, s62, 8192\n"
    "s_add_u32 s62, s62, s59\n"
    "buffer_load_dwordx4 v93, s[36:39], s62 offen offset:0 sc1 lds\n"
    "buffer_load_dwordx4 v93, s[36:39], s62 offen offset:1024 sc1 lds\n"
    "s_add_u32 m0, s52, 0x4000\n"
    "s_add_u32 s62, s97, 10\n"
    "s_sub_u32 s50, s62, 14\n"
    "s_cselect_b32 s62, s62, s50\n"
    "s_mul_i32 s62, s62, 65536\n"
    "s_add_u32 s62, s62, s57\n"
    "s_waitcnt vmcnt(14)\n"
    "s_barrier\n"
    "buffer_load_dwordx4 v90, s[28:31], s62 offen offset:0 lds\n"
    "buffer_load_dwordx4 v91, s[28:31], s62 offen offset:1024 lds\n"
    "s_add_u32 m0, s52, 0xa000\n"
    "s_add_u32 s62, s97, 10\n"
    "s_sub_u32 s50, s62, 14\n"
    "s_cselect_b32 s62, s62, s50\n"
    "s_mul_i32 s62, s62, 8192\n"
    "s_add_u32 s62, s62, s59\n"
    "buffer_load_dwordx4 v93, s[36:39], s62 offen offset:0 sc1 lds\n"
    "buffer_load_dwordx4 v93, s[36:39], s62 offen offset:1024 sc1 lds\n"
    "s_add_u32 m0, s52, 0x14000\n"
    "s_add_u32 s62, s97, 11\n"
    "s_sub_u32 s50, s62, 14\n"
    "s_cselect_b32 s62, s62, s50\n"
    "s_mul_i32 s62, s62, 65536\n"
    "s_add_u32 s62, s62, s57\n"
    "buffer_load_dwordx4 v90, s[28:31], s62 offen offset:0 lds\n"
    "buffer_load_dwordx4 v91, s[28:31], s62 offen offset:1024 lds\n"
    "s_add_u32 m0, s52, 0x20000\n"
    "s_add_u32 s62, s97, 11\n"
    "s_sub_u32 s50, s62, 14\n"
    "s_cselect_b32 s62, s62, s50\n"
    "s_mul_i32 s62, s62, 8192\n"
    "s_add_u32 s62, s62, s59\n"
    "buffer_load_dwordx4 v93, s[36:39], s62 offen offset:0 sc1 lds\n"
    "buffer_load_dwordx4 v93, s[36:39], s62 offen offset:1024 sc1 lds\n"
    "s_add_u32 m0, s52, 0xc000\n"
    "s_add_u32 s62, s97, 12\n"
    "s_sub_u32 s50, s62, 14\n"
    "s_cselect_b32 s62, s62, s50\n"
    "s_mul_i32 s62, s62, 65536\n"
    "s_add_u32 s62, s62, s57\n"
    "s_waitcnt vmcnt(18)\n"
    "s_barrier\n"
    "buffer_load_dwordx4 v90, s[28:31], s62 offen offset:0 lds\n"
    "buffer_load_dwordx4 v91, s[28:31], s62 offen offset:1024 lds\n"
    "s_add_u32 m0, s52, 0xe000\n"
    "s_add_u32 s62, s97, 12\n"
    "s_sub_u32 s50, s62, 14\n"
    "s_cselect_b32 s62, s62, s50\n"
    "s_mul_i32 s62, s62, 8192\n"
    "s_add_u32 s62, s62, s59\n"
    "buffer_load_dwordx4 v93, s[36:39], s62 offen offset:0 sc1 lds\n"
    "buffer_load_dwordx4 v93, s[36:39], s62 offen offset:1024 sc1 lds\n"
    "s_add_u32 m0, s52, 0x22000\n"
    "s_add_u32 s62, s97, 13\n"
    "s_sub_u32 s50, s62, 14\n"
    "s_cselect_b32 s62, s62, s50\n"
    "s_mul_i32 s62, s62, 65536\n"
    "s_add_u32 s62, s62, s57\n"
    "buffer_load_dwordx4 v90, s[28:31], s62 offen offset:0 lds\n"
    "buffer_load_dwordx4 v91, s[28:31], s62 offen offset:1024 lds\n"
    "s_add_u32 m0, s52, 0x24000\n"
    "s_add_u32 s62, s97, 13\n"
    "s_sub_u32 s50, s62, 14\n"
    "s_cselect_b32 s62, s62, s50\n"
    "s_mul_i32 s62, s62, 8192\n"
    "s_add_u32 s62, s62, s59\n"
    "buffer_load_dwordx4 v93, s[36:39], s62 offen offset:0 sc1 lds\n"
    "buffer_load_dwordx4 v93, s[36:39], s62 offen offset:1024 sc1 lds\n"
    "s_waitcnt vmcnt(22)\n"
    "s_barrier\n"
    "s_waitcnt vmcnt(16)\n"
    "s_barrier\n"
    "s_waitcnt vmcnt(8)\n"
    "s_barrier\n"
    "s_waitcnt vmcnt(0)\n"
    "s_barrier\n"
    "s_barrier\n"
    "s_barrier\n"
    "ds_read_b128 v[124:127], v123 offset:0\n"
    "ds_read_b128 v[128:131], v152 offset:0\n"
    "ds_read_b128 v[132:135], v123 offset:16384\n"
    "ds_read_b128 v[136:139], v152 offset:16384\n"
    "v_lshlrev_b32 v144, 16, v118\n"
    "v_and_b32 v145, 0xffff0000, v118\n"
    "v_lshlrev_b32 v146, 16, v119\n"
    "v_and_b32 v147, 0xffff0000, v119\n"
    "v_lshlrev_b32 v148, 16, v120\n"
    "v_and_b32 v149, 0xffff0000, v120\n"
    "v_lshlrev_b32 v150, 16, v121\n"
    "v_and_b32 v151, 0xffff0000, v121\n"
    "s_waitcnt lgkmcnt(0)\n"
    "v_pk_add_f32 v[124:125], v[124:125], v[132:133]\n"
    "v_pk_add_f32 v[126:127], v[126:127], v[134:135]\n"
    "v_pk_add_f32 v[128:129], v[128:129], v[136:137]\n"
    "v_pk_add_f32 v[130:131], v[130:131], v[138:139]\n"
    "v_pk_add_f32 v[124:125], v[124:125], v[144:145]\n"
    "v_pk_add_f32 v[126:127], v[126:127], v[146:147]\n"
    "v_pk_add_f32 v[128:129], v[128:129], v[148:149]\n"
    "v_pk_add_f32 v[130:131], v[130:131], v[150:151]\n"
    "v_cvt_pk_bf16_f32 v140, v124, v125\n"
    "v_cvt_pk_bf16_f32 v141, v126, v127\n"
    "v_cvt_pk_bf16_f32 v142, v128, v129\n"
    "v_cvt_pk_bf16_f32 v143, v130, v131\n"
    "buffer_store_dwordx4 v[140:143], v122, s[32:35], 0 offen\n"
    "s_endpgm\n"
    "L_end_%=:\n"
    :
    : "{s[0:1]}"(__builtin_amdgcn_kernarg_segment_ptr()), "{s2}"(blockIdx.x), "{v0}"(threadIdx.x),
      "{s3}"((unsigned)(size_t)(__attribute__((address_space(3))) char*)smem)
    : "v1","v2","v3","v4","v5","v6","v7","v8","v9","v10","v11","v12","v13","v14","v15","v16","v17","v18","v19","v20","v21","v22","v23","v24","v25","v26","v27","v28","v29","v30","v31","v32","v33","v34","v35","v36","v37","v38","v39","v40","v41","v42","v43","v44","v45","v46","v47","v48","v49","v50","v51","v52","v53","v54","v55","v56","v57","v58","v59","v60","v61","v62","v63","v64","v65","v66","v67","v68","v69","v70","v71","v72","v73","v74","v75","v76","v77","v78","v79","v80","v81","v82","v83","v84","v85","v86","v87","v88","v89","v90","v91","v92","v93","v94","v95","v96","v97","v98","v99","v100","v101","v102","v103","v104","v105","v106","v107","v108","v109","v110","v111","v112","v113","v114","v115","v116","v117","v118","v119","v120","v121","v122","v123","v124","v125","v126","v127","v128","v129","v130","v131","v132","v133","v134","v135","v136","v137","v138","v139","v140","v141","v142","v143","v144","v145","v146","v147","v148","v149","v150","v151","v152","v153","v154","v155","v156","v157","v158","v159","v160","v161","v162","v163","v164","v165","v166","v167","v168","v169","v170","v171","v172","v173","v174","v175","v176","v177","v178","v179","v180","v181","v182","v183","v184","v185","v186","v187","v188","v189","v190","v191","v192","v193","v194","v195","v196","v197","v198","v199","v200","v201","v202","v203","v204","v205","v206","v207","v208","v209","v210","v211","v212","v213","v214","v215","v216","v217","v218","v219","v220","v221","v222","v223","v224","v225","v226","v227","v228","v229","v230","v231","v232","v233","v234","v235","v236","v237","v238","v239","v240","v241","v242","v243","v244","v245","v246","v247","v248","v249","v250","v251","v252","v253","v254","v255","s4","s5","s6","s7","s8","s9","s10","s11","s12","s13","s14","s15","s16","s17","s18","s19","s20","s21","s22","s23","s24","s25","s26","s27","s28","s29","s30","s31","s33","s34","s35","s36","s37","s38","s39","s40","s41","s42","s43","s44","s45","s46","s47","s48","s49","s50","s51","s52","s53","s54","s55","s56","s57","s58","s59","s60","s61","s62","s63","s64","s65","s66","s67","s68","s69","s70","s71","s72","s73","s74","s75","s76","s77","s78","s79","s80","s81","s82","s83","s84","s85","s86","s87","s88","s89","s90","s91","s92","s93","s94","s95","s96","s97","s98","s99","vcc","scc","memory");
}

extern "C" void kernel_launch(void* const* d_in, const int* in_sizes, int n_in, void* d_out, int out_size, void* d_ws, size_t ws_size,
                              hipStream_t stream) {
  fused_block<<<256, 512, 0, stream>>>(d_in[0], d_in[1], d_in[2], d_in[4], d_in[5], d_in[6], d_in[7], d_out, d_ws);
}
```
